```python
import torch

def _softmax(x, axis=-1):
    return torch.softmax(x, dim=axis)
def _gelu(x):
    return torch.nn.functional.gelu(x, approximate="tanh")
def _silu(x):
    return torch.nn.functional.silu(x)
def _sigmoid(x):
    return torch.sigmoid(x)
def _layernorm(x, axis=-1, eps=1e-5):
    m = x.mean(dim=axis, keepdim=True)
    v = x.var(dim=axis, keepdim=True, unbiased=False)
    return (x - m) / torch.sqrt(v + eps)
def _scatter_add(tgt, idx, src, axis=0):
    return tgt.clone().index_add_(axis, idx, src)
def _segment_sum(src, seg, n):
    out = torch.zeros((n,) + src.shape[1:], dtype=src.dtype)
    return out.index_add_(0, seg, src)
def _pad(x, pw):
    flat = tuple(v for pair in reversed(pw) for v in pair)
    return torch.nn.functional.pad(x, flat)

kernel_name = 'parallel_sum__linear_kernel_hedgehog__geglu_ffn__prompt'

def setup_inputs():

    _w = (lambda a: (lambda t: t.to(torch.bfloat16) if t.is_floating_point() else t)(torch.as_tensor(a)))
    def _A_setup():
        B, S, D, H = 1, 2048, 1024, 32
        Dh = D // H
        return {
            "x": _w((lambda *s: torch.randn(*s, dtype=torch.bfloat16) * 0.05)(B, S, D)),
            "wqkv": _w((lambda *s: torch.randn(*s, dtype=torch.bfloat16) * 0.8)(D, 3 * D)),
            "w_phi": _w((lambda *s: torch.randn(*s, dtype=torch.bfloat16) * 0.8)(H, Dh, Dh) * (Dh ** -0.5)),
            "wo": _w((lambda *s: torch.randn(*s, dtype=torch.bfloat16) * 0.2)(D, D)),
        }
    _A_in = _A_setup()

    def _B_setup():
        B, S, D, F = 1, 2048, 1024, 3584
        return {
            "x": _w((lambda *s: torch.randn(*s, dtype=torch.bfloat16) * 0.05)(B, S, D)),
            "w_gate": _w((lambda *s: torch.randn(*s, dtype=torch.bfloat16) * 0.05)(D, F)),
            "w_up": _w((lambda *s: torch.randn(*s, dtype=torch.bfloat16) * 0.05)(D, F)),
            "w_down": _w((lambda *s: torch.randn(*s, dtype=torch.bfloat16) * 0.05)(F, D)),
        }
    _B_in = _B_setup()

    return {
        "A_x": _A_in["x"],
        "A_wqkv": _A_in["wqkv"],
        "A_w_phi": _A_in["w_phi"],
        "A_wo": _A_in["wo"],
        "B_x": _B_in["x"],
        "B_w_gate": _B_in["w_gate"],
        "B_w_up": _B_in["w_up"],
        "B_w_down": _B_in["w_down"],
    }

def reference(A_x, A_wqkv, A_w_phi, A_wo, B_x, B_w_gate, B_w_up, B_w_down):
    def _A_ref(x, wqkv, w_phi, wo):
        B, S, D = x.shape
        H = 32; Dh = D // H
        qkv = (lambda x, *d: x.permute(*d))((x @ wqkv).reshape(B, S, 3, H, Dh), 2, 0, 3, 1, 4)
        q, k, v = qkv[0], qkv[1], qkv[2]
        pq = torch.einsum("bhsd,hdf->bhsf", q, w_phi)
        pk = torch.einsum("bhsd,hdf->bhsf", k, w_phi)
        phi_q = torch.concatenate([_softmax(pq, axis=-1), _softmax(-pq, axis=-1)], axis=-1)
        phi_k = torch.concatenate([_softmax(pk, axis=-1), _softmax(-pk, axis=-1)], axis=-1)
        kv = torch.einsum("bhsf,bhsd->bhfd", phi_k, v)
        z  = torch.einsum("bhsf->bhf", phi_k)
        num = torch.einsum("bhqf,bhfd->bhqd", phi_q, kv)
        den = torch.einsum("bhqf,bhf->bhq", phi_q, z)[..., None] + 1e-6
        o = num / den
        return (lambda x, *d: x.permute(*d))(o, 0, 2, 1, 3).reshape(B, S, D) @ wo

    def _B_ref(x, w_gate, w_up, w_down):
        return (_gelu(x @ w_gate) * (x @ w_up)) @ w_down

    x = A_x
    xn = _layernorm(x, axis=-1)
    return x + _A_ref(xn, A_wqkv, A_w_phi, A_wo) + _B_ref(xn, B_w_gate, B_w_up, B_w_down)
```

```cpp
#include <hip/hip_runtime.h>
#include <hip/hip_bf16.h>
typedef unsigned short u16;
__device__ inline float bf2f(u16 b){ return __uint_as_float(((unsigned)b)<<16); }
__device__ inline u16 f2bf(float f){ unsigned u=__float_as_uint(f); u += 0x7FFFu + ((u>>16)&1u); return (u16)(u>>16); }
__device__ inline float rbf(float f){ return bf2f(f2bf(f)); }

constexpr int S=2048, D=1024, H=32, DH=32, F=3584;

__global__ void ln_kernel(const u16* x, u16* xn){
  int row=blockIdx.x; __shared__ float red[256];
  float v[4]; float s=0;
  for(int i=0;i<4;i++){ v[i]=bf2f(x[row*D+threadIdx.x+i*256]); s+=v[i]; }
  red[threadIdx.x]=s; __syncthreads();
  for(int o=128;o>0;o>>=1){ if(threadIdx.x<o) red[threadIdx.x]+=red[threadIdx.x+o]; __syncthreads(); }
  float mean=red[0]/D; __syncthreads();
  float q=0; for(int i=0;i<4;i++){ float d=v[i]-mean; q+=d*d; }
  red[threadIdx.x]=q; __syncthreads();
  for(int o=128;o>0;o>>=1){ if(threadIdx.x<o) red[threadIdx.x]+=red[threadIdx.x+o]; __syncthreads(); }
  float var=red[0]/D;
  float m=rbf(mean); float sd=rbf(sqrtf(rbf(rbf(var)+1e-5f)));
  for(int i=0;i<4;i++){ xn[row*D+threadIdx.x+i*256]=f2bf(rbf(v[i]-m)/sd); }
}
__global__ void gemm_kernel(const u16* A,int lda,const u16* B,int ldb,u16* C,int ldc,int M,int N,int K){
  __shared__ float As[16][17], Bs[16][17];
  int tx=threadIdx.x&15, ty=threadIdx.x>>4;
  int m=blockIdx.y*16+ty, n=blockIdx.x*16+tx; float acc=0;
  for(int k0=0;k0<K;k0+=16){
    As[ty][tx]=bf2f(A[(size_t)m*lda+k0+tx]); Bs[ty][tx]=bf2f(B[(size_t)(k0+ty)*ldb+n]); __syncthreads();
    for(int k=0;k<16;k++) acc+=As[ty][k]*Bs[k][tx];
    __syncthreads();
  }
  C[(size_t)m*ldc+n]=f2bf(acc);
}
__global__ void phi_kernel(const u16* qkv,int off,const u16* wphi,u16* phi){
  int s=blockIdx.x; int h=blockIdx.y*2+(threadIdx.x>>5); int f=threadIdx.x&31;
  float acc=0; for(int d=0;d<32;d++) acc+=bf2f(qkv[(size_t)s*3072+off+h*32+d])*bf2f(wphi[h*1024+d*32+f]);
  float p=rbf(acc);
  float mx=p, mn=p; for(int o=16;o>0;o>>=1){ mx=fmaxf(mx,__shfl_xor(mx,o)); mn=fminf(mn,__shfl_xor(mn,o)); }
  float e1=expf(p-mx), e2=expf(mn-p); float s1=e1,s2=e2;
  for(int o=16;o>0;o>>=1){ s1+=__shfl_xor(s1,o); s2+=__shfl_xor(s2,o); }
  phi[((size_t)s*32+h)*64+f]=f2bf(e1/s1); phi[((size_t)s*32+h)*64+32+f]=f2bf(e2/s2);
}
__global__ void kv_kernel(const u16* phik,const u16* qkv,u16* kv,u16* z){
  int h=blockIdx.x; int f=blockIdx.y; int d=threadIdx.x;
  float acc=0, zz=0;
  for(int s=0;s<S;s++){ float p=bf2f(phik[((size_t)s*32+h)*64+f]); acc+=p*bf2f(qkv[(size_t)s*3072+2048+h*32+d]); zz+=p; }
  kv[(h*64+f)*32+d]=f2bf(acc); if(d==0) z[h*64+f]=f2bf(zz);
}
__global__ void o_kernel(const u16* phiq,const u16* kv,const u16* z,u16* o){
  int s=blockIdx.x; int h=blockIdx.y*2+(threadIdx.x>>5); int d=threadIdx.x&31;
  float num=0, den=0;
  for(int f=0;f<64;f++){ float p=bf2f(phiq[((size_t)s*32+h)*64+f]); num+=p*bf2f(kv[(h*64+f)*32+d]); den+=p*bf2f(z[h*64+f]); }
  float dn=rbf(rbf(den)+1e-6f);
  o[(size_t)s*D+h*32+d]=f2bf(rbf(num)/dn);
}
__global__ void geglu_kernel(const u16* g,const u16* u,u16* h,int n){
  int i=blockIdx.x*256+threadIdx.x; if(i>=n) return;
  float x=bf2f(g[i]); float t=0.7978845608028654f*(x+0.044715f*x*x*x); float ge=0.5f*x*(1.f+tanhf(t));
  h[i]=f2bf(rbf(ge)*bf2f(u[i]));
}
__global__ void final_kernel(const u16* x,const u16* a,const u16* b,u16* out,int n){
  int i=blockIdx.x*256+threadIdx.x; if(i>=n) return;
  out[i]=f2bf(rbf(bf2f(x[i])+bf2f(a[i]))+bf2f(b[i]));
}
extern "C" void kernel_launch(void* const* d_in, const int* in_sizes, int n_in, void* d_out, int out_size, void* d_ws, size_t ws_size, hipStream_t stream){
  const u16* x=(const u16*)d_in[0]; const u16* wqkv=(const u16*)d_in[1]; const u16* wphi=(const u16*)d_in[2]; const u16* wo=(const u16*)d_in[3];
  const u16* wg=(const u16*)d_in[5]; const u16* wu=(const u16*)d_in[6]; const u16* wd=(const u16*)d_in[7];
  u16* ws=(u16*)d_ws; size_t o=0;
  u16* xn=ws+o; o+=S*D; u16* qkv=ws+o; o+=(size_t)S*3072; u16* phiq=ws+o; o+=(size_t)S*2048; u16* phik=ws+o; o+=(size_t)S*2048;
  u16* kv=ws+o; o+=32*64*32; u16* z=ws+o; o+=32*64; u16* oo=ws+o; o+=S*D; u16* g=ws+o; o+=(size_t)S*F; u16* u=ws+o; o+=(size_t)S*F; u16* h=ws+o; o+=(size_t)S*F;
  u16* a=ws+o; o+=S*D; u16* b=ws+o; o+=S*D;
  ln_kernel<<<S,256,0,stream>>>(x,xn);
  gemm_kernel<<<dim3(3072/16,S/16),256,0,stream>>>(xn,D,wqkv,3072,qkv,3072,S,3072,D);
  gemm_kernel<<<dim3(F/16,S/16),256,0,stream>>>(xn,D,wg,F,g,F,S,F,D);
  gemm_kernel<<<dim3(F/16,S/16),256,0,stream>>>(xn,D,wu,F,u,F,S,F,D);
  phi_kernel<<<dim3(S,16),64,0,stream>>>(qkv,0,wphi,phiq);
  phi_kernel<<<dim3(S,16),64,0,stream>>>(qkv,1024,wphi,phik);
  kv_kernel<<<dim3(32,64),32,0,stream>>>(phik,qkv,kv,z);
  o_kernel<<<dim3(S,16),64,0,stream>>>(phiq,kv,z,oo);
  geglu_kernel<<<(S*F+255)/256,256,0,stream>>>(g,u,h,S*F);
  gemm_kernel<<<dim3(D/16,S/16),256,0,stream>>>(oo,D,wo,D,a,D,S,D,D);
  gemm_kernel<<<dim3(D/16,S/16),256,0,stream>>>(h,F,wd,D,b,D,S,D,F);
  final_kernel<<<(S*D+255)/256,256,0,stream>>>(x,a,b,(u16*)d_out,S*D);
}
```

```cpp
#include <hip/hip_runtime.h>

typedef unsigned short u16;
typedef short bf16x8 __attribute__((ext_vector_type(8)));
typedef short s16x4 __attribute__((ext_vector_type(4)));
typedef float f32x4 __attribute__((ext_vector_type(4)));
typedef float f32x2 __attribute__((ext_vector_type(2)));
typedef unsigned u32x4 __attribute__((ext_vector_type(4)));
typedef unsigned u32x2 __attribute__((ext_vector_type(2)));
#define LAS __attribute__((address_space(3)))
#define GAS __attribute__((address_space(1)))
typedef LAS unsigned char* lds_t;

constexpr int S_ = 2048, D_ = 1024, F_ = 3584;
constexpr int NKC2 = 144;

constexpr size_t WS_XN   = 0;
constexpr size_t WS_H    = WS_XN + (size_t)S_ * D_ * 2;
constexpr size_t WS_PQ   = WS_H + (size_t)8 * NKC2 * 16 * 1024;
constexpr size_t WS_PART = WS_PQ + (size_t)32 * 128 * 1024;
constexpr size_t WS_PZ   = WS_PART + (size_t)8 * 32 * 8 * 1024;
constexpr size_t WS_XCH  = WS_PZ + (size_t)8 * 32 * 64 * 4;
constexpr size_t WS_CNT  = WS_XCH + (size_t)64 * 4 * 4 * 2 * 8 * 1024;
constexpr size_t WS_END  = WS_CNT + 64 * 4 * 256;

__device__ __forceinline__ float bf2f(u16 b) { return __uint_as_float(((unsigned)b) << 16); }
__device__ __forceinline__ unsigned pk_bf16(float lo, float hi) {
    typedef __bf16 bf2 __attribute__((ext_vector_type(2)));
    bf2 r = {(__bf16)lo, (__bf16)hi};
    return __builtin_bit_cast(unsigned, r);
}
__device__ __forceinline__ float rbf(float x) { return __uint_as_float(pk_bf16(x, x) & 0xffff0000u); }
__device__ __forceinline__ float lo_f(unsigned w) { return __uint_as_float(w << 16); }
__device__ __forceinline__ float hi_f(unsigned w) { return __uint_as_float(w & 0xffff0000u); }

#define WAIT_V(n) asm volatile("s_waitcnt vmcnt(" #n ")" ::: "memory")
#define WAIT_L0() asm volatile("s_waitcnt lgkmcnt(0)" ::: "memory")
#define BAR() __builtin_amdgcn_s_barrier()
#define SCHED() __builtin_amdgcn_sched_barrier(0)

__device__ __forceinline__ void glds16(const void* g, lds_t l) {
    __builtin_amdgcn_global_load_lds((const GAS unsigned*)g, (LAS unsigned*)l, 16, 0, 0);
}
__device__ __forceinline__ bf16x8 lds_b128(lds_t p) { return *(const LAS bf16x8*)p; }
__device__ __forceinline__ bf16x8 lds_tr2(lds_t p, int off0, int off1) {
    s16x4 a = __builtin_amdgcn_ds_read_tr16_b64_v4i16((LAS s16x4*)(p + off0));
    s16x4 b = __builtin_amdgcn_ds_read_tr16_b64_v4i16((LAS s16x4*)(p + off1));
    return __builtin_shufflevector(a, b, 0, 1, 2, 3, 4, 5, 6, 7);
}
template <int OFF0, int OFF1> __device__ __forceinline__ bf16x8 lds_tr2_asm(unsigned addr) {
    s16x4 a, b;
    asm volatile("ds_read_b64_tr_b16 %0, %2 offset:%3\n\tds_read_b64_tr_b16 %1, %2 offset:%4" : "=&v"(a), "=&v"(b) : "v"(addr), "i"(OFF0), "i"(OFF1) : "memory");
    return __builtin_shufflevector(a, b, 0, 1, 2, 3, 4, 5, 6, 7);
}
#define MFMA(a, b, c) __builtin_amdgcn_mfma_f32_16x16x32_bf16(a, b, c, 0, 0, 0)

__global__ __launch_bounds__(256) void k_ln(const u16* __restrict__ x, unsigned char* __restrict__ ws) {
    __shared__ float red[2][4][8];
    const int tid = threadIdx.x, lane = tid & 63, w = tid >> 6, r8 = lane & 7;
    const int row = blockIdx.x * 8 + r8;
    if (tid == 0) { if (blockIdx.x < 256) *(unsigned*)(ws + WS_CNT + (size_t)blockIdx.x * 256) = 0u; }
    u32x4 v[4];
#pragma unroll
    for (int it = 0; it < 4; ++it) { const int p = it * 32 + w * 8 + (lane >> 3); v[it] = *(const u32x4*)(x + (size_t)row * D_ + p * 8); }
    float f[32]; float s = 0.f;
#pragma unroll
    for (int it = 0; it < 4; ++it)
#pragma unroll
        for (int j = 0; j < 4; ++j) { f[it * 8 + 2 * j] = lo_f(v[it][j]); f[it * 8 + 2 * j + 1] = hi_f(v[it][j]); s += f[it * 8 + 2 * j] + f[it * 8 + 2 * j + 1]; }
    s += __shfl_xor(s, 8); s += __shfl_xor(s, 16); s += __shfl_xor(s, 32);
    if (lane < 8) red[0][w][lane] = s;
    __syncthreads();
    const float mean = (red[0][0][r8] + red[0][1][r8] + red[0][2][r8] + red[0][3][r8]) * (1.0f / D_);
    float q = 0.f;
#pragma unroll
    for (int i = 0; i < 32; ++i) { const float d = f[i] - mean; q += d * d; }
    q += __shfl_xor(q, 8); q += __shfl_xor(q, 16); q += __shfl_xor(q, 32);
    if (lane < 8) red[1][w][lane] = q;
    __syncthreads();
    const float var = (red[1][0][r8] + red[1][1][r8] + red[1][2][r8] + red[1][3][r8]) * (1.0f / D_);
    const float m = rbf(mean), sd = rbf(sqrtf(rbf(rbf(var) + 1e-5f)));
    const int panel = row >> 8, rb = (row >> 4) & 15, r16 = row & 15;
#pragma unroll
    for (int it = 0; it < 4; ++it) {
        const int p = it * 32 + w * 8 + (lane >> 3), kc = p >> 2, kq = p & 3;
        u32x4 o;
#pragma unroll
        for (int j = 0; j < 4; ++j) o[j] = pk_bf16(rbf(f[it * 8 + 2 * j] - m) / sd, rbf(f[it * 8 + 2 * j + 1] - m) / sd);
        *(u32x4*)(ws + WS_XN + ((size_t)(panel * 32 + kc) * 16 + rb) * 1024 + (kq * 16 + r16) * 16) = o;
    }
}

constexpr int G1_SA = 16384, G1_STAGE = 36864, G1_NST = 4, G1_LDS_MAIN = G1_STAGE * G1_NST;
constexpr int G1_WPHI = G1_LDS_MAIN;
constexpr int G1_LDS = G1_LDS_MAIN + 4096;

__device__ __forceinline__ int swzB(int k) { return (((k >> 1) & 1) << 1) | (((k >> 3) & 1) << 2); }
__device__ __forceinline__ int swzP(int k) { return ((k >> 2) & 1) | (((k >> 1) & 1) << 1) | (((k >> 3) & 1) << 2); }

__device__ __forceinline__ float gelu_mul(float g, float u) {
    constexpr float C0 = -2.0f * 0.7978845608028654f * 1.4426950408889634f, C1 = C0 * 0.044715f;
    const float t = g * g, p = __builtin_fmaf(t, C1, C0), e = __builtin_amdgcn_exp2f(g * p);
    return g * u * __builtin_amdgcn_rcpf(1.0f + e);
}
__device__ __forceinline__ u32x4 pack_frag(const float (&a)[4], const float (&b)[4]) {
    unsigned x0 = pk_bf16(a[0], a[1]), x1 = pk_bf16(a[2], a[3]), y0 = pk_bf16(b[0], b[1]), y1 = pk_bf16(b[2], b[3]);
    auto r0 = __builtin_amdgcn_permlane16_swap(x0, y0, false, false);
    auto r1 = __builtin_amdgcn_permlane16_swap(x1, y1, false, false);
    u32x4 o; o[0] = r0[0]; o[1] = r1[0]; o[2] = r0[1]; o[3] = r1[1];
    return o;
}
__device__ __forceinline__ int frag_off(int lane) { const int g = lane >> 4, kq = ((g & 1) << 1) | (g >> 1); return (kq * 16 + (lane & 15)) * 16; }

struct SoftPM { bf16x8 p, m; };
__device__ __forceinline__ void soft_pm(const float (&pv)[8], float (&pp)[8], float (&pm)[8]) {
    float mx = pv[0], mn = pv[0];
#pragma unroll
    for (int i = 1; i < 8; ++i) { mx = fmaxf(mx, pv[i]); mn = fminf(mn, pv[i]); }
    mx = fmaxf(mx, __shfl_xor(mx, 16)); mx = fmaxf(mx, __shfl_xor(mx, 32));
    mn = fminf(mn, __shfl_xor(mn, 16)); mn = fminf(mn, __shfl_xor(mn, 32));
    constexpr float L2E = 1.4426950408889634f;
    float sp = 0.f, sm = 0.f;
#pragma unroll
    for (int i = 0; i < 8; ++i) { pp[i] = __builtin_amdgcn_exp2f((pv[i] - mx) * L2E); pm[i] = __builtin_amdgcn_exp2f((mn - pv[i]) * L2E); sp += pp[i]; sm += pm[i]; }
    sp += __shfl_xor(sp, 16); sp += __shfl_xor(sp, 32);
    sm += __shfl_xor(sm, 16); sm += __shfl_xor(sm, 32);
    const float rp = 1.0f / sp, rm = 1.0f / sm;
#pragma unroll
    for (int i = 0; i < 8; ++i) { pp[i] *= rp; pm[i] *= rm; }
}

template <int GRP>
__device__ __forceinline__ void g1_mainloop(lds_t lds, f32x4 (&acc)[8][5], const u16* __restrict__ wqkv, const u16* __restrict__ wg, const u16* __restrict__ wu,
                                            const unsigned char* __restrict__ xn, const int j, const int kvt, const int wl, const int lane, const int (&ba)[5]) {
    const unsigned char* bp[5]; const unsigned char* ap = nullptr;
    const size_t ldq = (size_t)32 * 3072 * 2, ldg = (size_t)32 * F_ * 2, ld4 = kvt ? ldq : ldg;
    if (GRP == 0) {
        const int gq = (j & 1) ? 7 * j + 3 : 7 * j, gp = (j & 1) ? 7 * j + 1 : 7 * j + 4, gh = (j & 1) ? 7 * j : 7 * j + 6;
        const int krow = 8 * wl + (lane >> 3), chunk = (lane & 7) ^ swzB(krow);
        const size_t rq = (size_t)krow * 3072 * 2 + chunk * 16, rg = (size_t)krow * F_ * 2 + chunk * 16;
        if (!kvt) {
            bp[0] = (const unsigned char*)(wqkv + 64 * j) + rq;
            bp[1] = (const unsigned char*)(wg + 32 * gq) + rg;      bp[2] = (const unsigned char*)(wg + 32 * gq + 64) + rg;
            bp[3] = (const unsigned char*)(wu + 32 * gq) + rg;      bp[4] = (const unsigned char*)(wu + 32 * gq + 64) + rg;
        } else {
            bp[0] = (const unsigned char*)(wqkv + 1024 + 64 * j) + rq;
            bp[1] = (const unsigned char*)(wg + 32 * gp) + rg;
            bp[2] = (chunk < 4 ? (const unsigned char*)(wg + 32 * gh) : (const unsigned char*)(wu + 32 * gh) - 64) + rg;
            bp[3] = (const unsigned char*)(wu + 32 * gp) + rg;
            bp[4] = (const unsigned char*)(wqkv + 2048 + 64 * j) + rq;
        }
    } else {
        ap = xn + wl * 4096 + lane * 16;
    }
    const int aoff = GRP * 8192 + lane * 16;
    auto issue = [&](int so) {
        if (GRP == 0) {
#pragma unroll
            for (int c = 0; c < 5; ++c) { glds16(bp[c], lds + so + G1_SA + c * 4096 + wl * 1024); }
            bp[0] += ldq; bp[1] += ldg; bp[2] += ldg; bp[3] += ldg; bp[4] += ld4;
        } else {
#pragma unroll
            for (int i = 0; i < 4; ++i) glds16(ap + i * 1024, lds + so + wl * 4096 + i * 1024);
            ap += 16384;
        }
    };
    issue(0); issue(G1_STAGE); issue(2 * G1_STAGE);
    if (GRP == 0) WAIT_V(10); else WAIT_V(8);
    BAR();
    if (GRP == 1) BAR();
    int so = 0, sl = 3 * G1_STAGE;
    bf16x8 A[8], B[5];
#pragma unroll 1
    for (int t = 0; t < 32; ++t) {
        const bool more = t < 29;
#pragma unroll
        for (int n = 0; n < 5; ++n) B[n] = lds_tr2_asm<0, 512>((unsigned)(size_t)lds + so + ba[n]);
#pragma unroll
        for (int m = 0; m < 8; ++m) A[m] = lds_b128(lds + so + aoff + m * 1024);
        if (more) issue(sl);
        WAIT_L0();
        if (GRP == 1) { if (more) WAIT_V(8); else WAIT_V(0); }
        SCHED(); BAR(); SCHED();
        __builtin_amdgcn_s_setprio(1);
#pragma unroll
        for (int m = 0; m < 8; ++m)
#pragma unroll
            for (int n = 0; n < 5; ++n) acc[m][n] = MFMA(B[n], A[m], acc[m][n]);
        __builtin_amdgcn_s_setprio(0);
        if (GRP == 0) { if (more) WAIT_V(10); else WAIT_V(0); }
        SCHED(); BAR(); SCHED();
        so = (so == 3 * G1_STAGE) ? 0 : so + G1_STAGE;
        sl = (sl == 3 * G1_STAGE) ? 0 : sl + G1_STAGE;
    }
    if (GRP == 0) BAR();
}

__device__ __forceinline__ void g1_epilogue(lds_t lds, f32x4 (&acc)[8][5], unsigned char* __restrict__ ws, const int wid, const int panel, const int j, const int kvt) {
    int tz = threadIdx.x; asm volatile("" : "+v"(tz));
    const int lane = tz & 63, g = lane >> 4, l15 = lane & 15, grp = wid >> 2, wc = wid & 3;
    const int gq = (j & 1) ? 7 * j + 3 : 7 * j, gp = (j & 1) ? 7 * j + 1 : 7 * j + 4, gh = (j & 1) ? 7 * j : 7 * j + 6;
    constexpr int E_KQ = 0, E_V = 40960, E_PHI = E_V + 32768;
    {
        const int head = wc >> 1, dt = wc & 1;
#pragma unroll
        for (int m = 0; m < 8; ++m) {
            const int tok = 128 * grp + 16 * m + l15;
            u32x2 w2 = {pk_bf16(acc[m][4][0], acc[m][4][1]), pk_bf16(acc[m][4][2], acc[m][4][3])};
            *(LAS u32x2*)(lds + E_KQ + head * 20480 + tok * 80 + dt * 32 + g * 8) = w2;
        }
        if (kvt && wc == 3) {
#pragma unroll
            for (int m = 0; m < 8; ++m) {
                const int tok = 128 * grp + 16 * m + l15, sw = swzP(tok);
#pragma unroll
                for (int n = 0; n < 4; ++n) {
                    u32x2 w2 = {pk_bf16(acc[m][n][0], acc[m][n][1]), pk_bf16(acc[m][n][2], acc[m][n][3])};
                    *(LAS u32x2*)(lds + E_V + tok * 128 + (((2 * n + (g >> 1)) ^ sw) << 4) + (g & 1) * 8) = w2;
                }
            }
        }
    }
    if (!(kvt && wc == 3)) {
        const int kc = !kvt ? gq + wc : (wc < 2 ? gp + wc : gh);
        unsigned char* hb = ws + WS_H + ((size_t)(panel * NKC2 + kc) * 16 + 8 * grp) * 1024 + frag_off(lane);
#pragma unroll
        for (int m = 0; m < 8; ++m) {
            float a[4], b[4];
#pragma unroll
            for (int e = 0; e < 4; ++e) { a[e] = gelu_mul(acc[m][0][e], acc[m][2][e]); b[e] = gelu_mul(acc[m][1][e], acc[m][3][e]); }
            *(u32x4*)(hb + m * 1024) = pack_frag(a, b);
        }
    }
    __syncthreads();
    {
        const int q = (lane >> 2) & 3, p = lane & 3;
#pragma unroll
        for (int hh = 0; hh < 2; ++hh) {
            bf16x8 wf[2];
#pragma unroll
            for (int ft = 0; ft < 2; ++ft) wf[ft] = lds_tr2(lds + G1_WPHI + hh * 2048 + (8 * g + q) * 64 + ft * 32 + p * 8, 0, 256);
#pragma unroll
            for (int tb = 0; tb < 2; ++tb) {
                const int tok = 32 * wid + 16 * tb + l15;
                const bf16x8 qf = lds_b128(lds + E_KQ + hh * 20480 + tok * 80 + g * 16);
                const f32x4 z4 = {0.f, 0.f, 0.f, 0.f};
                const f32x4 p0 = MFMA(wf[0], qf, z4), p1 = MFMA(wf[1], qf, z4);
                const unsigned w0 = pk_bf16(p0[0], p0[1]), w1 = pk_bf16(p0[2], p0[3]), w2 = pk_bf16(p1[0], p1[1]), w3 = pk_bf16(p1[2], p1[3]);
                if (!kvt) {
                    u32x4 o = {w0, w1, w2, w3};
                    *(u32x4*)(ws + WS_PQ + ((size_t)(2 * j + hh) * 128 + panel * 16 + 2 * wid + tb) * 1024 + lane * 16) = o;
                } else {
                    const float pv[8] = {lo_f(w0), hi_f(w0), lo_f(w1), hi_f(w1), lo_f(w2), hi_f(w2), lo_f(w3), hi_f(w3)};
                    float pp[8], pm[8];
                    soft_pm(pv, pp, pm);
                    const int sw = swzP(tok);
                    lds_t pb = lds + E_PHI + hh * 32768 + tok * 128 + (g & 1) * 8;
                    *(LAS u32x2*)(pb + (((0 + (g >> 1)) ^ sw) << 4)) = (u32x2){pk_bf16(pp[0], pp[1]), pk_bf16(pp[2], pp[3])};
                    *(LAS u32x2*)(pb + (((2 + (g >> 1)) ^ sw) << 4)) = (u32x2){pk_bf16(pp[4], pp[5]), pk_bf16(pp[6], pp[7])};
                    *(LAS u32x2*)(pb + (((4 + (g >> 1)) ^ sw) << 4)) = (u32x2){pk_bf16(pm[0], pm[1]), pk_bf16(pm[2], pm[3])};
                    *(LAS u32x2*)(pb + (((6 + (g >> 1)) ^ sw) << 4)) = (u32x2){pk_bf16(pm[4], pm[5]), pk_bf16(pm[6], pm[7])};
                }
            }
        }
    }
    if (kvt) {
        __syncthreads();
        const int hh = wid >> 2, ft = wid & 3, q = (lane >> 2) & 3, p = lane & 3;
        f32x4 kv0 = {0.f, 0.f, 0.f, 0.f}, kv1 = kv0, zz = kv0;
        const bf16x8 ones = {0x3F80, 0x3F80, 0x3F80, 0x3F80, 0x3F80, 0x3F80, 0x3F80, 0x3F80};
#pragma unroll
        for (int ks = 0; ks < 8; ++ks) {
            const int tok = 32 * ks + 8 * g + q;
            const int s0 = swzP(tok), s1 = swzP(tok + 4);
            auto rd = [&](lds_t base, int tl) {
                const int c = 2 * tl + (p >> 1);
                s16x4 a = __builtin_amdgcn_ds_read_tr16_b64_v4i16((LAS s16x4*)(base + tok * 128 + ((c ^ s0) << 4) + (p & 1) * 8));
                s16x4 b = __builtin_amdgcn_ds_read_tr16_b64_v4i16((LAS s16x4*)(base + (tok + 4) * 128 + ((c ^ s1) << 4) + (p & 1) * 8));
                return __builtin_shufflevector(a, b, 0, 1, 2, 3, 4, 5, 6, 7);
            };
            const bf16x8 pf = rd(lds + E_PHI + hh * 32768, ft);
            const bf16x8 v0 = rd(lds + E_V, 2 * hh), v1 = rd(lds + E_V, 2 * hh + 1);
            kv0 = MFMA(pf, v0, kv0); kv1 = MFMA(pf, v1, kv1); zz = MFMA(pf, ones, zz);
        }
        unsigned char* pb = ws + WS_PART + ((size_t)((panel * 32 + 2 * j + hh) * 4 + ft) * 2) * 1024 + lane * 16;
        *(f32x4*)pb = kv0; *(f32x4*)(pb + 1024) = kv1;
        if (l15 == 0) *(f32x4*)(ws + WS_PZ + ((size_t)(panel * 32 + 2 * j + hh) * 64 + 16 * ft + 4 * g) * 4) = zz;
    }
}

__global__ __launch_bounds__(512, 2) void k_gemm1(const u16* __restrict__ wqkv, const u16* __restrict__ wphi, const u16* __restrict__ wg, const u16* __restrict__ wu,
                                                  unsigned char* __restrict__ ws) {
    extern __shared__ __attribute__((aligned(16))) unsigned char shm[];
    lds_t lds = (lds_t)shm;
    const int tid = threadIdx.x, wid = __builtin_amdgcn_readfirstlane(tid >> 6), lane = tid & 63;
    const int grp = wid >> 2, wc = wid & 3;
    const int xcd = blockIdx.x & 7, r = blockIdx.x >> 3, panel = r & 7, ct = r >> 3, j = 2 * xcd + (ct >> 1), kvt = ct & 1;
    const int g = lane >> 4;
    if (wid < 4) glds16(wphi + (size_t)(2 * j) * 1024 + wid * 512 + lane * 8, lds + G1_WPHI + wid * 1024);

    int ba[5];
    {
        int cgA, tlA, cgB, tlB;
        if (!kvt) { cgA = 1 + (wc >> 1); tlA = 2 * (wc & 1); cgB = 3 + (wc >> 1); tlB = tlA; }
        else if (wc < 2) { cgA = 1; tlA = 2 * wc; cgB = 3; tlB = 2 * wc; }
        else if (wc == 2) { cgA = 2; tlA = 0; cgB = 2; tlB = 2; }
        else { cgA = 4; tlA = 0; cgB = 4; tlB = 2; }
        const int q = (lane >> 2) & 3, p = lane & 3, krow = 8 * g + q, sw = swzB(krow);
        auto adr = [&](int cg, int tl) { return G1_SA + cg * 4096 + krow * 128 + (((2 * tl + (p >> 1)) ^ sw) << 4) + (p & 1) * 8; };
        ba[0] = adr(cgA, tlA); ba[1] = adr(cgA, tlA + 1); ba[2] = adr(cgB, tlB); ba[3] = adr(cgB, tlB + 1); ba[4] = adr(0, wc);
    }
    f32x4 acc[8][5];
#pragma unroll
    for (int m = 0; m < 8; ++m)
#pragma unroll
        for (int n = 0; n < 5; ++n) acc[m][n] = (f32x4){0.f, 0.f, 0.f, 0.f};

    const unsigned char* xn = ws + WS_XN + (size_t)panel * 32 * 16384;
    if (grp == 0) g1_mainloop<0>(lds, acc, wqkv, wg, wu, xn, j, kvt, wc, lane, ba);
    else          g1_mainloop<1>(lds, acc, wqkv, wg, wu, xn, j, kvt, wc, lane, ba);

    g1_epilogue(lds, acc, ws, wid, panel, j, kvt);
}

__global__ __launch_bounds__(256) void k_attn_o(unsigned char* __restrict__ ws) {
    __shared__ __attribute__((aligned(16))) unsigned char sm[4 * 3 * 512];
    const int tid = threadIdx.x, lane = tid & 63, w = tid >> 6, g = lane >> 4;
    const int xcd = blockIdx.x & 7, r = blockIdx.x >> 3, head = 4 * xcd + (r & 3), panel = r >> 2;
    u32x4 pq[4];
#pragma unroll
    for (int i = 0; i < 4; ++i) pq[i] = *(const u32x4*)(ws + WS_PQ + ((size_t)head * 128 + panel * 16 + 4 * w + i) * 1024 + lane * 16);
    {
        f32x4 a0 = {0.f, 0.f, 0.f, 0.f}, a1 = a0, az = a0;
#pragma unroll
        for (int p = 0; p < 8; ++p) {
            const unsigned char* pb = ws + WS_PART + ((size_t)((p * 32 + head) * 4 + w) * 2) * 1024 + lane * 16;
            a0 += *(const f32x4*)pb; a1 += *(const f32x4*)(pb + 1024);
            az += *(const f32x4*)(ws + WS_PZ + ((size_t)(p * 32 + head) * 64 + 16 * w + 4 * g) * 4);
        }
        *(u32x2*)(sm + (w * 3 + 0) * 512 + lane * 8) = (u32x2){pk_bf16(a0[0], a0[1]), pk_bf16(a0[2], a0[3])};
        *(u32x2*)(sm + (w * 3 + 1) * 512 + lane * 8) = (u32x2){pk_bf16(a1[0], a1[1]), pk_bf16(a1[2], a1[3])};
        *(u32x2*)(sm + (w * 3 + 2) * 512 + lane * 8) = (u32x2){pk_bf16(az[0], az[1]), pk_bf16(az[2], az[3])};
    }
    __syncthreads();
    bf16x8 kf[2][3];
#pragma unroll
    for (int s = 0; s < 2; ++s)
#pragma unroll
        for (int d = 0; d < 3; ++d) {
            const u32x2 a = *(const u32x2*)(sm + ((2 * s) * 3 + d) * 512 + lane * 8), b = *(const u32x2*)(sm + ((2 * s + 1) * 3 + d) * 512 + lane * 8);
            const u32x4 t = {a[0], a[1], b[0], b[1]};
            kf[s][d] = __builtin_bit_cast(bf16x8, t);
        }
#pragma unroll
    for (int i = 0; i < 4; ++i) {
        const float pv[8] = {lo_f(pq[i][0]), hi_f(pq[i][0]), lo_f(pq[i][1]), hi_f(pq[i][1]), lo_f(pq[i][2]), hi_f(pq[i][2]), lo_f(pq[i][3]), hi_f(pq[i][3])};
        float pp[8], pm[8];
        soft_pm(pv, pp, pm);
        const u32x4 tp = {pk_bf16(pp[0], pp[1]), pk_bf16(pp[2], pp[3]), pk_bf16(pp[4], pp[5]), pk_bf16(pp[6], pp[7])};
        const u32x4 tm = {pk_bf16(pm[0], pm[1]), pk_bf16(pm[2], pm[3]), pk_bf16(pm[4], pm[5]), pk_bf16(pm[6], pm[7])};
        const bf16x8 P = __builtin_bit_cast(bf16x8, tp), M = __builtin_bit_cast(bf16x8, tm);
        const f32x4 z4 = {0.f, 0.f, 0.f, 0.f};
        const f32x4 n0 = MFMA(kf[1][0], M, MFMA(kf[0][0], P, z4));
        const f32x4 n1 = MFMA(kf[1][1], M, MFMA(kf[0][1], P, z4));
        const f32x4 dn = MFMA(kf[1][2], M, MFMA(kf[0][2], P, z4));
        const float den = rbf(rbf(dn[0]) + 1e-6f);
        float a[4], b[4];
#pragma unroll
        for (int e = 0; e < 4; ++e) { a[e] = rbf(n0[e]) / den; b[e] = rbf(n1[e]) / den; }
        *(u32x4*)(ws + WS_H + ((size_t)(panel * NKC2 + 112 + head) * 16 + 4 * w + i) * 1024 + frag_off(lane)) = pack_frag(a, b);
    }
}

constexpr int G2_SA = 32768, G2_STAGE = 49152, G2_LDS = 3 * G2_STAGE;

template <int GRP>
__device__ __forceinline__ void g2_mainloop(lds_t lds, f32x4 (&acc)[4][4], const unsigned char* ap, const unsigned char* wd_l, const unsigned char* wo_l,
                                            const int kc0, const int wl, const int lane, const int aoff, const int (&ba)[2]) {
    auto issue = [&](int so, int t) {
#pragma unroll
        for (int i = 0; i < 4; ++i) glds16(ap + (size_t)t * 32768 + i * 1024, lds + so + (wl >> 1) * 16384 + (GRP * 8 + (wl & 1) * 4 + i) * 1024);
        if (GRP == 0) {
#pragma unroll
            for (int s = 0; s < 2; ++s) {
                const int kc = kc0 + 2 * t + s;
                const unsigned char* b = (kc < 112) ? wd_l + (size_t)kc * 65536 : wo_l + (size_t)(kc - 112) * 65536;
#pragma unroll
                for (int c = 0; c < 2; ++c) glds16(b + c * 128, lds + so + G2_SA + s * 8192 + c * 4096 + wl * 1024);
            }
        }
    };
    issue(0, 0); issue(G2_STAGE, 1);
    if (GRP == 0) WAIT_V(8); else WAIT_V(4);
    BAR();
    if (GRP == 1) BAR();
    int so = 0, sl = 2 * G2_STAGE;
    bf16x8 A[2][4], B[2][4];
#pragma unroll 1
    for (int t = 0; t < 18; ++t) {
        const bool more = t < 16;
#pragma unroll
        for (int s = 0; s < 2; ++s) {
#pragma unroll
            for (int n = 0; n < 4; ++n) B[s][n] = lds_tr2_asm<0, 512>((unsigned)(size_t)lds + so + s * 8192 + (ba[n & 1] ^ ((n >> 1) << 6)));
#pragma unroll
            for (int m = 0; m < 4; ++m) A[s][m] = lds_b128(lds + so + s * 16384 + aoff + m * 1024);
        }
        if (more) issue(sl, t + 2);
        WAIT_L0();
        SCHED(); BAR(); SCHED();
        __builtin_amdgcn_s_setprio(1);
#pragma unroll
        for (int s = 0; s < 2; ++s)
#pragma unroll
            for (int m = 0; m < 4; ++m)
#pragma unroll
                for (int n = 0; n < 4; ++n) acc[m][n] = MFMA(B[s][n], A[s][m], acc[m][n]);
        __builtin_amdgcn_s_setprio(0);
        if (more) { if (GRP == 0) WAIT_V(8); else WAIT_V(4); } else WAIT_V(0);
        SCHED(); BAR(); SCHED();
        so = (so == 2 * G2_STAGE) ? 0 : so + G2_STAGE;
        sl = (sl == 2 * G2_STAGE) ? 0 : sl + G2_STAGE;
    }
    if (GRP == 0) BAR();
}

__global__ __launch_bounds__(512, 2) void k_gemm2(const u16* __restrict__ x, const u16* __restrict__ wd, const u16* __restrict__ wo,
                                                  unsigned char* __restrict__ ws, u16* __restrict__ out) {
    extern __shared__ __attribute__((aligned(16))) unsigned char shm[];
    lds_t lds = (lds_t)shm;
    const int tid = threadIdx.x, wid = __builtin_amdgcn_readfirstlane(tid >> 6), lane = tid & 63;
    const int grp = wid >> 2, wl = wid & 3, wm = wl >> 1, wn = wl & 1;
    const int xcd = blockIdx.x & 7, r = blockIdx.x >> 3, ks = xcd >> 1, pm = 4 * (xcd & 1) + (r & 3), pn = r >> 2;
    const int g = lane >> 4, l15 = lane & 15;
    const int kc0 = 36 * ks;
    const unsigned char* ap = ws + WS_H + ((size_t)(pm * NKC2 + kc0 + (wl >> 1)) * 16 + grp * 8 + (wl & 1) * 4) * 1024 + lane * 16;
    const int krow = 8 * wl + (lane >> 3), chunk = (lane & 7) ^ swzB(krow);
    const unsigned char* wd_l = (const unsigned char*)(wd + (size_t)krow * D_ + pn * 128) + chunk * 16;
    const unsigned char* wo_l = (const unsigned char*)(wo + (size_t)krow * D_ + pn * 128) + chunk * 16;
    int ba[2];
    {
        const int q = (lane >> 2) & 3, p = lane & 3, kr = 8 * g + q, sw = swzB(kr);
        ba[0] = G2_SA + wn * 4096 + kr * 128 + (((0 + (p >> 1)) ^ sw) << 4) + (p & 1) * 8;
        ba[1] = G2_SA + wn * 4096 + kr * 128 + (((2 + (p >> 1)) ^ sw) << 4) + (p & 1) * 8;
    }
    const int aoff = (grp * 8 + wm * 4) * 1024 + lane * 16;
    f32x4 acc[4][4];
#pragma unroll
    for (int m = 0; m < 4; ++m)
#pragma unroll
        for (int n = 0; n < 4; ++n) acc[m][n] = (f32x4){0.f, 0.f, 0.f, 0.f};
    if (grp == 0) g2_mainloop<0>(lds, acc, ap, wd_l, wo_l, kc0, wl, lane, aoff, ba);
    else          g2_mainloop<1>(lds, acc, ap, wd_l, wo_l, kc0, wl, lane, aoff, ba);

    const int tile = pm * 8 + pn, qd = 2 * grp + wm;
    unsigned* cnt = (unsigned*)(ws + WS_CNT + (size_t)(tile * 4 + qd) * 256);
    if (qd != ks) {
        const __amdgpu_buffer_rsrc_t rs = __builtin_amdgcn_make_buffer_rsrc((void*)(ws + WS_XCH), 0, 64 * 4 * 4 * 2 * 8 * 1024, 0x00020000);
        const unsigned base = (unsigned)((((tile * 4 + ks) * 4 + qd) * 2 + wn) * 8) * 1024 + lane * 16;
#pragma unroll
        for (int m = 0; m < 4; ++m)
#pragma unroll
            for (int n2 = 0; n2 < 2; ++n2) {
                const f32x4 a = acc[m][2 * n2], b = acc[m][2 * n2 + 1];
                const u32x4 w = {pk_bf16(a[0], a[1]), pk_bf16(a[2], a[3]), pk_bf16(b[0], b[1]), pk_bf16(b[2], b[3])};
                __builtin_amdgcn_raw_buffer_store_b128(w, rs, base + (m * 2 + n2) * 1024, 0, 16);
            }
        WAIT_V(0);
        if (lane == 0) __hip_atomic_fetch_add(cnt, 1u, __ATOMIC_RELAXED, __HIP_MEMORY_SCOPE_AGENT);
    } else {
        const int row0 = pm * 256 + qd * 64 + l15, col0 = pn * 128 + wn * 64;
        const int kq = ((g & 1) << 1) | (g >> 1);
        u32x4 xr[4][2];
#pragma unroll
        for (int m = 0; m < 4; ++m)
#pragma unroll
            for (int n2 = 0; n2 < 2; ++n2) xr[m][n2] = *(const u32x4*)(x + (size_t)(row0 + 16 * m) * D_ + col0 + 32 * n2 + 8 * kq);
        unsigned spins = 0;
        while ((unsigned)__builtin_amdgcn_readfirstlane(__hip_atomic_load(cnt, __ATOMIC_RELAXED, __HIP_MEMORY_SCOPE_AGENT)) < 6u) {
            if (++spins > (1u << 22)) break;
            __builtin_amdgcn_s_sleep(1);
        }
        const __amdgpu_buffer_rsrc_t rs = __builtin_amdgcn_make_buffer_rsrc((void*)(ws + WS_XCH), 0, 64 * 4 * 4 * 2 * 8 * 1024, 0x00020000);
#pragma unroll
        for (int s = 1; s < 4; ++s) {
            const int src = (ks + s) & 3;
            const unsigned base = (unsigned)((((tile * 4 + src) * 4 + qd) * 2 + wn) * 8) * 1024 + lane * 16;
#pragma unroll
            for (int m = 0; m < 4; ++m)
#pragma unroll
                for (int n2 = 0; n2 < 2; ++n2) {
                    const u32x4 w = __builtin_amdgcn_raw_buffer_load_b128(rs, base + (m * 2 + n2) * 1024, 0, 16);
                    acc[m][2 * n2][0] += lo_f(w[0]); acc[m][2 * n2][1] += hi_f(w[0]); acc[m][2 * n2][2] += lo_f(w[1]); acc[m][2 * n2][3] += hi_f(w[1]);
                    acc[m][2 * n2 + 1][0] += lo_f(w[2]); acc[m][2 * n2 + 1][1] += hi_f(w[2]); acc[m][2 * n2 + 1][2] += lo_f(w[3]); acc[m][2 * n2 + 1][3] += hi_f(w[3]);
                }
        }
#pragma unroll
        for (int m = 0; m < 4; ++m)
#pragma unroll
            for (int n2 = 0; n2 < 2; ++n2) {
                float a[4], b[4];
#pragma unroll
                for (int e = 0; e < 4; ++e) {
                    auto rr = __builtin_amdgcn_permlane16_swap(__float_as_uint(acc[m][2 * n2][e]), __float_as_uint(acc[m][2 * n2 + 1][e]), false, false);
                    a[e] = __uint_as_float(rr[0]); b[e] = __uint_as_float(rr[1]);
                }
                const u32x4 xv = xr[m][n2];
                u32x4 o;
                o[0] = pk_bf16(a[0] + lo_f(xv[0]), a[1] + hi_f(xv[0])); o[1] = pk_bf16(a[2] + lo_f(xv[1]), a[3] + hi_f(xv[1]));
                o[2] = pk_bf16(b[0] + lo_f(xv[2]), b[1] + hi_f(xv[2])); o[3] = pk_bf16(b[2] + lo_f(xv[3]), b[3] + hi_f(xv[3]));
                *(u32x4*)(out + (size_t)(row0 + 16 * m) * D_ + col0 + 32 * n2 + 8 * kq) = o;
            }
    }
}

extern "C" void kernel_launch(void* const* d_in, const int* in_sizes, int n_in, void* d_out, int out_size, void* d_ws, size_t ws_size, hipStream_t stream) {
    const u16* x = (const u16*)d_in[0]; const u16* wqkv = (const u16*)d_in[1]; const u16* wphi = (const u16*)d_in[2]; const u16* wo = (const u16*)d_in[3];
    const u16* wg = (const u16*)d_in[5]; const u16* wu = (const u16*)d_in[6]; const u16* wd = (const u16*)d_in[7];
    unsigned char* ws = (unsigned char*)d_ws;
    hipFuncSetAttribute((const void*)k_gemm1, hipFuncAttributeMaxDynamicSharedMemorySize, G1_LDS);
    hipFuncSetAttribute((const void*)k_gemm2, hipFuncAttributeMaxDynamicSharedMemorySize, G2_LDS);
    k_ln<<<256, 256, 0, stream>>>(x, ws);
    k_gemm1<<<256, 512, G1_LDS, stream>>>(wqkv, wphi, wg, wu, ws);
    k_attn_o<<<256, 256, 0, stream>>>(ws);
    k_gemm2<<<256, 512, G2_LDS, stream>>>(x, wd, wo, ws, (u16*)d_out);
}
```

```cpp
#include <hip/hip_runtime.h>
#include <type_traits>

typedef unsigned short u16;
typedef unsigned long long u64;
typedef short bf16x8 __attribute__((ext_vector_type(8)));
typedef short s16x4 __attribute__((ext_vector_type(4)));
typedef float f32x4 __attribute__((ext_vector_type(4)));
typedef float f32x2 __attribute__((ext_vector_type(2)));
typedef unsigned u32x4 __attribute__((ext_vector_type(4)));
typedef unsigned u32x2 __attribute__((ext_vector_type(2)));
#define LAS __attribute__((address_space(3)))
#define GAS __attribute__((address_space(1)))
typedef LAS unsigned char* lds_t;
typedef const unsigned char* cptr;

constexpr int S_ = 2048, D_ = 1024, F_ = 3584;
constexpr int NKC2 = 144;

constexpr unsigned WS_XN   = 0;
constexpr unsigned WS_H    = WS_XN + S_ * D_ * 2;
constexpr unsigned WS_PART = WS_H + 8 * NKC2 * 16 * 1024;
constexpr unsigned WS_PZ   = WS_PART + 8 * 32 * 4 * 1024;
constexpr unsigned WS_XCH  = WS_PZ + 8 * 32 * 64 * 4;
constexpr unsigned WS_FLG  = WS_XCH + 64 * 4 * 8 * 8 * 1024;
constexpr unsigned FL_LN = 0, FL_KV = FL_LN + 256, FL_H = FL_KV + 128, FL_O = FL_H + 256, FL_X = FL_O + 128, FL_EPOCH = FL_X + 2048, FL_N = FL_EPOCH + 1;
constexpr unsigned WS_END  = WS_FLG + FL_N * 64;

constexpr int G1_SA = 16384, G1_STAGE = 36864;
constexpr int L_MISC = 158720, L_WPHI = 159744, L_TOTAL = 163840;
constexpr int E_KP = 0, E_V = 65536;
constexpr int G2_SB = 98304;

__device__ __forceinline__ unsigned pk_bf16(float lo, float hi) {
    typedef __bf16 bf2 __attribute__((ext_vector_type(2)));
    bf2 r = {(__bf16)lo, (__bf16)hi};
    return __builtin_bit_cast(unsigned, r);
}
__device__ __forceinline__ float rbf(float x) { return __uint_as_float(pk_bf16(x, x) & 0xffff0000u); }
__device__ __forceinline__ float lo_f(unsigned w) { return __uint_as_float(w << 16); }
__device__ __forceinline__ float hi_f(unsigned w) { return __uint_as_float(w & 0xffff0000u); }

#define STAMP_DECL
#define STAMP(i)
#define STAMP_ARG
#define STAMP_PASS
#define STAMP_OUT(kern, lo, n)

#define WAIT_V(n) asm volatile("s_waitcnt vmcnt(" #n ")" ::: "memory")
#define WAIT_L0() do { asm volatile("" ::: "memory"); __builtin_amdgcn_s_waitcnt(0xC07F); asm volatile("" ::: "memory"); } while (0)
#define BAR() __builtin_amdgcn_s_barrier()
#define LBAR() asm volatile("s_waitcnt lgkmcnt(0)\n\ts_barrier" ::: "memory")
#define SCHED() __builtin_amdgcn_sched_barrier(0)
#define MFMA(a, b, c) __builtin_amdgcn_mfma_f32_16x16x32_bf16(a, b, c, 0, 0, 0)

__device__ __forceinline__ cptr uni(cptr p) {
    const u64 v = (u64)p;
    return (cptr)(((u64)(unsigned)__builtin_amdgcn_readfirstlane((unsigned)(v >> 32)) << 32) | (unsigned)__builtin_amdgcn_readfirstlane((unsigned)v));
}
__device__ __forceinline__ void glds_v(cptr g, unsigned l) {
    asm volatile("s_mov_b32 m0, %1\n\ts_nop 0\n\tglobal_load_lds_dwordx4 %0, off" ::"v"(g), "s"(l) : "memory");
}
__device__ __forceinline__ void glds_s(unsigned voff, cptr sb, unsigned l) {
    asm volatile("s_nop 2\n\ts_mov_b32 m0, %2\n\ts_nop 0\n\tglobal_load_lds_dwordx4 %0, %1" ::"v"(voff), "s"(sb), "s"(l) : "memory");
}
__device__ __forceinline__ void glds_s_o128(unsigned voff, cptr sb, unsigned l) {
    asm volatile("s_mov_b32 m0, %2\n\ts_nop 0\n\tglobal_load_lds_dwordx4 %0, %1 offset:128" ::"v"(voff), "s"(sb), "s"(l - 128u) : "memory");
}
template <int OFF> __device__ __forceinline__ void glds_s_sc1_first(unsigned voff, cptr sb, unsigned l) {
    asm volatile("s_nop 2\n\ts_mov_b32 m0, %2\n\ts_nop 0\n\tglobal_load_lds_dwordx4 %0, %1 offset:%3 sc1" ::"v"(voff), "s"(sb), "s"(l), "i"(OFF) : "memory");
}
template <int OFF> __device__ __forceinline__ void glds_s_sc1_next(unsigned voff, cptr sb) {
    asm volatile("global_load_lds_dwordx4 %0, %1 offset:%2 sc1" ::"v"(voff), "s"(sb), "i"(OFF) : "memory");
}
template <int OFF> __device__ __forceinline__ void gld_sc1(u32x4& d, unsigned voff, cptr sb) {
    asm volatile("global_load_dwordx4 %0, %1, %2 offset:%3 sc1" : "+v"(d) : "v"(voff), "s"(sb), "i"(OFF) : "memory");
}
__device__ __forceinline__ bf16x8 lds_b128(lds_t p) { return *(const LAS bf16x8*)p; }
__device__ __forceinline__ bf16x8 lds_tr2(lds_t p0, lds_t p1) {
    s16x4 a = __builtin_amdgcn_ds_read_tr16_b64_v4i16((LAS s16x4*)p0);
    s16x4 b = __builtin_amdgcn_ds_read_tr16_b64_v4i16((LAS s16x4*)p1);
    return __builtin_shufflevector(a, b, 0, 1, 2, 3, 4, 5, 6, 7);
}
template <int OFF> __device__ __forceinline__ bf16x8 lds_tr2_asm(unsigned addr) {
    s16x4 a, b;
    asm volatile("ds_read_b64_tr_b16 %0, %2 offset:%3\n\tds_read_b64_tr_b16 %1, %2 offset:%4" : "=&v"(a), "=&v"(b) : "v"(addr), "i"(OFF), "i"(OFF + 512) : "memory");
    return __builtin_shufflevector(a, b, 0, 1, 2, 3, 4, 5, 6, 7);
}

__device__ __forceinline__ float x16(float v, float& o) { auto r = __builtin_amdgcn_permlane16_swap(__float_as_uint(v), __float_as_uint(v), false, false); o = __uint_as_float(r[1]); return __uint_as_float(r[0]); }
__device__ __forceinline__ float x32(float v, float& o) { auto r = __builtin_amdgcn_permlane32_swap(__float_as_uint(v), __float_as_uint(v), false, false); o = __uint_as_float(r[1]); return __uint_as_float(r[0]); }
__device__ __forceinline__ float sum_g(float v) { float o, a = x16(v, o); v = a + o; a = x32(v, o); return a + o; }
__device__ __forceinline__ float max_g(float v) { float o, a = x16(v, o); v = fmaxf(a, o); a = x32(v, o); return fmaxf(a, o); }
__device__ __forceinline__ float min_g(float v) { float o, a = x16(v, o); v = fminf(a, o); a = x32(v, o); return fminf(a, o); }
__device__ __forceinline__ float ror8(float v) { return __int_as_float(__builtin_amdgcn_update_dpp(0, __float_as_int(v), 0x128, 0xf, 0xf, false)); }

__device__ __forceinline__ int swzB(int k) { return (((k >> 1) & 1) << 1) | (((k >> 3) & 1) << 2); }
__device__ __forceinline__ int swzP(int k) { return ((k >> 2) & 1) | (((k >> 1) & 1) << 1) | (((k >> 3) & 1) << 2); }
__device__ __forceinline__ int swzF(int k) { return ((k >> 2) & 1) | ((k & 3) << 1) | (((k >> 3) & 1) << 3); }

__device__ __forceinline__ f32x2 gelu_mul2(f32x2 g, f32x2 u) {
    constexpr float C0 = -2.0f * 0.7978845608028654f * 1.4426950408889634f, C1 = C0 * 0.044715f;
    const f32x2 a = g * (g * g * C1 + C0);
    const f32x2 d = (f32x2){__builtin_amdgcn_exp2f(a[0]), __builtin_amdgcn_exp2f(a[1])} + 1.0f;
    return (g * u) * (f32x2){__builtin_amdgcn_rcpf(d[0]), __builtin_amdgcn_rcpf(d[1])};
}
__device__ __forceinline__ u32x4 pack_frag(const f32x2 a0, const f32x2 a1, const f32x2 b0, const f32x2 b1) {
    unsigned x0 = pk_bf16(a0[0], a0[1]), x1 = pk_bf16(a1[0], a1[1]), y0 = pk_bf16(b0[0], b0[1]), y1 = pk_bf16(b1[0], b1[1]);
    auto r0 = __builtin_amdgcn_permlane16_swap(x0, y0, false, false);
    auto r1 = __builtin_amdgcn_permlane16_swap(x1, y1, false, false);
    u32x4 o; o[0] = r0[0]; o[1] = r1[0]; o[2] = r0[1]; o[3] = r1[1];
    return o;
}
__device__ __forceinline__ int frag_kq(int lane) { const int g = lane >> 4; return ((g & 1) << 1) | (g >> 1); }
__device__ __forceinline__ int frag_off(int lane) { return (frag_kq(lane) * 16 + (lane & 15)) * 16; }

__device__ __forceinline__ float max3(float a, float b, float c) { float r; asm("v_max3_f32 %0, %1, %2, %3" : "=v"(r) : "v"(a), "v"(b), "v"(c)); return r; }
__device__ __forceinline__ float min3(float a, float b, float c) { float r; asm("v_min3_f32 %0, %1, %2, %3" : "=v"(r) : "v"(a), "v"(b), "v"(c)); return r; }
__device__ __forceinline__ float max2(float a, float b) { float r; asm("v_max_f32 %0, %1, %2" : "=v"(r) : "v"(a), "v"(b)); return r; }
template <int NU, class Op> __device__ __forceinline__ void red2_g(float (&a)[NU], float (&b)[NU], Op op) {
    float z[NU], y[NU];
#pragma unroll
    for (int u = 0; u < NU; ++u) {
        auto r = __builtin_amdgcn_permlane16_swap(__float_as_uint(a[u]), __float_as_uint(b[u]), false, false);
        z[u] = op(__uint_as_float(r[0]), __uint_as_float(r[1]));
    }
#pragma unroll
    for (int u = 0; u < NU; ++u) {
        auto q = __builtin_amdgcn_permlane32_swap(__float_as_uint(z[u]), __float_as_uint(z[u]), false, false);
        y[u] = op(__uint_as_float(q[0]), __uint_as_float(q[1]));
    }
#pragma unroll
    for (int u = 0; u < NU; ++u) {
        auto w = __builtin_amdgcn_permlane16_swap(__float_as_uint(y[u]), __float_as_uint(y[u]), false, false);
        a[u] = __uint_as_float(w[0]); b[u] = __uint_as_float(w[1]);
    }
}
template <int NU> __device__ __forceinline__ void soft_pm(const u32x4 (&w)[NU], u32x4 (&P)[NU], u32x4 (&M)[NU]) {
    constexpr float L2E = 1.4426950408889634f;
    f32x2 pv[NU][4]; float mx[NU], nm[NU];
#pragma unroll
    for (int u = 0; u < NU; ++u) {
#pragma unroll
        for (int i = 0; i < 4; ++i) pv[u][i] = (f32x2){lo_f(w[u][i]), hi_f(w[u][i])};
        mx[u] = max3(max3(max3(pv[u][0][0], pv[u][0][1], pv[u][1][0]), pv[u][1][1], pv[u][2][0]), max2(pv[u][2][1], pv[u][3][0]), pv[u][3][1]);
        nm[u] = -min3(min3(min3(pv[u][0][0], pv[u][0][1], pv[u][1][0]), pv[u][1][1], pv[u][2][0]), min3(pv[u][2][1], pv[u][3][0], pv[u][3][1]), pv[u][3][1]);
    }
    red2_g<NU>(mx, nm, [](float a, float b) { return max2(a, b); });
    f32x2 pp[NU][4], pm[NU][4]; float sp[NU], sm[NU];
#pragma unroll
    for (int u = 0; u < NU; ++u) {
        const float cp = -mx[u] * L2E, cm = -nm[u] * L2E;
#pragma unroll
        for (int i = 0; i < 4; ++i) {
            const f32x2 a = pv[u][i] * L2E + cp, b = pv[u][i] * (-L2E) + cm;
            pp[u][i] = (f32x2){__builtin_amdgcn_exp2f(a[0]), __builtin_amdgcn_exp2f(a[1])};
            pm[u][i] = (f32x2){__builtin_amdgcn_exp2f(b[0]), __builtin_amdgcn_exp2f(b[1])};
        }
        const f32x2 s2 = (pp[u][0] + pp[u][1]) + (pp[u][2] + pp[u][3]), m2 = (pm[u][0] + pm[u][1]) + (pm[u][2] + pm[u][3]);
        sp[u] = s2[0] + s2[1]; sm[u] = m2[0] + m2[1];
    }
    red2_g<NU>(sp, sm, [](float a, float b) { return a + b; });
#pragma unroll
    for (int u = 0; u < NU; ++u) {
        const float rp = __builtin_amdgcn_rcpf(sp[u]), rm = __builtin_amdgcn_rcpf(sm[u]);
#pragma unroll
        for (int i = 0; i < 4; ++i) { const f32x2 a = pp[u][i] * rp, b = pm[u][i] * rm; P[u][i] = pk_bf16(a[0], a[1]); M[u][i] = pk_bf16(b[0], b[1]); }
    }
}

__device__ __forceinline__ GAS u64* flagp(unsigned char* ws, unsigned i) { return (GAS u64*)(ws + WS_FLG + (size_t)i * 64); }
__device__ __forceinline__ void flag_set(unsigned char* ws, unsigned i, u64 tag) { __hip_atomic_store(flagp(ws, i), tag, __ATOMIC_RELAXED, __HIP_MEMORY_SCOPE_AGENT); }
__device__ __forceinline__ void flag_wait(unsigned char* ws, unsigned i0, unsigned st, int n, u64 tag, int lane) {
    GAS u64* p = flagp(ws, i0 + st * (unsigned)(lane < n ? lane : 0));
#pragma unroll 1
    for (unsigned it = 0; it < (1u << 21); ++it) {
        const u64 v = __hip_atomic_load(p, __ATOMIC_RELAXED, __HIP_MEMORY_SCOPE_AGENT);
        if (__all(v == tag)) break;
        __builtin_amdgcn_s_sleep(1);
    }
    asm volatile("" ::: "memory");
}
__device__ __forceinline__ __amdgpu_buffer_rsrc_t ws_rsrc(unsigned char* ws) { return __builtin_amdgcn_make_buffer_rsrc((void*)ws, 0, WS_END, 0x00020000); }
#define ST_SC1(v, rs, off) __builtin_amdgcn_raw_buffer_store_b128(v, rs, off, 0, 16)
#define LD_SC1(rs, off) __builtin_amdgcn_raw_buffer_load_b128(rs, off, 0, 16)

template <int GRP>
__device__ __forceinline__ void g1_mainloop(const unsigned ldsb, lds_t lds, f32x4 (&acc)[8][5], cptr (&bp)[5], const size_t ld4, cptr xn, const int wl, const int lane, const int (&ba)[5]) {
    const size_t ldq = (size_t)32 * 3072 * 2, ldg = (size_t)32 * F_ * 2;
    const int aoff = GRP * 8192 + lane * 16;
    const unsigned avoff = wl * 4096 + lane * 16;
    int so = 0, sl = 3 * G1_STAGE;
    cptr an = xn + 3 * 16384;
    bf16x8 A[8], B[5];
#pragma unroll 1
    for (int t = 0; t < 32; ++t) {
        const bool more = t < 29;
        const unsigned lb = ldsb + sl + G1_SA + wl * 1024, la = ldsb + sl + wl * 4096, rb = ldsb + so;
        if (more) {
            if (GRP == 0) {
                B[0] = lds_tr2_asm<0>(rb + ba[0]); B[1] = lds_tr2_asm<0>(rb + ba[1]);
                glds_v(bp[0], lb);
                B[2] = lds_tr2_asm<0>(rb + ba[2]); B[3] = lds_tr2_asm<0>(rb + ba[3]);
                glds_v(bp[1], lb + 4096);
                B[4] = lds_tr2_asm<0>(rb + ba[4]);
                A[0] = lds_b128(lds + so + aoff); A[1] = lds_b128(lds + so + aoff + 1024);
                glds_v(bp[2], lb + 8192);
                A[2] = lds_b128(lds + so + aoff + 2048); A[3] = lds_b128(lds + so + aoff + 3072); A[4] = lds_b128(lds + so + aoff + 4096);
                glds_v(bp[3], lb + 12288);
                A[5] = lds_b128(lds + so + aoff + 5120); A[6] = lds_b128(lds + so + aoff + 6144); A[7] = lds_b128(lds + so + aoff + 7168);
                glds_v(bp[4], lb + 16384);
                bp[0] += ldq; bp[1] += ldg; bp[2] += ldg; bp[3] += ldg; bp[4] += ld4;
                WAIT_L0(); WAIT_V(10);
            } else {
                B[0] = lds_tr2_asm<0>(rb + ba[0]); B[1] = lds_tr2_asm<0>(rb + ba[1]);
                glds_s_sc1_first<0>(avoff, an, la);
                B[2] = lds_tr2_asm<0>(rb + ba[2]); B[3] = lds_tr2_asm<0>(rb + ba[3]);
                glds_s_sc1_next<1024>(avoff, an);
                B[4] = lds_tr2_asm<0>(rb + ba[4]);
                A[0] = lds_b128(lds + so + aoff); A[1] = lds_b128(lds + so + aoff + 1024); A[2] = lds_b128(lds + so + aoff + 2048);
                glds_s_sc1_next<2048>(avoff, an);
                A[3] = lds_b128(lds + so + aoff + 3072); A[4] = lds_b128(lds + so + aoff + 4096); A[5] = lds_b128(lds + so + aoff + 5120);
                glds_s_sc1_next<3072>(avoff, an);
                A[6] = lds_b128(lds + so + aoff + 6144); A[7] = lds_b128(lds + so + aoff + 7168);
                an += 16384;
                WAIT_L0(); WAIT_V(8);
            }
        } else {
#pragma unroll
            for (int n = 0; n < 5; ++n) B[n] = lds_tr2_asm<0>(rb + ba[n]);
#pragma unroll
            for (int m = 0; m < 8; ++m) A[m] = lds_b128(lds + so + aoff + m * 1024);
            WAIT_L0(); WAIT_V(0);
        }
        so = (so == 3 * G1_STAGE) ? 0 : so + G1_STAGE;
        sl = (sl == 3 * G1_STAGE) ? 0 : sl + G1_STAGE;
        SCHED(); BAR(); SCHED();
        __builtin_amdgcn_s_setprio(1);
#pragma unroll
        for (int m = 0; m < 8; ++m)
#pragma unroll
            for (int n = 0; n < 5; ++n) acc[m][n] = MFMA(B[n], A[m], acc[m][n]);
        __builtin_amdgcn_s_setprio(0);
        SCHED(); BAR(); SCHED();
    }
}

__device__ __forceinline__ void g2_issueB(unsigned bvoff, cptr sb, unsigned l) {
    glds_s(bvoff, sb, l);
    glds_s_o128(bvoff, sb, l + 4096);
}

__global__ __launch_bounds__(512, 2) void k_fused(const u16* __restrict__ x, const u16* __restrict__ wqkv, const u16* __restrict__ wphi, const u16* __restrict__ wo,
                                                  const u16* __restrict__ wg, const u16* __restrict__ wu, const u16* __restrict__ wd,
                                                  unsigned char* __restrict__ ws, u16* __restrict__ out) {
    extern __shared__ __attribute__((aligned(16))) unsigned char shm[];
    lds_t lds = (lds_t)shm;
    const unsigned ldsb = (unsigned)(size_t)lds;
    const int wid = __builtin_amdgcn_readfirstlane(threadIdx.x >> 6);
    const int grp = wid >> 2, wc = wid & 3;
    const int x8 = blockIdx.x & 7, r = blockIdx.x >> 3, ks = x8 >> 1, panel = 4 * (x8 & 1) + (r >> 3), c8 = r & 7, j = 4 * ks + (c8 >> 1), kvt = c8 & 1;
    const __amdgpu_buffer_rsrc_t rs = ws_rsrc(ws);
    STAMP_DECL; STAMP(0);
    u64 tag;
    f32x4 acc[8][5];

    {
        const int lane = threadIdx.x & 63, g = lane >> 4;
        const int row = panel * 256 + (ks * 8 + c8) * 8 + (lane & 7);
        u32x4 xa = {0, 0, 0, 0}, xb = {0, 0, 0, 0}; u32x2 ev = {0, 0};
        {
            const unsigned xo = (unsigned)(row * D_ + (wid * 8 + (lane >> 3)) * 8) * 2u, zero = 0;
            asm volatile("global_load_dwordx4 %0, %1, %2" : "+v"(xa) : "v"(xo), "s"(x) : "memory");
            asm volatile("global_load_dwordx4 %0, %1, %2 offset:1024" : "+v"(xb) : "v"(xo), "s"(x) : "memory");
            asm volatile("global_load_dwordx2 %0, %1, %2 sc1" : "+v"(ev) : "v"(zero), "s"(ws + WS_FLG + FL_EPOCH * 64) : "memory");
        }
        cptr bp[5];
        const size_t ldq = (size_t)32 * 3072 * 2, ldg = (size_t)32 * F_ * 2, ld4 = kvt ? ldq : ldg;
        if (grp == 0) {
            const int gq = (j & 1) ? 7 * j + 3 : 7 * j, gp = (j & 1) ? 7 * j + 1 : 7 * j + 4, gh = (j & 1) ? 7 * j : 7 * j + 6;
            const int krow = 8 * wc + (lane >> 3), chunk = (lane & 7) ^ swzB(krow);
            const size_t rq = (size_t)krow * 3072 * 2 + chunk * 16, rg = (size_t)krow * F_ * 2 + chunk * 16;
            if (!kvt) {
                bp[0] = (cptr)(wqkv + 64 * j) + rq;
                bp[1] = (cptr)(wg + 32 * gq) + rg;      bp[2] = (cptr)(wg + 32 * gq + 64) + rg;
                bp[3] = (cptr)(wu + 32 * gq) + rg;      bp[4] = (cptr)(wu + 32 * gq + 64) + rg;
            } else {
                bp[0] = (cptr)(wqkv + 1024 + 64 * j) + rq;
                bp[1] = (cptr)(wg + 32 * gp) + rg;
                bp[2] = (chunk < 4 ? (cptr)(wg + 32 * gh) : (cptr)(wu + 32 * gh) - 64) + rg;
                bp[3] = (cptr)(wu + 32 * gp) + rg;
                bp[4] = (cptr)(wqkv + 2048 + 64 * j) + rq;
            }
#pragma unroll
            for (int s = 0; s < 3; ++s) {
#pragma unroll
                for (int c = 0; c < 5; ++c) glds_v(bp[c], ldsb + s * G1_STAGE + G1_SA + c * 4096 + wc * 1024);
                bp[0] += ldq; bp[1] += ldg; bp[2] += ldg; bp[3] += ldg; bp[4] += ld4;
            }
            glds_v((cptr)(wphi + (size_t)(2 * j) * 1024 + wc * 512 + lane * 8), ldsb + L_WPHI + wc * 1024);
        } else {
#pragma unroll
            for (int c = 0; c < 5; ++c) bp[c] = nullptr;
        }
        asm volatile("s_cmp_eq_u32 %3, 0\n\ts_cbranch_scc1 .Lxw0_%=\n\ts_waitcnt vmcnt(0)\n\ts_branch .Lxw1_%=\n.Lxw0_%=:\n\ts_waitcnt vmcnt(16)\n.Lxw1_%=:"
                     : "+v"(xa), "+v"(xb), "+v"(ev) : "s"(grp) : "scc", "memory");
        {
            u64 z = (((u64)(unsigned)__builtin_amdgcn_readfirstlane(ev[1]) << 32) | (unsigned)__builtin_amdgcn_readfirstlane(ev[0])) + 0x9E3779B97F4A7C15ull;
            z = (z ^ (z >> 30)) * 0xBF58476D1CE4E5B9ull; z = (z ^ (z >> 27)) * 0x94D049BB133111EBull; tag = z ^ (z >> 31);
        }
        STAMP(1);
        float f[16]; float s = 0.f;
#pragma unroll
        for (int i = 0; i < 4; ++i) { f[2 * i] = lo_f(xa[i]); f[2 * i + 1] = hi_f(xa[i]); f[8 + 2 * i] = lo_f(xb[i]); f[8 + 2 * i + 1] = hi_f(xb[i]); }
#pragma unroll
        for (int i = 0; i < 16; ++i) s += f[i];
        s += ror8(s); s = sum_g(s);
        LAS float* red = (LAS float*)(lds + L_MISC);
        if (lane < 8) red[wid * 8 + lane] = s;
        LBAR();
        float mean = 0.f;
#pragma unroll
        for (int w = 0; w < 8; ++w) mean += red[w * 8 + (lane & 7)];
        mean *= (1.0f / D_);
        float q = 0.f;
#pragma unroll
        for (int i = 0; i < 16; ++i) { const float d = f[i] - mean; q += d * d; }
        q += ror8(q); q = sum_g(q);
        if (lane < 8) red[64 + wid * 8 + lane] = q;
        LBAR();
        float var = 0.f;
#pragma unroll
        for (int w = 0; w < 8; ++w) var += red[64 + w * 8 + (lane & 7)];
        var *= (1.0f / D_);
        const float m = rbf(mean), sd = rbf(sqrtf(rbf(rbf(var) + 1e-5f))), rsd = 1.0f / sd;
#pragma unroll
        for (int it = 0; it < 2; ++it) {
            const int p = it * 64 + wid * 8 + (lane >> 3), kc = p >> 2, kq = p & 3;
            u32x4 o;
#pragma unroll
            for (int i = 0; i < 4; ++i) {
                const unsigned w = pk_bf16(f[it * 8 + 2 * i] - m, f[it * 8 + 2 * i + 1] - m);
                const f32x2 a = {lo_f(w), hi_f(w)}, q0 = a * rsd;
                const f32x2 q1 = __builtin_elementwise_fma(__builtin_elementwise_fma(-q0, (f32x2){sd, sd}, a), (f32x2){rsd, rsd}, q0);
                o[i] = pk_bf16(q1[0], q1[1]);
            }
            ST_SC1(o, rs, WS_XN + (unsigned)(((panel * 32 + kc) * 16 + ((row >> 4) & 15)) * 1024 + (kq * 16 + (row & 15)) * 16));
        }
        WAIT_V(0);
        LBAR();
        STAMP(2);
        if (threadIdx.x == 0) flag_set(ws, FL_LN + panel * 32 + ks * 8 + c8, tag);
        if (wid == 0) flag_wait(ws, FL_LN + panel * 32, 1, 32, tag, lane);
        LBAR();
        STAMP(3);

        cptr xn = uni((cptr)ws + WS_XN + (size_t)panel * 32 * 16384);
        if (grp == 1) {
            const unsigned avoff = wc * 4096 + lane * 16;
#pragma unroll
            for (int s3 = 0; s3 < 3; ++s3) {
                const unsigned la = ldsb + s3 * G1_STAGE + wc * 4096;
                glds_s_sc1_first<0>(avoff, xn + s3 * 16384, la); glds_s_sc1_next<1024>(avoff, xn + s3 * 16384);
                glds_s_sc1_next<2048>(avoff, xn + s3 * 16384);   glds_s_sc1_next<3072>(avoff, xn + s3 * 16384);
            }
            WAIT_V(8);
        }
        int ba[5];
        {
            int cgA, tlA, cgB, tlB;
            if (!kvt) { cgA = 1 + (wc >> 1); tlA = 2 * (wc & 1); cgB = 3 + (wc >> 1); tlB = tlA; }
            else if (wc < 2) { cgA = 1; tlA = 2 * wc; cgB = 3; tlB = 2 * wc; }
            else if (wc == 2) { cgA = 2; tlA = 0; cgB = 2; tlB = 2; }
            else { cgA = 4; tlA = 0; cgB = 4; tlB = 2; }
            const int q4 = (lane >> 2) & 3, p = lane & 3, krow = 8 * g + q4, sw = swzB(krow);
            auto adr = [&](int cg, int tl) { return G1_SA + cg * 4096 + krow * 128 + (((2 * tl + (p >> 1)) ^ sw) << 4) + (p & 1) * 8; };
            ba[0] = adr(cgA, tlA); ba[1] = adr(cgA, tlA + 1); ba[2] = adr(cgB, tlB); ba[3] = adr(cgB, tlB + 1); ba[4] = adr(0, wc);
        }
#pragma unroll
        for (int m = 0; m < 8; ++m)
#pragma unroll
            for (int n = 0; n < 5; ++n) acc[m][n] = (f32x4){0.f, 0.f, 0.f, 0.f};
        BAR();
        STAMP(4);
        if (grp == 0) { g1_mainloop<0>(ldsb, lds, acc, bp, ld4, xn, wc, lane, ba); BAR(); }
        else          { BAR(); g1_mainloop<1>(ldsb, lds, acc, bp, ld4, xn, wc, lane, ba); }
        STAMP(5);
    }

    int tz = threadIdx.x; asm volatile("" : "+v"(tz));
    const int lane = tz & 63, g = lane >> 4, l15 = lane & 15;
    const unsigned bvoff = (unsigned)((8 * wc + (lane >> 3)) * 2048 + c8 * 256 + (((lane & 7) ^ swzB(8 * wc + (lane >> 3))) << 4));
    const unsigned bl = ldsb + G2_SB + grp * 24576 + wc * 1024;
    cptr sbp = uni((cptr)wd + ((size_t)(28 * ks + grp) << 16));
    auto prefetchB = [&]() { g2_issueB(bvoff, sbp, bl); g2_issueB(bvoff, sbp + 131072, bl + 8192); sbp += 262144; };
    {
        const int gq = (j & 1) ? 7 * j + 3 : 7 * j, gp = (j & 1) ? 7 * j + 1 : 7 * j + 4, gh = (j & 1) ? 7 * j : 7 * j + 6;
        {
            lds_t kp = lds + E_KP + (128 * grp + l15) * 256 + ((((wc >> 1) * 4 + (wc & 1) * 2 + (g >> 1)) ^ l15) << 4) + (g & 1) * 8;
#pragma unroll
            for (int m = 0; m < 8; ++m) *(LAS u32x2*)(kp + m * 4096) = (u32x2){pk_bf16(acc[m][4][0], acc[m][4][1]), pk_bf16(acc[m][4][2], acc[m][4][3])};
        }
        const bool has_h = !(kvt && wc == 3);
        const int hkc = !kvt ? gq + wc : (wc < 2 ? gp + wc : gh);
        const unsigned hoff = WS_H + (unsigned)(((panel * NKC2 + hkc) * 16 + 8 * grp) * 1024 + frag_off(lane));
        auto geglu_c = [&](int m) {
            const f32x2 a0 = gelu_mul2(acc[m][0].lo, acc[m][2].lo), a1 = gelu_mul2(acc[m][0].hi, acc[m][2].hi);
            const f32x2 b0 = gelu_mul2(acc[m][1].lo, acc[m][3].lo), b1 = gelu_mul2(acc[m][1].hi, acc[m][3].hi);
            return pack_frag(a0, a1, b0, b1);
        };
        auto geglu = [&](int m) { ST_SC1(geglu_c(m), rs, hoff + m * 1024); SCHED(); };
        const int q4 = (lane >> 2) & 3, p4 = lane & 3;
        auto logits = [&](u32x4 (&lg)[4]) {
            lds_t wp = lds + L_WPHI + (8 * g + q4) * 64 + p4 * 8;
            lds_t qp = lds + E_KP + (32 * wid + l15) * 256;
#pragma unroll
            for (int hh = 0; hh < 2; ++hh) {
                const bf16x8 wf0 = lds_tr2(wp + hh * 2048, wp + hh * 2048 + 256), wf1 = lds_tr2(wp + hh * 2048 + 32, wp + hh * 2048 + 288);
#pragma unroll
                for (int tb = 0; tb < 2; ++tb) {
                    const bf16x8 qf = lds_b128(qp + tb * 4096 + (((hh * 4 + g) ^ l15) << 4));
                    const f32x4 z4 = {0.f, 0.f, 0.f, 0.f};
                    const f32x4 p0 = MFMA(wf0, qf, z4), p1 = MFMA(wf1, qf, z4);
                    lg[hh * 2 + tb] = (u32x4){pk_bf16(p0[0], p0[1]), pk_bf16(p0[2], p0[3]), pk_bf16(p1[0], p1[1]), pk_bf16(p1[2], p1[3])};
                    asm volatile("" : "+v"(lg[hh * 2 + tb]));
                }
            }
        };
        if (kvt) {
            u32x4 h0 = {0, 0, 0, 0}, h1 = h0;
            if (wc == 3) {
                const int sw = swzP(l15);
                lds_t vp = lds + E_V + (128 * grp + l15) * 128 + (g & 1) * 8;
#pragma unroll
                for (int n = 0; n < 4; ++n) {
                    lds_t vn = vp + (((2 * n + (g >> 1)) ^ sw) << 4);
#pragma unroll
                    for (int m = 0; m < 8; ++m) *(LAS u32x2*)(vn + m * 2048) = (u32x2){pk_bf16(acc[m][n][0], acc[m][n][1]), pk_bf16(acc[m][n][2], acc[m][n][3])};
                }
            } else { h0 = geglu_c(0); h1 = geglu_c(1); }
            LBAR();
            STAMP(6);
            u32x4 lg[4], P[4], M[4];
            logits(lg);
            WAIT_L0();
            soft_pm<4>(lg, P, M);
            {
                const int sw = swzF(l15);
                lds_t pb = lds + E_KP + (32 * wid + l15) * 256 + (g & 1) * 8;
#pragma unroll
                for (int u = 0; u < 4; ++u) {
                    const int c0 = (u >> 1) * 8 + (g >> 1), tb = u & 1;
                    *(LAS u32x2*)(pb + tb * 4096 + (((c0 + 0) ^ sw) << 4)) = (u32x2){P[u][0], P[u][1]};
                    *(LAS u32x2*)(pb + tb * 4096 + (((c0 + 2) ^ sw) << 4)) = (u32x2){P[u][2], P[u][3]};
                    *(LAS u32x2*)(pb + tb * 4096 + (((c0 + 4) ^ sw) << 4)) = (u32x2){M[u][0], M[u][1]};
                    *(LAS u32x2*)(pb + tb * 4096 + (((c0 + 6) ^ sw) << 4)) = (u32x2){M[u][2], M[u][3]};
                }
            }
            LBAR();
            STAMP(7);
            {
                const int hh = wid >> 2, ft = wid & 3;
                f32x4 kv0 = {0.f, 0.f, 0.f, 0.f}, kv1 = kv0, zz = kv0;
                const bf16x8 ones = {0x3F80, 0x3F80, 0x3F80, 0x3F80, 0x3F80, 0x3F80, 0x3F80, 0x3F80};
                const int t0 = 8 * g + q4, t1 = t0 + 4, cp = hh * 8 + 2 * ft + (p4 >> 1), cv = hh * 4 + (p4 >> 1), hb8 = (p4 & 1) * 8;
                lds_t pf0 = lds + E_KP + t0 * 256 + ((cp ^ swzF(t0 & 15)) << 4) + hb8, pf1 = lds + E_KP + t1 * 256 + ((cp ^ swzF(t1 & 15)) << 4) + hb8;
                lds_t va0 = lds + E_V + t0 * 128 + ((cv ^ swzP(t0)) << 4) + hb8, va1 = lds + E_V + t1 * 128 + ((cv ^ swzP(t1)) << 4) + hb8;
                lds_t vb0 = lds + E_V + t0 * 128 + (((cv + 2) ^ swzP(t0)) << 4) + hb8, vb1 = lds + E_V + t1 * 128 + (((cv + 2) ^ swzP(t1)) << 4) + hb8;
#pragma unroll
                for (int k8 = 0; k8 < 8; ++k8) {
                    const bf16x8 pf = lds_tr2(pf0 + k8 * 8192, pf1 + k8 * 8192), v0 = lds_tr2(va0 + k8 * 4096, va1 + k8 * 4096), v1 = lds_tr2(vb0 + k8 * 4096, vb1 + k8 * 4096);
                    kv0 = MFMA(pf, v0, kv0); kv1 = MFMA(pf, v1, kv1); zz = MFMA(pf, ones, zz);
                }
                ST_SC1(((u32x4){pk_bf16(kv0[0], kv0[1]), pk_bf16(kv0[2], kv0[3]), pk_bf16(kv1[0], kv1[1]), pk_bf16(kv1[2], kv1[3])}), rs,
                       WS_PART + (unsigned)(((panel * 32 + 2 * j + hh) * 4 + ft) * 1024 + lane * 16));
                if (l15 == 0) ST_SC1(__builtin_bit_cast(u32x4, zz), rs, WS_PZ + (unsigned)(((panel * 32 + 2 * j + hh) * 64 + 16 * ft + 4 * g) * 4));
            }
            SCHED();
            u32x4 h2 = {0, 0, 0, 0};
            if (has_h) h2 = geglu_c(2);
            WAIT_V(0);
            LBAR();
            if (threadIdx.x == 0) flag_set(ws, FL_KV + j * 8 + panel, tag);
            STAMP(8);
            prefetchB();
            if (has_h) {
                ST_SC1(h0, rs, hoff); ST_SC1(h1, rs, hoff + 1024); ST_SC1(h2, rs, hoff + 2048); SCHED();
                geglu(3); geglu(4); geglu(5); geglu(6); geglu(7);
            }
            WAIT_V(0);
            LBAR();
            if (threadIdx.x == 0) flag_set(ws, FL_H + (panel * 4 + ks) * 8 + c8, tag);
            STAMP(9);
        } else {
            u32x4 lg[4], P[4], M[4];
#pragma unroll
            for (int m = 0; m < 8; ++m) geglu(m);
            prefetchB();
            LBAR();
            STAMP(6);
            logits(lg);
            soft_pm<4>(lg, P, M);
#pragma unroll
            for (int u = 0; u < 4; ++u) asm volatile("" : "+v"(P[u]), "+v"(M[u]));
            WAIT_V(4);
            LBAR();
            if (threadIdx.x == 0) flag_set(ws, FL_H + (panel * 4 + ks) * 8 + c8, tag);
            STAMP(7);
            if (wid == 0) flag_wait(ws, FL_KV + j * 8, 1, 8, tag, lane);
            LBAR();
            STAMP(8);
            {
                const int hh = wid >> 2, ft = wid & 3;
                f32x4 a0, a1, az;
                u32x4 pw[8]; f32x4 pz[8];
#pragma unroll
                for (int p = 0; p < 8; ++p) {
                    pw[p] = LD_SC1(rs, WS_PART + (unsigned)(((p * 32 + 2 * j + hh) * 4 + ft) * 1024 + lane * 16));
                    pz[p] = __builtin_bit_cast(f32x4, LD_SC1(rs, WS_PZ + (unsigned)(((p * 32 + 2 * j + hh) * 64 + 16 * ft + 4 * g) * 4)));
                }
#pragma unroll
                for (int p = 0; p < 8; ++p) {
                    const f32x4 b0 = {lo_f(pw[p][0]), hi_f(pw[p][0]), lo_f(pw[p][1]), hi_f(pw[p][1])}, b1 = {lo_f(pw[p][2]), hi_f(pw[p][2]), lo_f(pw[p][3]), hi_f(pw[p][3])};
                    if (p == 0) { a0 = b0; a1 = b1; az = pz[0]; } else { a0 += b0; a1 += b1; az += pz[p]; }
                }
                lds_t kb = lds + E_V + ((hh * 2 + (ft >> 1)) * 3) * 1024 + lane * 16 + (ft & 1) * 8;
                *(LAS u32x2*)(kb) = (u32x2){pk_bf16(a0[0], a0[1]), pk_bf16(a0[2], a0[3])};
                *(LAS u32x2*)(kb + 1024) = (u32x2){pk_bf16(a1[0], a1[1]), pk_bf16(a1[2], a1[3])};
                *(LAS u32x2*)(kb + 2048) = (u32x2){pk_bf16(az[0], az[1]), pk_bf16(az[2], az[3])};
            }
            LBAR();
            STAMP(9);
#pragma unroll
            for (int hh = 0; hh < 2; ++hh) {
                bf16x8 kf[2][3];
#pragma unroll
                for (int s = 0; s < 2; ++s)
#pragma unroll
                    for (int d = 0; d < 3; ++d) kf[s][d] = lds_b128(lds + E_V + ((hh * 2 + s) * 3 + d) * 1024 + lane * 16);
#pragma unroll
                for (int tb = 0; tb < 2; ++tb) {
                    const bf16x8 Pf = __builtin_bit_cast(bf16x8, P[hh * 2 + tb]), Mf = __builtin_bit_cast(bf16x8, M[hh * 2 + tb]);
                    const f32x4 z4 = {0.f, 0.f, 0.f, 0.f};
                    const f32x4 n0 = MFMA(kf[1][0], Mf, MFMA(kf[0][0], Pf, z4));
                    const f32x4 n1 = MFMA(kf[1][1], Mf, MFMA(kf[0][1], Pf, z4));
                    const f32x4 dn = MFMA(kf[1][2], Mf, MFMA(kf[0][2], Pf, z4));
                    const float rden = __builtin_amdgcn_rcpf(rbf(rbf(dn[0]) + 1e-6f));
                    ST_SC1(pack_frag(n0.lo * rden, n0.hi * rden, n1.lo * rden, n1.hi * rden), rs, WS_H + (unsigned)(((panel * NKC2 + 112 + 2 * j + hh) * 16 + 2 * wid + tb) * 1024 + frag_off(lane)));
                }
            }
        }
    }
    STAMP(10);
    STAMP_OUT(0, 0, 11);

    STAMP(0);
    if (wid == 0) flag_wait(ws, FL_H + (panel * 4 + ks) * 8, 1, 8, tag, lane);
    LBAR();
    STAMP(1);
    f32x4 c2[8][4];
    {
        const int wm = wc >> 1, wn = wc & 1;
        cptr sa = uni((cptr)ws + WS_H + ((size_t)(panel * NKC2 + 28 * ks + grp) << 14));
        const unsigned avoff = (unsigned)(wc * 4096 + lane * 16), al = ldsb + grp * 49152 + wc * 4096;
        auto issueA = [&](unsigned slot_off) {
            glds_s_sc1_first<0>(avoff, sa, al + slot_off); glds_s_sc1_next<1024>(avoff, sa); glds_s_sc1_next<2048>(avoff, sa); glds_s_sc1_next<3072>(avoff, sa);
            sa += 32768;
        };
        issueA(0); issueA(16384);
        unsigned bb[4];
        {
            const int q4 = (lane >> 2) & 3, p = lane & 3, kr = 8 * g + q4, sw = swzB(kr);
#pragma unroll
            for (int tl = 0; tl < 4; ++tl) bb[tl] = ldsb + G2_SB + grp * 24576 + wn * 4096 + kr * 128 + (((2 * tl + (p >> 1)) ^ sw) << 4) + (p & 1) * 8;
        }
        lds_t ap = lds + grp * 49152 + wm * 8192 + lane * 16;
#pragma unroll
        for (int m = 0; m < 8; ++m)
#pragma unroll
            for (int n = 0; n < 4; ++n) c2[m][n] = (f32x4){0.f, 0.f, 0.f, 0.f};
        u32x2 ofl = {0, 0};
        const unsigned fo = (FL_O + (panel * 4 + ks) * 4 + (lane & 3)) * 64;
        WAIT_V(4);
        LBAR();
        if (!kvt && threadIdx.x == 0) flag_set(ws, FL_O + (panel * 4 + ks) * 4 + (c8 >> 1), tag);
        if (grp == 1) BAR();
        STAMP(2);
        auto step = [&](auto uc, const int t) {
            constexpr int U = decltype(uc)::value, W = (U + 2) % 3;
            bf16x8 A[8], B[4];
#define FST(x)
            FST(f0);
            if (U == 0) {
                if (t == 12) {
                    asm volatile("" : "+v"(ofl));
                    const u64 v = ((u64)ofl[1] << 32) | ofl[0];
                    if (!__all(v == tag)) flag_wait(ws, FL_O + (panel * 4 + ks) * 4, 1, 4, tag, lane);
                    sa = uni((cptr)ws + WS_H + ((size_t)(panel * NKC2 + 112 + 8 * ks + grp) << 14));
                    sbp = uni((cptr)wo + ((size_t)(8 * ks + grp) << 16));
                }
                asm volatile("s_cmp_lg_u32 %3, 9\n\ts_cbranch_scc1 .Lofl_%=\n\tglobal_load_dwordx2 %0, %1, %2 sc1\n.Lofl_%=:" : "+v"(ofl) : "v"(fo), "s"(ws + WS_FLG), "s"(t) : "scc", "memory");
            }
            const bool more = U == 0 || t < 16;
            B[0] = lds_tr2_asm<U * 8192>(bb[0]); B[1] = lds_tr2_asm<U * 8192>(bb[1]);
            if (more) glds_s(bvoff, sbp, bl + W * 8192);
            B[2] = lds_tr2_asm<U * 8192>(bb[2]); B[3] = lds_tr2_asm<U * 8192>(bb[3]);
            if (more) { glds_s_o128(bvoff, sbp, bl + W * 8192 + 4096); sbp += 131072; }
            A[0] = lds_b128(ap + U * 16384); A[1] = lds_b128(ap + U * 16384 + 1024);
            if (more) { glds_s_sc1_first<0>(avoff, sa, al + W * 16384); glds_s_sc1_next<1024>(avoff, sa); }
            A[2] = lds_b128(ap + U * 16384 + 2048); A[3] = lds_b128(ap + U * 16384 + 3072); A[4] = lds_b128(ap + U * 16384 + 4096);
            if (more) { glds_s_sc1_next<2048>(avoff, sa); glds_s_sc1_next<3072>(avoff, sa); sa += 32768; }
            A[5] = lds_b128(ap + U * 16384 + 5120); A[6] = lds_b128(ap + U * 16384 + 6144); A[7] = lds_b128(ap + U * 16384 + 7168);
            FST(f1);
            WAIT_L0();
            FST(f2);
            if (!more) WAIT_V(0);
            SCHED(); BAR(); SCHED();
            FST(f3);
            __builtin_amdgcn_s_setprio(1);
#pragma unroll
            for (int m = 0; m < 8; ++m)
#pragma unroll
                for (int n = 0; n < 4; ++n) c2[m][n] = MFMA(B[n], A[m], c2[m][n]);
            __builtin_amdgcn_s_setprio(0);
            FST(f4);
            WAIT_V(6);
            FST(f5);
            SCHED(); BAR(); SCHED();
        };
#pragma unroll 1
        for (int t = 0; t < 18; t += 3) { step(std::integral_constant<int, 0>{}, t); step(std::integral_constant<int, 1>{}, t + 1); step(std::integral_constant<int, 2>{}, t + 2); }
        if (grp == 0) BAR();
    }
    STAMP(3);
    f32x4 c3[4][4];
    {
        int tz3 = threadIdx.x; asm volatile("" : "+v"(tz3));
        lds_t mine = lds + wid * 16384 + (tz3 & 63) * 16, theirs = lds + (wid ^ 4) * 16384 + (tz3 & 63) * 16;
        if (grp == 0) {
#pragma unroll
            for (int m = 0; m < 4; ++m)
#pragma unroll
                for (int n = 0; n < 4; ++n) { *(LAS f32x4*)(mine + (m * 4 + n) * 1024) = c2[4 + m][n]; c3[m][n] = c2[m][n]; }
        } else {
#pragma unroll
            for (int m = 0; m < 4; ++m)
#pragma unroll
                for (int n = 0; n < 4; ++n) { *(LAS f32x4*)(mine + (m * 4 + n) * 1024) = c2[m][n]; c3[m][n] = c2[4 + m][n]; }
        }
        LBAR();
#pragma unroll
        for (int m = 0; m < 4; ++m)
#pragma unroll
            for (int n = 0; n < 4; ++n) c3[m][n] += *(const LAS f32x4*)(theirs + (m * 4 + n) * 1024);
    }

    {
        int tz2 = threadIdx.x; asm volatile("" : "+v"(tz2));
        const int ln = tz2 & 63, tile = panel * 8 + c8, qd = 2 * ((wid & 3) >> 1) + grp, wn = wid & 1;
        if (qd != ks) {
            const unsigned base = WS_XCH + (unsigned)((((tile * 4 + ks) * 8 + wid) * 8) * 1024 + ln * 16);
#pragma unroll
            for (int m = 0; m < 4; ++m)
#pragma unroll
                for (int n2 = 0; n2 < 2; ++n2) {
                    const f32x4 a = c3[m][2 * n2], b = c3[m][2 * n2 + 1];
                    ST_SC1(((u32x4){pk_bf16(a[0], a[1]), pk_bf16(a[2], a[3]), pk_bf16(b[0], b[1]), pk_bf16(b[2], b[3])}), rs, base + (m * 2 + n2) * 1024);
                }
            STAMP(4);
            WAIT_V(0);
            if (ln == 0) flag_set(ws, FL_X + (tile * 4 + ks) * 8 + wid, tag);
            STAMP(5);
        } else {
            const int row0 = panel * 256 + qd * 64 + (ln & 15), col0 = c8 * 128 + wn * 64 + 8 * frag_kq(ln);
            u32x4 xr[4][2];
#pragma unroll
            for (int m = 0; m < 4; ++m)
#pragma unroll
                for (int n2 = 0; n2 < 2; ++n2) xr[m][n2] = *(const u32x4*)(x + (size_t)(row0 + 16 * m) * D_ + col0 + 32 * n2);
            {
                const int src = (ks + 1 + (ln < 3 ? ln : 0)) & 3;
                GAS u64* p = flagp(ws, FL_X + (tile * 4 + src) * 8 + wid);
#pragma unroll 1
                for (unsigned it = 0; it < (1u << 21); ++it) {
                    const u64 v = __hip_atomic_load(p, __ATOMIC_RELAXED, __HIP_MEMORY_SCOPE_AGENT);
                    if (__all(v == tag)) break;
                    __builtin_amdgcn_s_sleep(1);
                }
                asm volatile("" ::: "memory");
            }
            STAMP(4);
#pragma unroll
            for (int s = 0; s < 4; ++s) {
                if (s == ks) continue;
                const unsigned base = WS_XCH + (unsigned)((((tile * 4 + s) * 8 + wid) * 8) * 1024 + ln * 16);
#pragma unroll
                for (int m = 0; m < 4; ++m)
#pragma unroll
                    for (int n2 = 0; n2 < 2; ++n2) {
                        const u32x4 w = LD_SC1(rs, base + (m * 2 + n2) * 1024);
                        c3[m][2 * n2] += (f32x4){lo_f(w[0]), hi_f(w[0]), lo_f(w[1]), hi_f(w[1])};
                        c3[m][2 * n2 + 1] += (f32x4){lo_f(w[2]), hi_f(w[2]), lo_f(w[3]), hi_f(w[3])};
                    }
            }
            STAMP(5);
#pragma unroll
            for (int m = 0; m < 4; ++m)
#pragma unroll
                for (int n2 = 0; n2 < 2; ++n2) {
                    float a[4], b[4];
#pragma unroll
                    for (int e = 0; e < 4; ++e) {
                        auto rr = __builtin_amdgcn_permlane16_swap(__float_as_uint(c3[m][2 * n2][e]), __float_as_uint(c3[m][2 * n2 + 1][e]), false, false);
                        a[e] = __uint_as_float(rr[0]); b[e] = __uint_as_float(rr[1]);
                    }
                    const u32x4 xv = xr[m][n2];
                    u32x4 o;
                    o[0] = pk_bf16(a[0] + lo_f(xv[0]), a[1] + hi_f(xv[0])); o[1] = pk_bf16(a[2] + lo_f(xv[1]), a[3] + hi_f(xv[1]));
                    o[2] = pk_bf16(b[0] + lo_f(xv[2]), b[1] + hi_f(xv[2])); o[3] = pk_bf16(b[2] + lo_f(xv[3]), b[3] + hi_f(xv[3]));
                    *(u32x4*)(out + (size_t)(row0 + 16 * m) * D_ + col0 + 32 * n2) = o;
                }
        }
        const int ln2 = ln;
        if (blockIdx.x == 0 && wid == 0) {
#pragma unroll 1
            for (int i = 0; i < 4; ++i) flag_wait(ws, FL_LN + 64 * i, 1, 64, tag, ln2);
            if (ln2 == 0) flag_set(ws, FL_EPOCH, tag);
        }
    }
    STAMP(6);
    STAMP_OUT(1, 0, 7);
}

extern "C" void kernel_launch(void* const* d_in, const int* in_sizes, int n_in, void* d_out, int out_size, void* d_ws, size_t ws_size, hipStream_t stream) {
    const u16* x = (const u16*)d_in[0]; const u16* wqkv = (const u16*)d_in[1]; const u16* wphi = (const u16*)d_in[2]; const u16* wo = (const u16*)d_in[3];
    const u16* wg = (const u16*)d_in[5]; const u16* wu = (const u16*)d_in[6]; const u16* wd = (const u16*)d_in[7];
    (void)hipFuncSetAttribute((const void*)k_fused, hipFuncAttributeMaxDynamicSharedMemorySize, L_TOTAL);
    k_fused<<<256, 512, L_TOTAL, stream>>>(x, wqkv, wphi, wo, wg, wu, wd, (unsigned char*)d_ws, (u16*)d_out);
}
```

```cpp
#include <hip/hip_runtime.h>
#include <type_traits>

typedef unsigned short u16;
typedef unsigned long long u64;
typedef short bf16x8 __attribute__((ext_vector_type(8)));
typedef short s16x4 __attribute__((ext_vector_type(4)));
typedef float f32x4 __attribute__((ext_vector_type(4)));
typedef float f32x2 __attribute__((ext_vector_type(2)));
typedef unsigned u32x4 __attribute__((ext_vector_type(4)));
typedef unsigned u32x2 __attribute__((ext_vector_type(2)));
#define LAS __attribute__((address_space(3)))
#define GAS __attribute__((address_space(1)))
typedef LAS unsigned char* lds_t;
typedef const unsigned char* cptr;

constexpr int S_ = 2048, D_ = 1024, F_ = 3584;
constexpr int NKC2 = 144;

constexpr unsigned WS_XN   = 0;
constexpr unsigned WS_H    = WS_XN + S_ * D_ * 2;
constexpr unsigned WS_PART = WS_H + 8 * NKC2 * 16 * 1024;
constexpr unsigned WS_PZ   = WS_PART + 8 * 32 * 4 * 1024;
constexpr unsigned WS_XCH  = WS_PZ + 8 * 32 * 64 * 4;
constexpr unsigned WS_FLG  = WS_XCH + 64 * 4 * 8 * 8 * 1024;
constexpr unsigned FL_LN = 0, FL_KV = FL_LN + 256, FL_H = FL_KV + 128, FL_O = FL_H + 256, FL_X = FL_O + 128, FL_EPOCH = FL_X + 2048, FL_N = FL_EPOCH + 1;
constexpr unsigned WS_END  = WS_FLG + FL_N * 64;

constexpr int G1_SA = 16384, G1_STAGE = 36864;
constexpr int L_MISC = 158720, L_WPHI = 159744, L_TOTAL = 163840;
constexpr int E_KP = 0, E_V = 65536;
constexpr int G2_SB = 98304;

__device__ __forceinline__ unsigned pk_bf16(float lo, float hi) {
    typedef __bf16 bf2 __attribute__((ext_vector_type(2)));
    bf2 r = {(__bf16)lo, (__bf16)hi};
    return __builtin_bit_cast(unsigned, r);
}
__device__ __forceinline__ float rbf(float x) { return __uint_as_float(pk_bf16(x, x) & 0xffff0000u); }
__device__ __forceinline__ float lo_f(unsigned w) { return __uint_as_float(w << 16); }
__device__ __forceinline__ float hi_f(unsigned w) { return __uint_as_float(w & 0xffff0000u); }

#define STAMP_DECL
#define STAMP(i)
#define STAMP_ARG
#define STAMP_PASS
#define STAMP_OUT(kern, lo, n)

#define WAIT_V(n) asm volatile("s_waitcnt vmcnt(" #n ")" ::: "memory")
#define WAIT_L0() do { asm volatile("" ::: "memory"); __builtin_amdgcn_s_waitcnt(0xC07F); asm volatile("" ::: "memory"); } while (0)
#define BAR() __builtin_amdgcn_s_barrier()
#define LBAR() asm volatile("s_waitcnt lgkmcnt(0)\n\ts_barrier" ::: "memory")
#define SCHED() __builtin_amdgcn_sched_barrier(0)
#define MFMA(a, b, c) __builtin_amdgcn_mfma_f32_16x16x32_bf16(a, b, c, 0, 0, 0)

__device__ __forceinline__ cptr uni(cptr p) {
    const u64 v = (u64)p;
    return (cptr)(((u64)(unsigned)__builtin_amdgcn_readfirstlane((unsigned)(v >> 32)) << 32) | (unsigned)__builtin_amdgcn_readfirstlane((unsigned)v));
}
__device__ __forceinline__ void glds_v(cptr g, unsigned l) {
    asm volatile("s_mov_b32 m0, %1\n\ts_nop 0\n\tglobal_load_lds_dwordx4 %0, off" ::"v"(g), "s"(l) : "memory");
}
__device__ __forceinline__ void glds_s(unsigned voff, cptr sb, unsigned l) {
    asm volatile("s_nop 2\n\ts_mov_b32 m0, %2\n\ts_nop 0\n\tglobal_load_lds_dwordx4 %0, %1" ::"v"(voff), "s"(sb), "s"(l) : "memory");
}
__device__ __forceinline__ void glds_s_o128(unsigned voff, cptr sb, unsigned l) {
    asm volatile("s_mov_b32 m0, %2\n\ts_nop 0\n\tglobal_load_lds_dwordx4 %0, %1 offset:128" ::"v"(voff), "s"(sb), "s"(l - 128u) : "memory");
}
template <int OFF> __device__ __forceinline__ void glds_s_sc1_first(unsigned voff, cptr sb, unsigned l) {
    asm volatile("s_nop 2\n\ts_mov_b32 m0, %2\n\ts_nop 0\n\tglobal_load_lds_dwordx4 %0, %1 offset:%3 sc1" ::"v"(voff), "s"(sb), "s"(l), "i"(OFF) : "memory");
}
template <int OFF> __device__ __forceinline__ void glds_s_sc1_next(unsigned voff, cptr sb) {
    asm volatile("global_load_lds_dwordx4 %0, %1 offset:%2 sc1" ::"v"(voff), "s"(sb), "i"(OFF) : "memory");
}
template <int OFF> __device__ __forceinline__ void gld_sc1(u32x4& d, unsigned voff, cptr sb) {
    asm volatile("global_load_dwordx4 %0, %1, %2 offset:%3 sc1" : "+v"(d) : "v"(voff), "s"(sb), "i"(OFF) : "memory");
}
__device__ __forceinline__ bf16x8 lds_b128(lds_t p) { return *(const LAS bf16x8*)p; }
__device__ __forceinline__ bf16x8 lds_tr2(lds_t p0, lds_t p1) {
    s16x4 a = __builtin_amdgcn_ds_read_tr16_b64_v4i16((LAS s16x4*)p0);
    s16x4 b = __builtin_amdgcn_ds_read_tr16_b64_v4i16((LAS s16x4*)p1);
    return __builtin_shufflevector(a, b, 0, 1, 2, 3, 4, 5, 6, 7);
}
template <int OFF> __device__ __forceinline__ bf16x8 lds_tr2_asm(unsigned addr) {
    s16x4 a, b;
    asm volatile("ds_read_b64_tr_b16 %0, %2 offset:%3\n\tds_read_b64_tr_b16 %1, %2 offset:%4" : "=&v"(a), "=&v"(b) : "v"(addr), "i"(OFF), "i"(OFF + 512) : "memory");
    return __builtin_shufflevector(a, b, 0, 1, 2, 3, 4, 5, 6, 7);
}

__device__ __forceinline__ float x16(float v, float& o) { auto r = __builtin_amdgcn_permlane16_swap(__float_as_uint(v), __float_as_uint(v), false, false); o = __uint_as_float(r[1]); return __uint_as_float(r[0]); }
__device__ __forceinline__ float x32(float v, float& o) { auto r = __builtin_amdgcn_permlane32_swap(__float_as_uint(v), __float_as_uint(v), false, false); o = __uint_as_float(r[1]); return __uint_as_float(r[0]); }
__device__ __forceinline__ float sum_g(float v) { float o, a = x16(v, o); v = a + o; a = x32(v, o); return a + o; }
__device__ __forceinline__ float max_g(float v) { float o, a = x16(v, o); v = fmaxf(a, o); a = x32(v, o); return fmaxf(a, o); }
__device__ __forceinline__ float min_g(float v) { float o, a = x16(v, o); v = fminf(a, o); a = x32(v, o); return fminf(a, o); }
__device__ __forceinline__ float ror8(float v) { return __int_as_float(__builtin_amdgcn_update_dpp(0, __float_as_int(v), 0x128, 0xf, 0xf, false)); }

__device__ __forceinline__ int swzB(int k) { return (((k >> 1) & 1) << 1) | (((k >> 3) & 1) << 2); }
__device__ __forceinline__ int swzP(int k) { return ((k >> 2) & 1) | (((k >> 1) & 1) << 1) | (((k >> 3) & 1) << 2); }
__device__ __forceinline__ int swzF(int k) { return ((k >> 2) & 1) | ((k & 3) << 1) | (((k >> 3) & 1) << 3); }

__device__ __forceinline__ f32x2 gelu_mul2(f32x2 g, f32x2 u) {
    constexpr float C0 = -2.0f * 0.7978845608028654f * 1.4426950408889634f, C1 = C0 * 0.044715f;
    const f32x2 a = g * (g * g * C1 + C0);
    const f32x2 d = (f32x2){__builtin_amdgcn_exp2f(a[0]), __builtin_amdgcn_exp2f(a[1])} + 1.0f;
    return (g * u) * (f32x2){__builtin_amdgcn_rcpf(d[0]), __builtin_amdgcn_rcpf(d[1])};
}
__device__ __forceinline__ u32x4 pack_frag(const f32x2 a0, const f32x2 a1, const f32x2 b0, const f32x2 b1) {
    unsigned x0 = pk_bf16(a0[0], a0[1]), x1 = pk_bf16(a1[0], a1[1]), y0 = pk_bf16(b0[0], b0[1]), y1 = pk_bf16(b1[0], b1[1]);
    auto r0 = __builtin_amdgcn_permlane16_swap(x0, y0, false, false);
    auto r1 = __builtin_amdgcn_permlane16_swap(x1, y1, false, false);
    u32x4 o; o[0] = r0[0]; o[1] = r1[0]; o[2] = r0[1]; o[3] = r1[1];
    return o;
}
__device__ __forceinline__ int frag_kq(int lane) { const int g = lane >> 4; return ((g & 1) << 1) | (g >> 1); }
__device__ __forceinline__ int frag_off(int lane) { return (frag_kq(lane) * 16 + (lane & 15)) * 16; }

__device__ __forceinline__ float max3(float a, float b, float c) { float r; asm("v_max3_f32 %0, %1, %2, %3" : "=v"(r) : "v"(a), "v"(b), "v"(c)); return r; }
__device__ __forceinline__ float min3(float a, float b, float c) { float r; asm("v_min3_f32 %0, %1, %2, %3" : "=v"(r) : "v"(a), "v"(b), "v"(c)); return r; }
__device__ __forceinline__ float max2(float a, float b) { float r; asm("v_max_f32 %0, %1, %2" : "=v"(r) : "v"(a), "v"(b)); return r; }
template <int NU, class Op> __device__ __forceinline__ void red2_g(float (&a)[NU], float (&b)[NU], Op op) {
    float z[NU], y[NU];
#pragma unroll
    for (int u = 0; u < NU; ++u) {
        auto r = __builtin_amdgcn_permlane16_swap(__float_as_uint(a[u]), __float_as_uint(b[u]), false, false);
        z[u] = op(__uint_as_float(r[0]), __uint_as_float(r[1]));
    }
#pragma unroll
    for (int u = 0; u < NU; ++u) {
        auto q = __builtin_amdgcn_permlane32_swap(__float_as_uint(z[u]), __float_as_uint(z[u]), false, false);
        y[u] = op(__uint_as_float(q[0]), __uint_as_float(q[1]));
    }
#pragma unroll
    for (int u = 0; u < NU; ++u) {
        auto w = __builtin_amdgcn_permlane16_swap(__float_as_uint(y[u]), __float_as_uint(y[u]), false, false);
        a[u] = __uint_as_float(w[0]); b[u] = __uint_as_float(w[1]);
    }
}
template <int NU> __device__ __forceinline__ void soft_pm(const u32x4 (&w)[NU], u32x4 (&P)[NU], u32x4 (&M)[NU]) {
    constexpr float L2E = 1.4426950408889634f;
    f32x2 pv[NU][4]; float mx[NU], nm[NU];
#pragma unroll
    for (int u = 0; u < NU; ++u) {
#pragma unroll
        for (int i = 0; i < 4; ++i) pv[u][i] = (f32x2){lo_f(w[u][i]), hi_f(w[u][i])};
        mx[u] = max3(max3(max3(pv[u][0][0], pv[u][0][1], pv[u][1][0]), pv[u][1][1], pv[u][2][0]), max2(pv[u][2][1], pv[u][3][0]), pv[u][3][1]);
        nm[u] = -min3(min3(min3(pv[u][0][0], pv[u][0][1], pv[u][1][0]), pv[u][1][1], pv[u][2][0]), min3(pv[u][2][1], pv[u][3][0], pv[u][3][1]), pv[u][3][1]);
    }
    red2_g<NU>(mx, nm, [](float a, float b) { return max2(a, b); });
    f32x2 pp[NU][4], pm[NU][4]; float sp[NU], sm[NU];
#pragma unroll
    for (int u = 0; u < NU; ++u) {
        const float cp = -mx[u] * L2E, cm = -nm[u] * L2E;
#pragma unroll
        for (int i = 0; i < 4; ++i) {
            const f32x2 a = pv[u][i] * L2E + cp, b = pv[u][i] * (-L2E) + cm;
            pp[u][i] = (f32x2){__builtin_amdgcn_exp2f(a[0]), __builtin_amdgcn_exp2f(a[1])};
            pm[u][i] = (f32x2){__builtin_amdgcn_exp2f(b[0]), __builtin_amdgcn_exp2f(b[1])};
        }
        const f32x2 s2 = (pp[u][0] + pp[u][1]) + (pp[u][2] + pp[u][3]), m2 = (pm[u][0] + pm[u][1]) + (pm[u][2] + pm[u][3]);
        sp[u] = s2[0] + s2[1]; sm[u] = m2[0] + m2[1];
    }
    red2_g<NU>(sp, sm, [](float a, float b) { return a + b; });
#pragma unroll
    for (int u = 0; u < NU; ++u) {
        const float rp = __builtin_amdgcn_rcpf(sp[u]), rm = __builtin_amdgcn_rcpf(sm[u]);
#pragma unroll
        for (int i = 0; i < 4; ++i) { const f32x2 a = pp[u][i] * rp, b = pm[u][i] * rm; P[u][i] = pk_bf16(a[0], a[1]); M[u][i] = pk_bf16(b[0], b[1]); }
    }
}

__device__ __forceinline__ GAS u64* flagp(unsigned char* ws, unsigned i) { return (GAS u64*)(ws + WS_FLG + (size_t)i * 64); }
__device__ __forceinline__ void flag_set(unsigned char* ws, unsigned i, u64 tag) { __hip_atomic_store(flagp(ws, i), tag, __ATOMIC_RELAXED, __HIP_MEMORY_SCOPE_AGENT); }
__device__ __forceinline__ void flag_wait(unsigned char* ws, unsigned i0, unsigned st, int n, u64 tag, int lane) {
    GAS u64* p = flagp(ws, i0 + st * (unsigned)(lane < n ? lane : 0));
#pragma unroll 1
    for (unsigned it = 0; it < (1u << 21); ++it) {
        const u64 v = __hip_atomic_load(p, __ATOMIC_RELAXED, __HIP_MEMORY_SCOPE_AGENT);
        if (__all(v == tag)) break;
        __builtin_amdgcn_s_sleep(1);
    }
    asm volatile("" ::: "memory");
}
__device__ __forceinline__ __amdgpu_buffer_rsrc_t ws_rsrc(unsigned char* ws) { return __builtin_amdgcn_make_buffer_rsrc((void*)ws, 0, WS_END, 0x00020000); }
#define ST_SC1(v, rs, off) __builtin_amdgcn_raw_buffer_store_b128(v, rs, off, 0, 16)
#define LD_SC1(rs, off) __builtin_amdgcn_raw_buffer_load_b128(rs, off, 0, 16)

template <int GRP>
__device__ __forceinline__ void g1_mainloop(const unsigned ldsb, lds_t lds, f32x4 (&acc)[8][5], cptr (&bp)[5], const size_t ld4, cptr xn, const int wl, const int lane, const int (&ba)[5]) {
    const size_t ldq = (size_t)32 * 3072 * 2, ldg = (size_t)32 * F_ * 2;
    const int aoff = GRP * 8192 + lane * 16;
    const unsigned avoff = wl * 4096 + lane * 16;
    int so = 0, sl = 3 * G1_STAGE;
    cptr an = xn + 3 * 16384;
    bf16x8 A[8], B[5];
#pragma unroll 1
    for (int t = 0; t < 32; ++t) {
        const bool more = t < 29;
        const unsigned lb = ldsb + sl + G1_SA + wl * 1024, la = ldsb + sl + wl * 4096, rb = ldsb + so;
        if (more) {
            if (GRP == 0) {
                B[0] = lds_tr2_asm<0>(rb + ba[0]); B[1] = lds_tr2_asm<0>(rb + ba[1]);
                glds_v(bp[0], lb);
                B[2] = lds_tr2_asm<0>(rb + ba[2]); B[3] = lds_tr2_asm<0>(rb + ba[3]);
                glds_v(bp[1], lb + 4096);
                B[4] = lds_tr2_asm<0>(rb + ba[4]);
                A[0] = lds_b128(lds + so + aoff); A[1] = lds_b128(lds + so + aoff + 1024);
                glds_v(bp[2], lb + 8192);
                A[2] = lds_b128(lds + so + aoff + 2048); A[3] = lds_b128(lds + so + aoff + 3072); A[4] = lds_b128(lds + so + aoff + 4096);
                glds_v(bp[3], lb + 12288);
                A[5] = lds_b128(lds + so + aoff + 5120); A[6] = lds_b128(lds + so + aoff + 6144); A[7] = lds_b128(lds + so + aoff + 7168);
                glds_v(bp[4], lb + 16384);
                bp[0] += ldq; bp[1] += ldg; bp[2] += ldg; bp[3] += ldg; bp[4] += ld4;
                WAIT_L0(); WAIT_V(10);
            } else {
                B[0] = lds_tr2_asm<0>(rb + ba[0]); B[1] = lds_tr2_asm<0>(rb + ba[1]);
                glds_s_sc1_first<0>(avoff, an, la);
                B[2] = lds_tr2_asm<0>(rb + ba[2]); B[3] = lds_tr2_asm<0>(rb + ba[3]);
                glds_s_sc1_next<1024>(avoff, an);
                B[4] = lds_tr2_asm<0>(rb + ba[4]);
                A[0] = lds_b128(lds + so + aoff); A[1] = lds_b128(lds + so + aoff + 1024); A[2] = lds_b128(lds + so + aoff + 2048);
                glds_s_sc1_next<2048>(avoff, an);
                A[3] = lds_b128(lds + so + aoff + 3072); A[4] = lds_b128(lds + so + aoff + 4096); A[5] = lds_b128(lds + so + aoff + 5120);
                glds_s_sc1_next<3072>(avoff, an);
                A[6] = lds_b128(lds + so + aoff + 6144); A[7] = lds_b128(lds + so + aoff + 7168);
                an += 16384;
                WAIT_L0(); WAIT_V(8);
            }
        } else {
#pragma unroll
            for (int n = 0; n < 5; ++n) B[n] = lds_tr2_asm<0>(rb + ba[n]);
#pragma unroll
            for (int m = 0; m < 8; ++m) A[m] = lds_b128(lds + so + aoff + m * 1024);
            WAIT_L0(); WAIT_V(0);
        }
        so = (so == 3 * G1_STAGE) ? 0 : so + G1_STAGE;
        sl = (sl == 3 * G1_STAGE) ? 0 : sl + G1_STAGE;
        SCHED(); BAR(); SCHED();
        __builtin_amdgcn_s_setprio(1);
#pragma unroll
        for (int m = 0; m < 8; ++m)
#pragma unroll
            for (int n = 0; n < 5; ++n) acc[m][n] = MFMA(B[n], A[m], acc[m][n]);
        __builtin_amdgcn_s_setprio(0);
        SCHED(); BAR(); SCHED();
    }
}

__device__ __forceinline__ void g2_issueB(unsigned bvoff, cptr sb, unsigned l) {
    glds_s(bvoff, sb, l);
    glds_s_o128(bvoff, sb, l + 4096);
}

__global__ __launch_bounds__(512, 2) void k_fused(const u16* __restrict__ x, const u16* __restrict__ wqkv, const u16* __restrict__ wphi, const u16* __restrict__ wo,
                                                  const u16* __restrict__ wg, const u16* __restrict__ wu, const u16* __restrict__ wd,
                                                  unsigned char* __restrict__ ws, u16* __restrict__ out) {
    extern __shared__ __attribute__((aligned(16))) unsigned char shm[];
    lds_t lds = (lds_t)shm;
    const unsigned ldsb = (unsigned)(size_t)lds;
    const int wid = __builtin_amdgcn_readfirstlane(threadIdx.x >> 6);
    const int grp = wid >> 2, wc = wid & 3;
    const int x8 = blockIdx.x & 7, r = blockIdx.x >> 3, ks = x8 >> 1, panel = 4 * (x8 & 1) + (r >> 3), c8 = r & 7, j = 4 * ks + (c8 >> 1), kvt = c8 & 1;
    const __amdgpu_buffer_rsrc_t rs = ws_rsrc(ws);
    STAMP_DECL; STAMP(0);
    u64 tag;
    f32x4 acc[8][5];

    {
        const int lane = threadIdx.x & 63, g = lane >> 4;
        const int row = panel * 256 + (ks * 8 + c8) * 8 + (lane & 7);
        u32x4 xa = {0, 0, 0, 0}, xb = {0, 0, 0, 0}; u32x2 ev = {0, 0};
        {
            const unsigned xo = (unsigned)(row * D_ + (wid * 8 + (lane >> 3)) * 8) * 2u, zero = 0;
            asm volatile("global_load_dwordx4 %0, %1, %2" : "+v"(xa) : "v"(xo), "s"(x) : "memory");
            asm volatile("global_load_dwordx4 %0, %1, %2 offset:1024" : "+v"(xb) : "v"(xo), "s"(x) : "memory");
            asm volatile("global_load_dwordx2 %0, %1, %2 sc1" : "+v"(ev) : "v"(zero), "s"(ws + WS_FLG + FL_EPOCH * 64) : "memory");
        }
        cptr bp[5];
        const size_t ldq = (size_t)32 * 3072 * 2, ldg = (size_t)32 * F_ * 2, ld4 = kvt ? ldq : ldg;
        if (grp == 0) {
            const int gq = (j & 1) ? 7 * j + 3 : 7 * j, gp = (j & 1) ? 7 * j + 1 : 7 * j + 4, gh = (j & 1) ? 7 * j : 7 * j + 6;
            const int krow = 8 * wc + (lane >> 3), chunk = (lane & 7) ^ swzB(krow);
            const size_t rq = (size_t)krow * 3072 * 2 + chunk * 16, rg = (size_t)krow * F_ * 2 + chunk * 16;
            if (!kvt) {
                bp[0] = (cptr)(wqkv + 64 * j) + rq;
                bp[1] = (cptr)(wg + 32 * gq) + rg;      bp[2] = (cptr)(wg + 32 * gq + 64) + rg;
                bp[3] = (cptr)(wu + 32 * gq) + rg;      bp[4] = (cptr)(wu + 32 * gq + 64) + rg;
            } else {
                bp[0] = (cptr)(wqkv + 1024 + 64 * j) + rq;
                bp[1] = (cptr)(wg + 32 * gp) + rg;
                bp[2] = (chunk < 4 ? (cptr)(wg + 32 * gh) : (cptr)(wu + 32 * gh) - 64) + rg;
                bp[3] = (cptr)(wu + 32 * gp) + rg;
                bp[4] = (cptr)(wqkv + 2048 + 64 * j) + rq;
            }
#pragma unroll
            for (int s = 0; s < 3; ++s) {
#pragma unroll
                for (int c = 0; c < 5; ++c) glds_v(bp[c], ldsb + s * G1_STAGE + G1_SA + c * 4096 + wc * 1024);
                bp[0] += ldq; bp[1] += ldg; bp[2] += ldg; bp[3] += ldg; bp[4] += ld4;
            }
            glds_v((cptr)(wphi + (size_t)(2 * j) * 1024 + wc * 512 + lane * 8), ldsb + L_WPHI + wc * 1024);
        } else {
#pragma unroll
            for (int c = 0; c < 5; ++c) bp[c] = nullptr;
        }
        asm volatile("s_cmp_eq_u32 %3, 0\n\ts_cbranch_scc1 .Lxw0_%=\n\ts_waitcnt vmcnt(0)\n\ts_branch .Lxw1_%=\n.Lxw0_%=:\n\ts_waitcnt vmcnt(16)\n.Lxw1_%=:"
                     : "+v"(xa), "+v"(xb), "+v"(ev) : "s"(grp) : "scc", "memory");
        {
            u64 z = (((u64)(unsigned)__builtin_amdgcn_readfirstlane(ev[1]) << 32) | (unsigned)__builtin_amdgcn_readfirstlane(ev[0])) + 0x9E3779B97F4A7C15ull;
            z = (z ^ (z >> 30)) * 0xBF58476D1CE4E5B9ull; z = (z ^ (z >> 27)) * 0x94D049BB133111EBull; tag = z ^ (z >> 31);
        }
        STAMP(1);
        unsigned wt0 = 0, wt1 = 0;
        float f[16]; float s = 0.f;
#pragma unroll
        for (int i = 0; i < 4; ++i) { f[2 * i] = lo_f(xa[i]); f[2 * i + 1] = hi_f(xa[i]); f[8 + 2 * i] = lo_f(xb[i]); f[8 + 2 * i + 1] = hi_f(xb[i]); }
#pragma unroll
        for (int i = 0; i < 16; ++i) s += f[i];
        s += ror8(s); s = sum_g(s);
        LAS float* red = (LAS float*)(lds + L_MISC);
        if (lane < 8) red[wid * 8 + lane] = s;
        LBAR();
        float mean = 0.f;
#pragma unroll
        for (int w = 0; w < 8; ++w) mean += red[w * 8 + (lane & 7)];
        mean *= (1.0f / D_);
        float q = 0.f;
#pragma unroll
        for (int i = 0; i < 16; ++i) { const float d = f[i] - mean; q += d * d; }
        q += ror8(q); q = sum_g(q);
        if (lane < 8) red[64 + wid * 8 + lane] = q;
        LBAR();
        float var = 0.f;
#pragma unroll
        for (int w = 0; w < 8; ++w) var += red[64 + w * 8 + (lane & 7)];
        var *= (1.0f / D_);
        const float m = rbf(mean), sd = rbf(sqrtf(rbf(rbf(var) + 1e-5f))), rsd = 1.0f / sd;
#pragma unroll
        for (int it = 0; it < 2; ++it) {
            const int p = it * 64 + wid * 8 + (lane >> 3), kc = p >> 2, kq = p & 3;
            u32x4 o;
#pragma unroll
            for (int i = 0; i < 4; ++i) {
                const unsigned w = pk_bf16(f[it * 8 + 2 * i] - m, f[it * 8 + 2 * i + 1] - m);
                const f32x2 a = {lo_f(w), hi_f(w)}, q0 = a * rsd;
                const f32x2 q1 = __builtin_elementwise_fma(__builtin_elementwise_fma(-q0, (f32x2){sd, sd}, a), (f32x2){rsd, rsd}, q0);
                o[i] = pk_bf16(q1[0], q1[1]);
            }
            ST_SC1(o, rs, WS_XN + (unsigned)(((panel * 32 + kc) * 16 + ((row >> 4) & 15)) * 1024 + (kq * 16 + (row & 15)) * 16));
        }
        asm volatile("s_waitcnt vmcnt(0)" : "+v"(wt0), "+v"(wt1)::"memory");
        LBAR();
        STAMP(2);
        if (threadIdx.x == 0) flag_set(ws, FL_LN + panel * 32 + ks * 8 + c8, tag);
        unsigned touch0 = 0, touch1 = 0;
        if (wid >= 1 && wid <= 3) {
            const int l48 = lane < 48 ? lane : 47;
            const int L0 = blockIdx.x * 288 + (wid - 1) * 96 + l48, L1 = L0 + 48;
            cptr p0 = L0 < 57344 ? (cptr)wd + (size_t)L0 * 128 : (cptr)wo + (size_t)(L0 - 57344) * 128;
            cptr p1 = L1 < 57344 ? (cptr)wd + (size_t)L1 * 128 : (cptr)wo + (size_t)(L1 - 57344) * 128;
            asm volatile("global_load_dword %0, %2, off\n\tglobal_load_dword %1, %3, off" : "+v"(touch0), "+v"(touch1) : "v"(p0), "v"(p1) : "memory");
        }
        if (wid == 0) flag_wait(ws, FL_LN + panel * 32, 1, 32, tag, lane);
        LBAR();
        STAMP(3);

        cptr xn = uni((cptr)ws + WS_XN + (size_t)panel * 32 * 16384);
        if (grp == 1) {
            const unsigned avoff = wc * 4096 + lane * 16;
#pragma unroll
            for (int s3 = 0; s3 < 3; ++s3) {
                const unsigned la = ldsb + s3 * G1_STAGE + wc * 4096;
                glds_s_sc1_first<0>(avoff, xn + s3 * 16384, la); glds_s_sc1_next<1024>(avoff, xn + s3 * 16384);
                glds_s_sc1_next<2048>(avoff, xn + s3 * 16384);   glds_s_sc1_next<3072>(avoff, xn + s3 * 16384);
            }
            WAIT_V(8);
        }
        int ba[5];
        {
            int cgA, tlA, cgB, tlB;
            if (!kvt) { cgA = 1 + (wc >> 1); tlA = 2 * (wc & 1); cgB = 3 + (wc >> 1); tlB = tlA; }
            else if (wc < 2) { cgA = 1; tlA = 2 * wc; cgB = 3; tlB = 2 * wc; }
            else if (wc == 2) { cgA = 2; tlA = 0; cgB = 2; tlB = 2; }
            else { cgA = 4; tlA = 0; cgB = 4; tlB = 2; }
            const int q4 = (lane >> 2) & 3, p = lane & 3, krow = 8 * g + q4, sw = swzB(krow);
            auto adr = [&](int cg, int tl) { return G1_SA + cg * 4096 + krow * 128 + (((2 * tl + (p >> 1)) ^ sw) << 4) + (p & 1) * 8; };
            ba[0] = adr(cgA, tlA); ba[1] = adr(cgA, tlA + 1); ba[2] = adr(cgB, tlB); ba[3] = adr(cgB, tlB + 1); ba[4] = adr(0, wc);
        }
#pragma unroll
        for (int m = 0; m < 8; ++m)
#pragma unroll
            for (int n = 0; n < 5; ++n) acc[m][n] = (f32x4){0.f, 0.f, 0.f, 0.f};
        if (grp == 0) asm volatile("s_waitcnt vmcnt(0)" : "+v"(touch0), "+v"(touch1)::"memory");
        BAR();
        STAMP(4);
        if (grp == 0) { g1_mainloop<0>(ldsb, lds, acc, bp, ld4, xn, wc, lane, ba); BAR(); }
        else          { BAR(); g1_mainloop<1>(ldsb, lds, acc, bp, ld4, xn, wc, lane, ba); }
        STAMP(5);
    }

    int tz = threadIdx.x; asm volatile("" : "+v"(tz));
    const int lane = tz & 63, g = lane >> 4, l15 = lane & 15;
    const unsigned bvoff = (unsigned)((8 * wc + (lane >> 3)) * 2048 + c8 * 256 + (((lane & 7) ^ swzB(8 * wc + (lane >> 3))) << 4));
    const unsigned bl = ldsb + G2_SB + grp * 24576 + wc * 1024;
    cptr sbp = uni((cptr)wd + ((size_t)(28 * ks + grp) << 16));
    auto prefetchB = [&]() { g2_issueB(bvoff, sbp, bl); g2_issueB(bvoff, sbp + 131072, bl + 8192); sbp += 262144; };
    {
        const int gq = (j & 1) ? 7 * j + 3 : 7 * j, gp = (j & 1) ? 7 * j + 1 : 7 * j + 4, gh = (j & 1) ? 7 * j : 7 * j + 6;
        {
            lds_t kp = lds + E_KP + (128 * grp + l15) * 256 + ((((wc >> 1) * 4 + (wc & 1) * 2 + (g >> 1)) ^ l15) << 4) + (g & 1) * 8;
#pragma unroll
            for (int m = 0; m < 8; ++m) *(LAS u32x2*)(kp + m * 4096) = (u32x2){pk_bf16(acc[m][4][0], acc[m][4][1]), pk_bf16(acc[m][4][2], acc[m][4][3])};
        }
        const bool has_h = !(kvt && wc == 3);
        const int hkc = !kvt ? gq + wc : (wc < 2 ? gp + wc : gh);
        const unsigned hoff = WS_H + (unsigned)(((panel * NKC2 + hkc) * 16 + 8 * grp) * 1024 + frag_off(lane));
        auto geglu_c = [&](int m) {
            const f32x2 a0 = gelu_mul2(acc[m][0].lo, acc[m][2].lo), a1 = gelu_mul2(acc[m][0].hi, acc[m][2].hi);
            const f32x2 b0 = gelu_mul2(acc[m][1].lo, acc[m][3].lo), b1 = gelu_mul2(acc[m][1].hi, acc[m][3].hi);
            return pack_frag(a0, a1, b0, b1);
        };
        auto geglu = [&](int m) { ST_SC1(geglu_c(m), rs, hoff + m * 1024); SCHED(); };
        const int q4 = (lane >> 2) & 3, p4 = lane & 3;
        auto logits = [&](u32x4 (&lg)[4]) {
            lds_t wp = lds + L_WPHI + (8 * g + q4) * 64 + p4 * 8;
            lds_t qp = lds + E_KP + (32 * wid + l15) * 256;
#pragma unroll
            for (int hh = 0; hh < 2; ++hh) {
                const bf16x8 wf0 = lds_tr2(wp + hh * 2048, wp + hh * 2048 + 256), wf1 = lds_tr2(wp + hh * 2048 + 32, wp + hh * 2048 + 288);
#pragma unroll
                for (int tb = 0; tb < 2; ++tb) {
                    const bf16x8 qf = lds_b128(qp + tb * 4096 + (((hh * 4 + g) ^ l15) << 4));
                    const f32x4 z4 = {0.f, 0.f, 0.f, 0.f};
                    const f32x4 p0 = MFMA(wf0, qf, z4), p1 = MFMA(wf1, qf, z4);
                    lg[hh * 2 + tb] = (u32x4){pk_bf16(p0[0], p0[1]), pk_bf16(p0[2], p0[3]), pk_bf16(p1[0], p1[1]), pk_bf16(p1[2], p1[3])};
                    asm volatile("" : "+v"(lg[hh * 2 + tb]));
                }
            }
        };
        if (kvt) {
            if (wc == 3) {
                const int sw = swzP(l15);
                lds_t vp = lds + E_V + (128 * grp + l15) * 128 + (g & 1) * 8;
#pragma unroll
                for (int n = 0; n < 4; ++n) {
                    lds_t vn = vp + (((2 * n + (g >> 1)) ^ sw) << 4);
#pragma unroll
                    for (int m = 0; m < 8; ++m) *(LAS u32x2*)(vn + m * 2048) = (u32x2){pk_bf16(acc[m][n][0], acc[m][n][1]), pk_bf16(acc[m][n][2], acc[m][n][3])};
                }
            }
            LBAR();
            STAMP(6);
            u32x4 lg[4], P[4], M[4];
            logits(lg);
            WAIT_L0();
            soft_pm<4>(lg, P, M);
            {
                const int sw = swzF(l15);
                lds_t pb = lds + E_KP + (32 * wid + l15) * 256 + (g & 1) * 8;
#pragma unroll
                for (int u = 0; u < 4; ++u) {
                    const int c0 = (u >> 1) * 8 + (g >> 1), tb = u & 1;
                    *(LAS u32x2*)(pb + tb * 4096 + (((c0 + 0) ^ sw) << 4)) = (u32x2){P[u][0], P[u][1]};
                    *(LAS u32x2*)(pb + tb * 4096 + (((c0 + 2) ^ sw) << 4)) = (u32x2){P[u][2], P[u][3]};
                    *(LAS u32x2*)(pb + tb * 4096 + (((c0 + 4) ^ sw) << 4)) = (u32x2){M[u][0], M[u][1]};
                    *(LAS u32x2*)(pb + tb * 4096 + (((c0 + 6) ^ sw) << 4)) = (u32x2){M[u][2], M[u][3]};
                }
            }
            LBAR();
            STAMP(7);
            {
                const int hh = wid >> 2, ft = wid & 3;
                f32x4 kv0 = {0.f, 0.f, 0.f, 0.f}, kv1 = kv0, zz = kv0;
                const bf16x8 ones = {0x3F80, 0x3F80, 0x3F80, 0x3F80, 0x3F80, 0x3F80, 0x3F80, 0x3F80};
                const int t0 = 8 * g + q4, t1 = t0 + 4, cp = hh * 8 + 2 * ft + (p4 >> 1), cv = hh * 4 + (p4 >> 1), hb8 = (p4 & 1) * 8;
                lds_t pf0 = lds + E_KP + t0 * 256 + ((cp ^ swzF(t0 & 15)) << 4) + hb8, pf1 = lds + E_KP + t1 * 256 + ((cp ^ swzF(t1 & 15)) << 4) + hb8;
                lds_t va0 = lds + E_V + t0 * 128 + ((cv ^ swzP(t0)) << 4) + hb8, va1 = lds + E_V + t1 * 128 + ((cv ^ swzP(t1)) << 4) + hb8;
                lds_t vb0 = lds + E_V + t0 * 128 + (((cv + 2) ^ swzP(t0)) << 4) + hb8, vb1 = lds + E_V + t1 * 128 + (((cv + 2) ^ swzP(t1)) << 4) + hb8;
#pragma unroll
                for (int k8 = 0; k8 < 8; ++k8) {
                    const bf16x8 pf = lds_tr2(pf0 + k8 * 8192, pf1 + k8 * 8192), v0 = lds_tr2(va0 + k8 * 4096, va1 + k8 * 4096), v1 = lds_tr2(vb0 + k8 * 4096, vb1 + k8 * 4096);
                    kv0 = MFMA(pf, v0, kv0); kv1 = MFMA(pf, v1, kv1); zz = MFMA(pf, ones, zz);
                }
                ST_SC1(((u32x4){pk_bf16(kv0[0], kv0[1]), pk_bf16(kv0[2], kv0[3]), pk_bf16(kv1[0], kv1[1]), pk_bf16(kv1[2], kv1[3])}), rs,
                       WS_PART + (unsigned)(((panel * 32 + 2 * j + hh) * 4 + ft) * 1024 + lane * 16));
                if (l15 == 0) ST_SC1(__builtin_bit_cast(u32x4, zz), rs, WS_PZ + (unsigned)(((panel * 32 + 2 * j + hh) * 64 + 16 * ft + 4 * g) * 4));
            }
            SCHED();
            u32x4 h0 = {0, 0, 0, 0};
            if (has_h) h0 = geglu_c(0);
            WAIT_V(0);
            LBAR();
            if (threadIdx.x == 0) flag_set(ws, FL_KV + j * 8 + panel, tag);
            STAMP(8);
            prefetchB();
            if (has_h) {
                ST_SC1(h0, rs, hoff); SCHED();
                geglu(1); geglu(2); geglu(3); geglu(4); geglu(5); geglu(6); geglu(7);
            }
            WAIT_V(0);
            LBAR();
            if (threadIdx.x == 0) flag_set(ws, FL_H + (panel * 4 + ks) * 8 + c8, tag);
            STAMP(9);
        } else {
            u32x4 lg[4], P[4], M[4];
            LBAR();
            logits(lg);
            soft_pm<4>(lg, P, M);
#pragma unroll
            for (int u = 0; u < 4; ++u) asm volatile("" : "+v"(P[u]), "+v"(M[u]));
            SCHED();
            STAMP(6);
            geglu(0); geglu(1); geglu(2); geglu(3); geglu(4); geglu(5);
            STAMP(7);
            if (wid == 0) flag_wait(ws, FL_KV + j * 8, 1, 8, tag, lane);
            LBAR();
            STAMP(8);
            {
                const int hh = wid >> 2, ft = wid & 3;
                f32x4 a0, a1, az;
                u32x4 pw[8]; f32x4 pz[8];
#pragma unroll
                for (int p = 0; p < 8; ++p) {
                    pw[p] = LD_SC1(rs, WS_PART + (unsigned)(((p * 32 + 2 * j + hh) * 4 + ft) * 1024 + lane * 16));
                    pz[p] = __builtin_bit_cast(f32x4, LD_SC1(rs, WS_PZ + (unsigned)(((p * 32 + 2 * j + hh) * 64 + 16 * ft + 4 * g) * 4)));
                }
                SCHED();
                geglu(6); geglu(7);
#pragma unroll
                for (int p = 0; p < 8; ++p) {
                    const f32x4 b0 = {lo_f(pw[p][0]), hi_f(pw[p][0]), lo_f(pw[p][1]), hi_f(pw[p][1])}, b1 = {lo_f(pw[p][2]), hi_f(pw[p][2]), lo_f(pw[p][3]), hi_f(pw[p][3])};
                    if (p == 0) { a0 = b0; a1 = b1; az = pz[0]; } else { a0 += b0; a1 += b1; az += pz[p]; }
                }
                lds_t kb = lds + E_V + ((hh * 2 + (ft >> 1)) * 3) * 1024 + lane * 16 + (ft & 1) * 8;
                *(LAS u32x2*)(kb) = (u32x2){pk_bf16(a0[0], a0[1]), pk_bf16(a0[2], a0[3])};
                *(LAS u32x2*)(kb + 1024) = (u32x2){pk_bf16(a1[0], a1[1]), pk_bf16(a1[2], a1[3])};
                *(LAS u32x2*)(kb + 2048) = (u32x2){pk_bf16(az[0], az[1]), pk_bf16(az[2], az[3])};
            }
            prefetchB();
            WAIT_V(4);
            LBAR();
            if (threadIdx.x == 0) flag_set(ws, FL_H + (panel * 4 + ks) * 8 + c8, tag);
            STAMP(9);
#pragma unroll
            for (int hh = 0; hh < 2; ++hh) {
                bf16x8 kf[2][3];
#pragma unroll
                for (int s = 0; s < 2; ++s)
#pragma unroll
                    for (int d = 0; d < 3; ++d) kf[s][d] = lds_b128(lds + E_V + ((hh * 2 + s) * 3 + d) * 1024 + lane * 16);
#pragma unroll
                for (int tb = 0; tb < 2; ++tb) {
                    const bf16x8 Pf = __builtin_bit_cast(bf16x8, P[hh * 2 + tb]), Mf = __builtin_bit_cast(bf16x8, M[hh * 2 + tb]);
                    const f32x4 z4 = {0.f, 0.f, 0.f, 0.f};
                    const f32x4 n0 = MFMA(kf[1][0], Mf, MFMA(kf[0][0], Pf, z4));
                    const f32x4 n1 = MFMA(kf[1][1], Mf, MFMA(kf[0][1], Pf, z4));
                    const f32x4 dn = MFMA(kf[1][2], Mf, MFMA(kf[0][2], Pf, z4));
                    const float rden = __builtin_amdgcn_rcpf(rbf(rbf(dn[0]) + 1e-6f));
                    ST_SC1(pack_frag(n0.lo * rden, n0.hi * rden, n1.lo * rden, n1.hi * rden), rs, WS_H + (unsigned)(((panel * NKC2 + 112 + 2 * j + hh) * 16 + 2 * wid + tb) * 1024 + frag_off(lane)));
                }
            }
        }
    }
    STAMP(10);
    STAMP_OUT(0, 0, 11);

    STAMP(0);
    if (wid == 0) flag_wait(ws, FL_H + (panel * 4 + ks) * 8, 1, 8, tag, lane);
    LBAR();
    STAMP(1);
    f32x4 c2[8][4];
    {
        const int wm = wc >> 1, wn = wc & 1;
        cptr sa = uni((cptr)ws + WS_H + ((size_t)(panel * NKC2 + 28 * ks + grp) << 14));
        const unsigned avoff = (unsigned)(wc * 4096 + lane * 16), al = ldsb + grp * 49152 + wc * 4096;
        auto issueA = [&](unsigned slot_off) {
            glds_s_sc1_first<0>(avoff, sa, al + slot_off); glds_s_sc1_next<1024>(avoff, sa); glds_s_sc1_next<2048>(avoff, sa); glds_s_sc1_next<3072>(avoff, sa);
            sa += 32768;
        };
        issueA(0); issueA(16384);
        unsigned bb[4];
        {
            const int q4 = (lane >> 2) & 3, p = lane & 3, kr = 8 * g + q4, sw = swzB(kr);
#pragma unroll
            for (int tl = 0; tl < 4; ++tl) bb[tl] = ldsb + G2_SB + grp * 24576 + wn * 4096 + kr * 128 + (((2 * tl + (p >> 1)) ^ sw) << 4) + (p & 1) * 8;
        }
        lds_t ap = lds + grp * 49152 + wm * 8192 + lane * 16;
#pragma unroll
        for (int m = 0; m < 8; ++m)
#pragma unroll
            for (int n = 0; n < 4; ++n) c2[m][n] = (f32x4){0.f, 0.f, 0.f, 0.f};
        u32x2 ofl = {0, 0};
        const unsigned fo = (FL_O + (panel * 4 + ks) * 4 + (lane & 3)) * 64;
        WAIT_V(4);
        LBAR();
        if (!kvt && threadIdx.x == 0) flag_set(ws, FL_O + (panel * 4 + ks) * 4 + (c8 >> 1), tag);
        if (grp == 1) BAR();
        STAMP(2);
        auto step = [&](auto uc, const int t) {
            constexpr int U = decltype(uc)::value, W = (U + 2) % 3;
            bf16x8 A[8], B[4];
#define FST(x)
            FST(f0);
            if (U == 0) {
                if (t == 12) {
                    asm volatile("" : "+v"(ofl));
                    const u64 v = ((u64)ofl[1] << 32) | ofl[0];
                    if (!__all(v == tag)) flag_wait(ws, FL_O + (panel * 4 + ks) * 4, 1, 4, tag, lane);
                    sa = uni((cptr)ws + WS_H + ((size_t)(panel * NKC2 + 112 + 8 * ks + grp) << 14));
                    sbp = uni((cptr)wo + ((size_t)(8 * ks + grp) << 16));
                }
                asm volatile("s_cmp_lg_u32 %3, 9\n\ts_cbranch_scc1 .Lofl_%=\n\tglobal_load_dwordx2 %0, %1, %2 sc1\n.Lofl_%=:" : "+v"(ofl) : "v"(fo), "s"(ws + WS_FLG), "s"(t) : "scc", "memory");
            }
            const bool more = U == 0 || t < 16;
            B[0] = lds_tr2_asm<U * 8192>(bb[0]); B[1] = lds_tr2_asm<U * 8192>(bb[1]);
            if (more) glds_s(bvoff, sbp, bl + W * 8192);
            B[2] = lds_tr2_asm<U * 8192>(bb[2]); B[3] = lds_tr2_asm<U * 8192>(bb[3]);
            if (more) { glds_s_o128(bvoff, sbp, bl + W * 8192 + 4096); sbp += 131072; }
            A[0] = lds_b128(ap + U * 16384); A[1] = lds_b128(ap + U * 16384 + 1024);
            if (more) { glds_s_sc1_first<0>(avoff, sa, al + W * 16384); glds_s_sc1_next<1024>(avoff, sa); }
            A[2] = lds_b128(ap + U * 16384 + 2048); A[3] = lds_b128(ap + U * 16384 + 3072); A[4] = lds_b128(ap + U * 16384 + 4096);
            if (more) { glds_s_sc1_next<2048>(avoff, sa); glds_s_sc1_next<3072>(avoff, sa); sa += 32768; }
            A[5] = lds_b128(ap + U * 16384 + 5120); A[6] = lds_b128(ap + U * 16384 + 6144); A[7] = lds_b128(ap + U * 16384 + 7168);
            FST(f1);
            WAIT_L0();
            FST(f2);
            if (!more) WAIT_V(0);
            SCHED(); BAR(); SCHED();
            FST(f3);
            __builtin_amdgcn_s_setprio(1);
#pragma unroll
            for (int m = 0; m < 8; ++m)
#pragma unroll
                for (int n = 0; n < 4; ++n) c2[m][n] = MFMA(B[n], A[m], c2[m][n]);
            __builtin_amdgcn_s_setprio(0);
            FST(f4);
            WAIT_V(6);
            FST(f5);
            SCHED(); BAR(); SCHED();
        };
#pragma unroll 1
        for (int t = 0; t < 18; t += 3) { step(std::integral_constant<int, 0>{}, t); step(std::integral_constant<int, 1>{}, t + 1); step(std::integral_constant<int, 2>{}, t + 2); }
        if (grp == 0) BAR();
    }
    STAMP(3);
    f32x4 c3[4][4];
    {
        int tz3 = threadIdx.x; asm volatile("" : "+v"(tz3));
        lds_t mine = lds + wid * 16384 + (tz3 & 63) * 16, theirs = lds + (wid ^ 4) * 16384 + (tz3 & 63) * 16;
        if (grp == 0) {
#pragma unroll
            for (int m = 0; m < 4; ++m)
#pragma unroll
                for (int n = 0; n < 4; ++n) { *(LAS f32x4*)(mine + (m * 4 + n) * 1024) = c2[4 + m][n]; c3[m][n] = c2[m][n]; }
        } else {
#pragma unroll
            for (int m = 0; m < 4; ++m)
#pragma unroll
                for (int n = 0; n < 4; ++n) { *(LAS f32x4*)(mine + (m * 4 + n) * 1024) = c2[m][n]; c3[m][n] = c2[4 + m][n]; }
        }
        LBAR();
#pragma unroll
        for (int m = 0; m < 4; ++m)
#pragma unroll
            for (int n = 0; n < 4; ++n) c3[m][n] += *(const LAS f32x4*)(theirs + (m * 4 + n) * 1024);
    }

    {
        int tz2 = threadIdx.x; asm volatile("" : "+v"(tz2));
        const int ln = tz2 & 63, tile = panel * 8 + c8, qd = 2 * ((wid & 3) >> 1) + grp, wn = wid & 1;
        if (qd != ks) {
            const unsigned base = WS_XCH + (unsigned)((((tile * 4 + ks) * 8 + wid) * 8) * 1024 + ln * 16);
#pragma unroll
            for (int m = 0; m < 4; ++m)
#pragma unroll
                for (int n2 = 0; n2 < 2; ++n2) {
                    const f32x4 a = c3[m][2 * n2], b = c3[m][2 * n2 + 1];
                    ST_SC1(((u32x4){pk_bf16(a[0], a[1]), pk_bf16(a[2], a[3]), pk_bf16(b[0], b[1]), pk_bf16(b[2], b[3])}), rs, base + (m * 2 + n2) * 1024);
                }
            STAMP(4);
            WAIT_V(0);
            if (ln == 0) flag_set(ws, FL_X + (tile * 4 + ks) * 8 + wid, tag);
            STAMP(5);
        } else {
            const int row0 = panel * 256 + qd * 64 + (ln & 15), col0 = c8 * 128 + wn * 64 + 8 * frag_kq(ln);
            const __amdgpu_buffer_rsrc_t ors = __builtin_amdgcn_make_buffer_rsrc((void*)out, 0, S_ * D_ * 2, 0x00020000);
            u32x4 xr[4][2];
#pragma unroll
            for (int m = 0; m < 4; ++m)
#pragma unroll
                for (int n2 = 0; n2 < 2; ++n2) xr[m][n2] = *(const u32x4*)(x + (size_t)(row0 + 16 * m) * D_ + col0 + 32 * n2);
            {
                const int src = (ks + 1 + (ln < 3 ? ln : 0)) & 3;
                GAS u64* p = flagp(ws, FL_X + (tile * 4 + src) * 8 + wid);
#pragma unroll 1
                for (unsigned it = 0; it < (1u << 21); ++it) {
                    const u64 v = __hip_atomic_load(p, __ATOMIC_RELAXED, __HIP_MEMORY_SCOPE_AGENT);
                    if (__all(v == tag)) break;
                    __builtin_amdgcn_s_sleep(1);
                }
                asm volatile("" ::: "memory");
            }
            STAMP(4);
#pragma unroll
            for (int s = 0; s < 4; ++s) {
                if (s == ks) continue;
                const unsigned base = WS_XCH + (unsigned)((((tile * 4 + s) * 8 + wid) * 8) * 1024 + ln * 16);
#pragma unroll
                for (int m = 0; m < 4; ++m)
#pragma unroll
                    for (int n2 = 0; n2 < 2; ++n2) {
                        const u32x4 w = LD_SC1(rs, base + (m * 2 + n2) * 1024);
                        c3[m][2 * n2] += (f32x4){lo_f(w[0]), hi_f(w[0]), lo_f(w[1]), hi_f(w[1])};
                        c3[m][2 * n2 + 1] += (f32x4){lo_f(w[2]), hi_f(w[2]), lo_f(w[3]), hi_f(w[3])};
                    }
            }
            STAMP(5);
#pragma unroll
            for (int m = 0; m < 4; ++m)
#pragma unroll
                for (int n2 = 0; n2 < 2; ++n2) {
                    float a[4], b[4];
#pragma unroll
                    for (int e = 0; e < 4; ++e) {
                        auto rr = __builtin_amdgcn_permlane16_swap(__float_as_uint(c3[m][2 * n2][e]), __float_as_uint(c3[m][2 * n2 + 1][e]), false, false);
                        a[e] = __uint_as_float(rr[0]); b[e] = __uint_as_float(rr[1]);
                    }
                    const u32x4 xv = xr[m][n2];
                    u32x4 o;
                    o[0] = pk_bf16(a[0] + lo_f(xv[0]), a[1] + hi_f(xv[0])); o[1] = pk_bf16(a[2] + lo_f(xv[1]), a[3] + hi_f(xv[1]));
                    o[2] = pk_bf16(b[0] + lo_f(xv[2]), b[1] + hi_f(xv[2])); o[3] = pk_bf16(b[2] + lo_f(xv[3]), b[3] + hi_f(xv[3]));
                    __builtin_amdgcn_raw_buffer_store_b128(o, ors, (unsigned)(((row0 + 16 * m) * D_ + col0 + 32 * n2) * 2), 0, 16);
                }
        }
        const int ln2 = ln;
        if (blockIdx.x == 0 && wid == 0) {
#pragma unroll 1
            for (int i = 0; i < 4; ++i) flag_wait(ws, FL_LN + 64 * i, 1, 64, tag, ln2);
            if (ln2 == 0) flag_set(ws, FL_EPOCH, tag);
        }
    }
    STAMP(6);
    STAMP_OUT(1, 0, 7);
}

extern "C" void kernel_launch(void* const* d_in, const int* in_sizes, int n_in, void* d_out, int out_size, void* d_ws, size_t ws_size, hipStream_t stream) {
    const u16* x = (const u16*)d_in[0]; const u16* wqkv = (const u16*)d_in[1]; const u16* wphi = (const u16*)d_in[2]; const u16* wo = (const u16*)d_in[3];
    const u16* wg = (const u16*)d_in[5]; const u16* wu = (const u16*)d_in[6]; const u16* wd = (const u16*)d_in[7];
    (void)hipFuncSetAttribute((const void*)k_fused, hipFuncAttributeMaxDynamicSharedMemorySize, L_TOTAL);
    k_fused<<<256, 512, L_TOTAL, stream>>>(x, wqkv, wphi, wo, wg, wu, wd, (unsigned char*)d_ws, (u16*)d_out);
}
```

```cpp
#include <hip/hip_runtime.h>
#include <type_traits>

typedef unsigned short u16;
typedef unsigned long long u64;
typedef short bf16x8 __attribute__((ext_vector_type(8)));
typedef short s16x4 __attribute__((ext_vector_type(4)));
typedef float f32x4 __attribute__((ext_vector_type(4)));
typedef float f32x2 __attribute__((ext_vector_type(2)));
typedef unsigned u32x4 __attribute__((ext_vector_type(4)));
typedef unsigned u32x2 __attribute__((ext_vector_type(2)));
#define LAS __attribute__((address_space(3)))
#define GAS __attribute__((address_space(1)))
typedef LAS unsigned char* lds_t;
typedef const unsigned char* cptr;

constexpr int S_ = 2048, D_ = 1024, F_ = 3584;
constexpr int CODE_LINES = 256;
constexpr int NKC2 = 144;

constexpr unsigned WS_XN   = 0;
constexpr unsigned WS_H    = WS_XN + S_ * D_ * 2;
constexpr unsigned WS_PART = WS_H + 8 * NKC2 * 16 * 1024;
constexpr unsigned WS_PZ   = WS_PART + 8 * 32 * 4 * 1024;
constexpr unsigned WS_XCH  = WS_PZ + 8 * 32 * 64 * 4;
constexpr unsigned WS_FLG  = WS_XCH + 64 * 4 * 8 * 8 * 1024;
constexpr unsigned FL_LN = 0, FL_KV = FL_LN + 256, FL_H = FL_KV + 128, FL_O = FL_H + 256, FL_X = FL_O + 128, FL_EPOCH = FL_X + 2048, FL_N = FL_EPOCH + 1;
constexpr unsigned WS_END  = WS_FLG + FL_N * 64;

constexpr int G1_SA = 16384, G1_STAGE = 36864;
constexpr int L_MISC = 158720, L_WPHI = 159744, L_TOTAL = 163840;
constexpr int E_KP = 0, E_V = 65536;
constexpr int G2_SB = 98304;

__device__ __forceinline__ unsigned pk_bf16(float lo, float hi) {
    typedef __bf16 bf2 __attribute__((ext_vector_type(2)));
    bf2 r = {(__bf16)lo, (__bf16)hi};
    return __builtin_bit_cast(unsigned, r);
}
__device__ __forceinline__ float rbf(float x) { return __uint_as_float(pk_bf16(x, x) & 0xffff0000u); }
__device__ __forceinline__ float lo_f(unsigned w) { return __uint_as_float(w << 16); }
__device__ __forceinline__ float hi_f(unsigned w) { return __uint_as_float(w & 0xffff0000u); }

#define STAMP_DECL
#define STAMP(i)
#define STAMP_ARG
#define STAMP_PASS
#define STAMP_OUT(kern, lo, n)

#define XW_CNT "18"
#define WAIT_V(n) asm volatile("s_waitcnt vmcnt(" #n ")" ::: "memory")
#define WAIT_L0() do { asm volatile("" ::: "memory"); __builtin_amdgcn_s_waitcnt(0xC07F); asm volatile("" ::: "memory"); } while (0)
#define BAR() __builtin_amdgcn_s_barrier()
#define LBAR() asm volatile("s_waitcnt lgkmcnt(0)\n\ts_barrier" ::: "memory")
#define SCHED() __builtin_amdgcn_sched_barrier(0)
#define MFMA(a, b, c) __builtin_amdgcn_mfma_f32_16x16x32_bf16(a, b, c, 0, 0, 0)

__device__ __forceinline__ cptr uni(cptr p) {
    const u64 v = (u64)p;
    return (cptr)(((u64)(unsigned)__builtin_amdgcn_readfirstlane((unsigned)(v >> 32)) << 32) | (unsigned)__builtin_amdgcn_readfirstlane((unsigned)v));
}
__device__ __forceinline__ void glds_v(cptr g, unsigned l) {
    asm volatile("s_mov_b32 m0, %1\n\ts_nop 0\n\tglobal_load_lds_dwordx4 %0, off" ::"v"(g), "s"(l) : "memory");
}
__device__ __forceinline__ void glds_s(unsigned voff, cptr sb, unsigned l) {
    asm volatile("s_nop 2\n\ts_mov_b32 m0, %2\n\ts_nop 0\n\tglobal_load_lds_dwordx4 %0, %1" ::"v"(voff), "s"(sb), "s"(l) : "memory");
}
__device__ __forceinline__ void glds_s_o128(unsigned voff, cptr sb, unsigned l) {
    asm volatile("s_mov_b32 m0, %2\n\ts_nop 0\n\tglobal_load_lds_dwordx4 %0, %1 offset:128" ::"v"(voff), "s"(sb), "s"(l - 128u) : "memory");
}
template <int OFF> __device__ __forceinline__ void glds_s_sc1_first(unsigned voff, cptr sb, unsigned l) {
    asm volatile("s_nop 2\n\ts_mov_b32 m0, %2\n\ts_nop 0\n\tglobal_load_lds_dwordx4 %0, %1 offset:%3 sc1" ::"v"(voff), "s"(sb), "s"(l), "i"(OFF) : "memory");
}
template <int OFF> __device__ __forceinline__ void glds_s_sc1_next(unsigned voff, cptr sb) {
    asm volatile("global_load_lds_dwordx4 %0, %1 offset:%2 sc1" ::"v"(voff), "s"(sb), "i"(OFF) : "memory");
}
template <int OFF> __device__ __forceinline__ void gld_sc1(u32x4& d, unsigned voff, cptr sb) {
    asm volatile("global_load_dwordx4 %0, %1, %2 offset:%3 sc1" : "+v"(d) : "v"(voff), "s"(sb), "i"(OFF) : "memory");
}
__device__ __forceinline__ bf16x8 lds_b128(lds_t p) { return *(const LAS bf16x8*)p; }
__device__ __forceinline__ bf16x8 lds_tr2(lds_t p0, lds_t p1) {
    s16x4 a = __builtin_amdgcn_ds_read_tr16_b64_v4i16((LAS s16x4*)p0);
    s16x4 b = __builtin_amdgcn_ds_read_tr16_b64_v4i16((LAS s16x4*)p1);
    return __builtin_shufflevector(a, b, 0, 1, 2, 3, 4, 5, 6, 7);
}
template <int OFF> __device__ __forceinline__ bf16x8 lds_tr2_asm(unsigned addr) {
    s16x4 a, b;
    asm volatile("ds_read_b64_tr_b16 %0, %2 offset:%3\n\tds_read_b64_tr_b16 %1, %2 offset:%4" : "=&v"(a), "=&v"(b) : "v"(addr), "i"(OFF), "i"(OFF + 512) : "memory");
    return __builtin_shufflevector(a, b, 0, 1, 2, 3, 4, 5, 6, 7);
}

__device__ __forceinline__ float x16(float v, float& o) { auto r = __builtin_amdgcn_permlane16_swap(__float_as_uint(v), __float_as_uint(v), false, false); o = __uint_as_float(r[1]); return __uint_as_float(r[0]); }
__device__ __forceinline__ float x32(float v, float& o) { auto r = __builtin_amdgcn_permlane32_swap(__float_as_uint(v), __float_as_uint(v), false, false); o = __uint_as_float(r[1]); return __uint_as_float(r[0]); }
__device__ __forceinline__ float sum_g(float v) { float o, a = x16(v, o); v = a + o; a = x32(v, o); return a + o; }
__device__ __forceinline__ float max_g(float v) { float o, a = x16(v, o); v = fmaxf(a, o); a = x32(v, o); return fmaxf(a, o); }
__device__ __forceinline__ float min_g(float v) { float o, a = x16(v, o); v = fminf(a, o); a = x32(v, o); return fminf(a, o); }
__device__ __forceinline__ float ror8(float v) { return __int_as_float(__builtin_amdgcn_update_dpp(0, __float_as_int(v), 0x128, 0xf, 0xf, false)); }

__device__ __forceinline__ int swzB(int k) { return (((k >> 1) & 1) << 1) | (((k >> 3) & 1) << 2); }
__device__ __forceinline__ int swzP(int k) { return ((k >> 2) & 1) | (((k >> 1) & 1) << 1) | (((k >> 3) & 1) << 2); }
__device__ __forceinline__ int swzF(int k) { return ((k >> 2) & 1) | ((k & 3) << 1) | (((k >> 3) & 1) << 3); }

__device__ __forceinline__ f32x2 gelu_mul2(f32x2 g, f32x2 u) {
    constexpr float C0 = -2.0f * 0.7978845608028654f * 1.4426950408889634f, C1 = C0 * 0.044715f;
    const f32x2 a = g * (g * g * C1 + C0);
    const f32x2 d = (f32x2){__builtin_amdgcn_exp2f(a[0]), __builtin_amdgcn_exp2f(a[1])} + 1.0f;
    return (g * u) * (f32x2){__builtin_amdgcn_rcpf(d[0]), __builtin_amdgcn_rcpf(d[1])};
}
__device__ __forceinline__ u32x4 pack_frag(const f32x2 a0, const f32x2 a1, const f32x2 b0, const f32x2 b1) {
    unsigned x0 = pk_bf16(a0[0], a0[1]), x1 = pk_bf16(a1[0], a1[1]), y0 = pk_bf16(b0[0], b0[1]), y1 = pk_bf16(b1[0], b1[1]);
    auto r0 = __builtin_amdgcn_permlane16_swap(x0, y0, false, false);
    auto r1 = __builtin_amdgcn_permlane16_swap(x1, y1, false, false);
    u32x4 o; o[0] = r0[0]; o[1] = r1[0]; o[2] = r0[1]; o[3] = r1[1];
    return o;
}
__device__ __forceinline__ int frag_kq(int lane) { const int g = lane >> 4; return ((g & 1) << 1) | (g >> 1); }
__device__ __forceinline__ int frag_off(int lane) { return (frag_kq(lane) * 16 + (lane & 15)) * 16; }

__device__ __forceinline__ float max3(float a, float b, float c) { float r; asm("v_max3_f32 %0, %1, %2, %3" : "=v"(r) : "v"(a), "v"(b), "v"(c)); return r; }
__device__ __forceinline__ float min3(float a, float b, float c) { float r; asm("v_min3_f32 %0, %1, %2, %3" : "=v"(r) : "v"(a), "v"(b), "v"(c)); return r; }
__device__ __forceinline__ float max2(float a, float b) { float r; asm("v_max_f32 %0, %1, %2" : "=v"(r) : "v"(a), "v"(b)); return r; }
template <int NU, class Op> __device__ __forceinline__ void red2_g(float (&a)[NU], float (&b)[NU], Op op) {
    float z[NU], y[NU];
#pragma unroll
    for (int u = 0; u < NU; ++u) {
        auto r = __builtin_amdgcn_permlane16_swap(__float_as_uint(a[u]), __float_as_uint(b[u]), false, false);
        z[u] = op(__uint_as_float(r[0]), __uint_as_float(r[1]));
    }
#pragma unroll
    for (int u = 0; u < NU; ++u) {
        auto q = __builtin_amdgcn_permlane32_swap(__float_as_uint(z[u]), __float_as_uint(z[u]), false, false);
        y[u] = op(__uint_as_float(q[0]), __uint_as_float(q[1]));
    }
#pragma unroll
    for (int u = 0; u < NU; ++u) {
        auto w = __builtin_amdgcn_permlane16_swap(__float_as_uint(y[u]), __float_as_uint(y[u]), false, false);
        a[u] = __uint_as_float(w[0]); b[u] = __uint_as_float(w[1]);
    }
}
template <int NU> __device__ __forceinline__ void soft_pm(const u32x4 (&w)[NU], u32x4 (&P)[NU], u32x4 (&M)[NU]) {
    constexpr float L2E = 1.4426950408889634f;
    f32x2 pv[NU][4]; float mx[NU], nm[NU];
#pragma unroll
    for (int u = 0; u < NU; ++u) {
#pragma unroll
        for (int i = 0; i < 4; ++i) pv[u][i] = (f32x2){lo_f(w[u][i]), hi_f(w[u][i])};
        mx[u] = max3(max3(max3(pv[u][0][0], pv[u][0][1], pv[u][1][0]), pv[u][1][1], pv[u][2][0]), max2(pv[u][2][1], pv[u][3][0]), pv[u][3][1]);
        nm[u] = -min3(min3(min3(pv[u][0][0], pv[u][0][1], pv[u][1][0]), pv[u][1][1], pv[u][2][0]), min3(pv[u][2][1], pv[u][3][0], pv[u][3][1]), pv[u][3][1]);
    }
    red2_g<NU>(mx, nm, [](float a, float b) { return max2(a, b); });
    f32x2 pp[NU][4], pm[NU][4]; float sp[NU], sm[NU];
#pragma unroll
    for (int u = 0; u < NU; ++u) {
        const float cp = -mx[u] * L2E, cm = -nm[u] * L2E;
#pragma unroll
        for (int i = 0; i < 4; ++i) {
            const f32x2 a = pv[u][i] * L2E + cp, b = pv[u][i] * (-L2E) + cm;
            pp[u][i] = (f32x2){__builtin_amdgcn_exp2f(a[0]), __builtin_amdgcn_exp2f(a[1])};
            pm[u][i] = (f32x2){__builtin_amdgcn_exp2f(b[0]), __builtin_amdgcn_exp2f(b[1])};
        }
        const f32x2 s2 = (pp[u][0] + pp[u][1]) + (pp[u][2] + pp[u][3]), m2 = (pm[u][0] + pm[u][1]) + (pm[u][2] + pm[u][3]);
        sp[u] = s2[0] + s2[1]; sm[u] = m2[0] + m2[1];
    }
    red2_g<NU>(sp, sm, [](float a, float b) { return a + b; });
#pragma unroll
    for (int u = 0; u < NU; ++u) {
        const float rp = __builtin_amdgcn_rcpf(sp[u]), rm = __builtin_amdgcn_rcpf(sm[u]);
#pragma unroll
        for (int i = 0; i < 4; ++i) { const f32x2 a = pp[u][i] * rp, b = pm[u][i] * rm; P[u][i] = pk_bf16(a[0], a[1]); M[u][i] = pk_bf16(b[0], b[1]); }
    }
}

__device__ __forceinline__ GAS u64* flagp(unsigned char* ws, unsigned i) { return (GAS u64*)(ws + WS_FLG + (size_t)i * 64); }
__device__ __forceinline__ void flag_set(unsigned char* ws, unsigned i, u64 tag) { __hip_atomic_store(flagp(ws, i), tag, __ATOMIC_RELAXED, __HIP_MEMORY_SCOPE_AGENT); }
__device__ __forceinline__ void flag_wait(unsigned char* ws, unsigned i0, unsigned st, int n, u64 tag, int lane) {
    GAS u64* p = flagp(ws, i0 + st * (unsigned)(lane < n ? lane : 0));
#pragma unroll 1
    for (unsigned it = 0; it < (1u << 21); ++it) {
        const u64 v = __hip_atomic_load(p, __ATOMIC_RELAXED, __HIP_MEMORY_SCOPE_AGENT);
        if (__all(v == tag)) break;
        __builtin_amdgcn_s_sleep(1);
    }
    asm volatile("" ::: "memory");
}
__device__ __forceinline__ __amdgpu_buffer_rsrc_t ws_rsrc(unsigned char* ws) { return __builtin_amdgcn_make_buffer_rsrc((void*)ws, 0, WS_END, 0x00020000); }
#define ST_SC1(v, rs, off) __builtin_amdgcn_raw_buffer_store_b128(v, rs, off, 0, 16)
#define LD_SC1(rs, off) __builtin_amdgcn_raw_buffer_load_b128(rs, off, 0, 16)

template <int GRP>
__device__ __forceinline__ void g1_mainloop(const unsigned ldsb, lds_t lds, f32x4 (&acc)[8][5], cptr (&bp)[5], const size_t ld4, cptr xn, const int wl, const int lane, const int (&ba)[5],
                                            cptr wd, cptr wo) {
    const size_t ldq = (size_t)32 * 3072 * 2, ldg = (size_t)32 * F_ * 2;
    const int aoff = GRP * 8192 + lane * 16;
    const int p0 = wl == 0 ? 0 : 5 * wl + 1;
    const unsigned av0 = p0 * 1024 + lane * 16, av1 = av0 + 4096;
    int so = 0, sl = 3 * G1_STAGE;
    cptr an = xn + 3 * 16384;
    unsigned touch = 0;
    cptr tp = wd + (size_t)(blockIdx.x * 224 + (lane & 15)) * 128;
    bf16x8 A[8], B[5];
#pragma unroll 1
    for (int t = 0; t < 32; ++t) {
        const bool more = t < 29;
        const unsigned lb = ldsb + sl + G1_SA + wl * 1024, la = ldsb + sl + p0 * 1024, rb = ldsb + so;
        if (more) {
            if (GRP == 0) {
                B[0] = lds_tr2_asm<0>(rb + ba[0]); B[1] = lds_tr2_asm<0>(rb + ba[1]);
                glds_v(bp[0], lb);
                B[2] = lds_tr2_asm<0>(rb + ba[2]); B[3] = lds_tr2_asm<0>(rb + ba[3]);
                glds_v(bp[1], lb + 4096);
                B[4] = lds_tr2_asm<0>(rb + ba[4]);
                A[0] = lds_b128(lds + so + aoff); A[1] = lds_b128(lds + so + aoff + 1024);
                glds_v(bp[2], lb + 8192);
                A[2] = lds_b128(lds + so + aoff + 2048); A[3] = lds_b128(lds + so + aoff + 3072); A[4] = lds_b128(lds + so + aoff + 4096);
                glds_v(bp[3], lb + 12288);
                A[5] = lds_b128(lds + so + aoff + 5120); A[6] = lds_b128(lds + so + aoff + 6144); A[7] = lds_b128(lds + so + aoff + 7168);
                glds_v(bp[4], lb + 16384);
                bp[0] += ldq; bp[1] += ldg; bp[2] += ldg; bp[3] += ldg; bp[4] += ld4;
                WAIT_L0(); WAIT_V(10);
            } else if (wl < 3) {
                B[0] = lds_tr2_asm<0>(rb + ba[0]); B[1] = lds_tr2_asm<0>(rb + ba[1]);
                glds_s_sc1_first<0>(av0, an, la);
                B[2] = lds_tr2_asm<0>(rb + ba[2]); B[3] = lds_tr2_asm<0>(rb + ba[3]);
                glds_s_sc1_next<1024>(av0, an);
                B[4] = lds_tr2_asm<0>(rb + ba[4]);
                A[0] = lds_b128(lds + so + aoff); A[1] = lds_b128(lds + so + aoff + 1024);
                glds_s_sc1_next<2048>(av0, an);
                A[2] = lds_b128(lds + so + aoff + 2048); A[3] = lds_b128(lds + so + aoff + 3072);
                glds_s_sc1_next<3072>(av0, an);
                A[4] = lds_b128(lds + so + aoff + 4096); A[5] = lds_b128(lds + so + aoff + 5120);
                glds_s_sc1_first<0>(av1, an, la + 4096);
                A[6] = lds_b128(lds + so + aoff + 6144); A[7] = lds_b128(lds + so + aoff + 7168);
                if (wl == 0) glds_s_sc1_next<1024>(av1, an);
                an += 16384;
                WAIT_L0();
                if (wl == 0) WAIT_V(12); else WAIT_V(10);
            } else {
                if (t < 18) {
                    asm volatile("global_load_dword %0, %1, off" : "+v"(touch) : "v"(tp) : "memory");
                    if (t == 13) tp = wo + (size_t)(blockIdx.x * 64 + (lane & 15)) * 128; else tp += 2048;
                }
#pragma unroll
                for (int n = 0; n < 5; ++n) B[n] = lds_tr2_asm<0>(rb + ba[n]);
#pragma unroll
                for (int m = 0; m < 8; ++m) A[m] = lds_b128(lds + so + aoff + m * 1024);
                WAIT_L0();
            }
        } else {
#pragma unroll
            for (int n = 0; n < 5; ++n) B[n] = lds_tr2_asm<0>(rb + ba[n]);
#pragma unroll
            for (int m = 0; m < 8; ++m) A[m] = lds_b128(lds + so + aoff + m * 1024);
            WAIT_L0(); WAIT_V(0);
        }
        so = (so == 3 * G1_STAGE) ? 0 : so + G1_STAGE;
        sl = (sl == 3 * G1_STAGE) ? 0 : sl + G1_STAGE;
        SCHED(); BAR(); SCHED();
        __builtin_amdgcn_s_setprio(1);
#pragma unroll
        for (int m = 0; m < 8; ++m)
#pragma unroll
            for (int n = 0; n < 5; ++n) { const int nn = (m & 1) ? 4 - n : n; acc[m][nn] = MFMA(B[nn], A[m], acc[m][nn]); }
        __builtin_amdgcn_s_setprio(0);
        SCHED(); BAR(); SCHED();
    }
    asm volatile("" ::"v"(touch));
}

__device__ __forceinline__ void g2_issueB(unsigned bvoff, cptr sb, unsigned l) {
    glds_s(bvoff, sb, l);
    glds_s_o128(bvoff, sb, l + 4096);
}

__global__ __launch_bounds__(512, 2) void k_fused(const u16* __restrict__ x, const u16* __restrict__ wqkv, const u16* __restrict__ wphi, const u16* __restrict__ wo,
                                                  const u16* __restrict__ wg, const u16* __restrict__ wu, const u16* __restrict__ wd,
                                                  unsigned char* __restrict__ ws, u16* __restrict__ out) {
    extern __shared__ __attribute__((aligned(16))) unsigned char shm[];
    lds_t lds = (lds_t)shm;
    const unsigned ldsb = (unsigned)(size_t)lds;
    const int wid = __builtin_amdgcn_readfirstlane(threadIdx.x >> 6);
    const int grp = wid >> 2, wc = wid & 3;
    const int x8 = blockIdx.x & 7, r = blockIdx.x >> 3, ks = x8 >> 1, panel = 4 * (x8 & 1) + (r >> 3), c8 = r & 7, j = 4 * ks + (c8 >> 1), kvt = c8 & 1;
    const __amdgpu_buffer_rsrc_t rs = ws_rsrc(ws);
    STAMP_DECL; STAMP(0);
    u64 tag;
    f32x4 acc[8][5];

    {
        const int lane = threadIdx.x & 63, g = lane >> 4;
        const int row = panel * 256 + (ks * 8 + c8) * 8 + (lane & 7);
        u32x4 xv[4]; u32x2 ev = {0, 0};
#pragma unroll
        for (int i = 0; i < 4; ++i) xv[i] = (u32x4){0, 0, 0, 0};
        {
            const unsigned zero = 0;
            asm volatile("global_load_dwordx2 %0, %1, %2 sc1" : "+v"(ev) : "v"(zero), "s"(ws + WS_FLG + FL_EPOCH * 64) : "memory");
        }
        unsigned tlbt = 0, codet = 0;
        cptr bp[5];
        const size_t ldq = (size_t)32 * 3072 * 2, ldg = (size_t)32 * F_ * 2, ld4 = kvt ? ldq : ldg;
        if (grp == 0) {
            const int gq = (j & 1) ? 7 * j + 3 : 7 * j, gp = (j & 1) ? 7 * j + 1 : 7 * j + 4, gh = (j & 1) ? 7 * j : 7 * j + 6;
            const int krow = 8 * wc + (lane >> 3), chunk = (lane & 7) ^ swzB(krow);
            const size_t rq = (size_t)krow * 3072 * 2 + chunk * 16, rg = (size_t)krow * F_ * 2 + chunk * 16;
            if (!kvt) {
                bp[0] = (cptr)(wqkv + 64 * j) + rq;
                bp[1] = (cptr)(wg + 32 * gq) + rg;      bp[2] = (cptr)(wg + 32 * gq + 64) + rg;
                bp[3] = (cptr)(wu + 32 * gq) + rg;      bp[4] = (cptr)(wu + 32 * gq + 64) + rg;
            } else {
                bp[0] = (cptr)(wqkv + 1024 + 64 * j) + rq;
                bp[1] = (cptr)(wg + 32 * gp) + rg;
                bp[2] = (chunk < 4 ? (cptr)(wg + 32 * gh) : (cptr)(wu + 32 * gh) - 64) + rg;
                bp[3] = (cptr)(wu + 32 * gp) + rg;
                bp[4] = (cptr)(wqkv + 2048 + 64 * j) + rq;
            }
#pragma unroll
            for (int s = 0; s < 3; ++s) {
#pragma unroll
                for (int c = 0; c < 5; ++c) glds_v(bp[c], ldsb + s * G1_STAGE + G1_SA + c * 4096 + wc * 1024);
                bp[0] += ldq; bp[1] += ldg; bp[2] += ldg; bp[3] += ldg; bp[4] += ld4;
            }
            glds_v((cptr)(wphi + (size_t)(2 * j) * 1024 + wc * 512 + lane * 8), ldsb + L_WPHI + wc * 1024);
            {
                const int e = r * 40 + wc * 10 + (lane < 10 ? lane : 9);
                cptr p;
                if (e < 96) p = (cptr)wqkv + (size_t)e * 65536;
                else if (e < 208) p = (cptr)wg + (size_t)(e - 96) * 65536;
                else if (e < 320) p = (cptr)wu + (size_t)(e - 208) * 65536;
                else if (e < 432) p = (cptr)wd + (size_t)(e - 320) * 65536;
                else if (e < 464) p = (cptr)wo + (size_t)(e - 432) * 65536;
                else if (e < 528) p = (cptr)out + (size_t)(e - 464) * 65536;
                else if (e < 592) p = (cptr)x + (size_t)(e - 528) * 65536;
                else p = (cptr)ws + (size_t)min(e - 592, (int)(WS_END / 65536)) * 65536;
                asm volatile("global_load_dword %0, %1, off sc1" : "+v"(tlbt) : "v"(p) : "memory");
            }
            {
                u64 pc; asm volatile("s_getpc_b64 %0" : "=s"(pc));
                const int ln_ = wc * 64 + lane;
                cptr p = (cptr)(pc & ~(u64)0x7f) + (ln_ < CODE_LINES ? ln_ : CODE_LINES - 1) * 128;
                asm volatile("global_load_dword %0, %1, off" : "+v"(codet) : "v"(p) : "memory");
            }
        } else {
#pragma unroll
            for (int c = 0; c < 5; ++c) bp[c] = nullptr;
            const unsigned xo = (unsigned)(row * D_ + (wc * 8 + (lane >> 3)) * 8) * 2u;
            asm volatile("global_load_dwordx4 %0, %4, %5\n\tglobal_load_dwordx4 %1, %4, %5 offset:512\n\tglobal_load_dwordx4 %2, %4, %5 offset:1024\n\tglobal_load_dwordx4 %3, %4, %5 offset:1536\n\t"
                         "s_waitcnt vmcnt(0)" : "+v"(xv[0]), "+v"(xv[1]), "+v"(xv[2]), "+v"(xv[3]) : "v"(xo), "s"(x) : "memory");
        }
        asm volatile("s_waitcnt vmcnt(" XW_CNT ")" : "+v"(ev)::"memory");
        {
            u64 z = (((u64)(unsigned)__builtin_amdgcn_readfirstlane(ev[1]) << 32) | (unsigned)__builtin_amdgcn_readfirstlane(ev[0])) + 0x9E3779B97F4A7C15ull;
            z = (z ^ (z >> 30)) * 0xBF58476D1CE4E5B9ull; z = (z ^ (z >> 27)) * 0x94D049BB133111EBull; tag = z ^ (z >> 31);
        }
        STAMP(1);
        LAS f32x2* red = (LAS f32x2*)(lds + L_MISC);
        float f[32];
        if (grp == 1) {
            float s = 0.f;
#pragma unroll
            for (int i = 0; i < 4; ++i)
#pragma unroll
                for (int k = 0; k < 4; ++k) { f[8 * i + 2 * k] = lo_f(xv[i][k]); f[8 * i + 2 * k + 1] = hi_f(xv[i][k]); }
            { f32x2 s2 = {0.f, 0.f};
#pragma unroll
              for (int i = 0; i < 16; ++i) s2 += (f32x2){f[2 * i], f[2 * i + 1]};
              s = s2[0] + s2[1]; }
            s += ror8(s); s = sum_g(s);
            const float mw = s * (1.0f / 256.0f);
            f32x2 q2 = {0.f, 0.f};
#pragma unroll
            for (int i = 0; i < 16; ++i) { const f32x2 d = (f32x2){f[2 * i], f[2 * i + 1]} - mw; q2 += d * d; }
            float q = q2[0] + q2[1];
            q += ror8(q); q = sum_g(q);
            if (lane < 8) red[wc * 8 + lane] = (f32x2){s, q};
        }
        LBAR();
        if (grp == 1) {
            f32x2 p[4];
#pragma unroll
            for (int w = 0; w < 4; ++w) p[w] = red[w * 8 + (lane & 7)];
            const float mean = ((p[0][0] + p[1][0]) + (p[2][0] + p[3][0])) * (1.0f / D_);
            float m2 = (p[0][1] + p[1][1]) + (p[2][1] + p[3][1]);
#pragma unroll
            for (int w = 0; w < 4; ++w) { const float dm = p[w][0] * (1.0f / 256.0f) - mean; m2 += 256.0f * dm * dm; }
            const float var = m2 * (1.0f / D_);
            const float m = rbf(mean), sd = rbf(sqrtf(rbf(rbf(var) + 1e-5f))), rsd = 1.0f / sd;
#pragma unroll
            for (int it = 0; it < 4; ++it) {
                const int pc = it * 32 + wc * 8 + (lane >> 3), kc = pc >> 2, kq = pc & 3;
                u32x4 o;
#pragma unroll
                for (int i = 0; i < 4; ++i) {
                    const unsigned w = pk_bf16(f[it * 8 + 2 * i] - m, f[it * 8 + 2 * i + 1] - m);
                    const f32x2 a = {lo_f(w), hi_f(w)}, q0 = a * rsd;
                    const f32x2 q1 = __builtin_elementwise_fma(__builtin_elementwise_fma(-q0, (f32x2){sd, sd}, a), (f32x2){rsd, rsd}, q0);
                    o[i] = pk_bf16(q1[0], q1[1]);
                }
                ST_SC1(o, rs, WS_XN + (unsigned)(((panel * 32 + kc) * 16 + ((row >> 4) & 15)) * 1024 + (kq * 16 + (row & 15)) * 16));
            }
            WAIT_V(0);
        }
        LBAR();
        STAMP(2);
        if (threadIdx.x == 256) flag_set(ws, FL_LN + panel * 32 + ks * 8 + c8, tag);
        if (wid == 0) flag_wait(ws, FL_LN + panel * 32, 1, 32, tag, lane);
        LBAR();
        STAMP(3);

        cptr xn = uni((cptr)ws + WS_XN + (size_t)panel * 32 * 16384);
        if (grp == 1 && wc < 3) {
            const int p0 = wc == 0 ? 0 : 5 * wc + 1;
            const unsigned av0 = p0 * 1024 + lane * 16, av1 = av0 + 4096;
#pragma unroll
            for (int s3 = 0; s3 < 3; ++s3) {
                const unsigned la = ldsb + s3 * G1_STAGE + p0 * 1024;
                glds_s_sc1_first<0>(av0, xn + s3 * 16384, la); glds_s_sc1_next<1024>(av0, xn + s3 * 16384);
                glds_s_sc1_next<2048>(av0, xn + s3 * 16384);   glds_s_sc1_next<3072>(av0, xn + s3 * 16384);
                glds_s_sc1_first<0>(av1, xn + s3 * 16384, la + 4096);
                if (wc == 0) glds_s_sc1_next<1024>(av1, xn + s3 * 16384);
            }
            if (wc == 0) WAIT_V(12); else WAIT_V(10);
        }
        int ba[5];
        {
            int cgA, tlA, cgB, tlB;
            if (!kvt) { cgA = 1 + (wc >> 1); tlA = 2 * (wc & 1); cgB = 3 + (wc >> 1); tlB = tlA; }
            else if (wc < 2) { cgA = 1; tlA = 2 * wc; cgB = 3; tlB = 2 * wc; }
            else if (wc == 2) { cgA = 2; tlA = 0; cgB = 2; tlB = 2; }
            else { cgA = 4; tlA = 0; cgB = 4; tlB = 2; }
            const int q4 = (lane >> 2) & 3, p = lane & 3, krow = 8 * g + q4, sw = swzB(krow);
            auto adr = [&](int cg, int tl) { return G1_SA + cg * 4096 + krow * 128 + (((2 * tl + (p >> 1)) ^ sw) << 4) + (p & 1) * 8; };
            ba[0] = adr(cgA, tlA); ba[1] = adr(cgA, tlA + 1); ba[2] = adr(cgB, tlB); ba[3] = adr(cgB, tlB + 1); ba[4] = adr(0, wc);
        }
#pragma unroll
        for (int m = 0; m < 8; ++m)
#pragma unroll
            for (int n = 0; n < 5; ++n) acc[m][n] = (f32x4){0.f, 0.f, 0.f, 0.f};
        if (grp == 0) asm volatile("s_waitcnt vmcnt(0)" : "+v"(tlbt), "+v"(codet)::"memory");
        BAR();
        STAMP(4);
        if (grp == 0) { g1_mainloop<0>(ldsb, lds, acc, bp, ld4, xn, wc, lane, ba, (cptr)wd, (cptr)wo); BAR(); }
        else          { BAR(); g1_mainloop<1>(ldsb, lds, acc, bp, ld4, xn, wc, lane, ba, (cptr)wd, (cptr)wo); }
        STAMP(5);
    }

    int tz = threadIdx.x; asm volatile("" : "+v"(tz));
    const int lane = tz & 63, g = lane >> 4, l15 = lane & 15;
    const unsigned bvoff = (unsigned)((8 * wc + (lane >> 3)) * 2048 + c8 * 256 + (((lane & 7) ^ swzB(8 * wc + (lane >> 3))) << 4));
    const unsigned bl = ldsb + G2_SB + grp * 24576 + wc * 1024;
    cptr sbp = uni((cptr)wd + ((size_t)(28 * ks + grp) << 16));
    auto prefetchB = [&]() { g2_issueB(bvoff, sbp, bl); g2_issueB(bvoff, sbp + 131072, bl + 8192); sbp += 262144; };
    {
        const int gq = (j & 1) ? 7 * j + 3 : 7 * j, gp = (j & 1) ? 7 * j + 1 : 7 * j + 4, gh = (j & 1) ? 7 * j : 7 * j + 6;
        {
            lds_t kp = lds + E_KP + (128 * grp + l15) * 256 + ((((wc >> 1) * 4 + (wc & 1) * 2 + (g >> 1)) ^ l15) << 4) + (g & 1) * 8;
#pragma unroll
            for (int m = 0; m < 8; ++m) *(LAS u32x2*)(kp + m * 4096) = (u32x2){pk_bf16(acc[m][4][0], acc[m][4][1]), pk_bf16(acc[m][4][2], acc[m][4][3])};
        }
        const bool has_h = !(kvt && wc == 3);
        const int hkc = !kvt ? gq + wc : (wc < 2 ? gp + wc : gh);
        const unsigned hoff = WS_H + (unsigned)(((panel * NKC2 + hkc) * 16 + 8 * grp) * 1024 + frag_off(lane));
        auto geglu_c = [&](int m) {
            const f32x2 a0 = gelu_mul2(acc[m][0].lo, acc[m][2].lo), a1 = gelu_mul2(acc[m][0].hi, acc[m][2].hi);
            const f32x2 b0 = gelu_mul2(acc[m][1].lo, acc[m][3].lo), b1 = gelu_mul2(acc[m][1].hi, acc[m][3].hi);
            return pack_frag(a0, a1, b0, b1);
        };
        auto geglu = [&](int m) { ST_SC1(geglu_c(m), rs, hoff + m * 1024); SCHED(); };
        const int q4 = (lane >> 2) & 3, p4 = lane & 3;
        auto logits = [&](u32x4 (&lg)[4]) {
            lds_t wp = lds + L_WPHI + (8 * g + q4) * 64 + p4 * 8;
            lds_t qp = lds + E_KP + (32 * wid + l15) * 256;
#pragma unroll
            for (int hh = 0; hh < 2; ++hh) {
                const bf16x8 wf0 = lds_tr2(wp + hh * 2048, wp + hh * 2048 + 256), wf1 = lds_tr2(wp + hh * 2048 + 32, wp + hh * 2048 + 288);
#pragma unroll
                for (int tb = 0; tb < 2; ++tb) {
                    const bf16x8 qf = lds_b128(qp + tb * 4096 + (((hh * 4 + g) ^ l15) << 4));
                    const f32x4 z4 = {0.f, 0.f, 0.f, 0.f};
                    const f32x4 p0 = MFMA(wf0, qf, z4), p1 = MFMA(wf1, qf, z4);
                    lg[hh * 2 + tb] = (u32x4){pk_bf16(p0[0], p0[1]), pk_bf16(p0[2], p0[3]), pk_bf16(p1[0], p1[1]), pk_bf16(p1[2], p1[3])};
                    asm volatile("" : "+v"(lg[hh * 2 + tb]));
                }
            }
        };
        if (kvt) {
            LBAR();
            if (wc == 3) {
                const int sw = swzP(l15);
                lds_t vp = lds + E_V + (128 * grp + l15) * 128 + (g & 1) * 8;
#pragma unroll
                for (int n = 0; n < 4; ++n) {
                    lds_t vn = vp + (((2 * n + (g >> 1)) ^ sw) << 4);
#pragma unroll
                    for (int m = 0; m < 8; ++m) *(LAS u32x2*)(vn + m * 2048) = (u32x2){pk_bf16(acc[m][n][0], acc[m][n][1]), pk_bf16(acc[m][n][2], acc[m][n][3])};
                }
            }
            STAMP(6);
            u32x4 lg[4], P[4], M[4];
            logits(lg);
            WAIT_L0();
            soft_pm<4>(lg, P, M);
            {
                const int sw = swzF(l15);
                lds_t pb = lds + E_KP + (32 * wid + l15) * 256 + (g & 1) * 8;
#pragma unroll
                for (int u = 0; u < 4; ++u) {
                    const int c0 = (u >> 1) * 8 + (g >> 1), tb = u & 1;
                    *(LAS u32x2*)(pb + tb * 4096 + (((c0 + 0) ^ sw) << 4)) = (u32x2){P[u][0], P[u][1]};
                    *(LAS u32x2*)(pb + tb * 4096 + (((c0 + 2) ^ sw) << 4)) = (u32x2){P[u][2], P[u][3]};
                    *(LAS u32x2*)(pb + tb * 4096 + (((c0 + 4) ^ sw) << 4)) = (u32x2){M[u][0], M[u][1]};
                    *(LAS u32x2*)(pb + tb * 4096 + (((c0 + 6) ^ sw) << 4)) = (u32x2){M[u][2], M[u][3]};
                }
            }
            LBAR();
            STAMP(7);
            {
                const int hh = wid >> 2, ft = wid & 3;
                f32x4 kv0 = {0.f, 0.f, 0.f, 0.f}, kv1 = kv0, zz = kv0;
                const bf16x8 ones = {0x3F80, 0x3F80, 0x3F80, 0x3F80, 0x3F80, 0x3F80, 0x3F80, 0x3F80};
                const int t0 = 8 * g + q4, t1 = t0 + 4, cp = hh * 8 + 2 * ft + (p4 >> 1), cv = hh * 4 + (p4 >> 1), hb8 = (p4 & 1) * 8;
                lds_t pf0 = lds + E_KP + t0 * 256 + ((cp ^ swzF(t0 & 15)) << 4) + hb8, pf1 = lds + E_KP + t1 * 256 + ((cp ^ swzF(t1 & 15)) << 4) + hb8;
                lds_t va0 = lds + E_V + t0 * 128 + ((cv ^ swzP(t0)) << 4) + hb8, va1 = lds + E_V + t1 * 128 + ((cv ^ swzP(t1)) << 4) + hb8;
                lds_t vb0 = lds + E_V + t0 * 128 + (((cv + 2) ^ swzP(t0)) << 4) + hb8, vb1 = lds + E_V + t1 * 128 + (((cv + 2) ^ swzP(t1)) << 4) + hb8;
#pragma unroll
                for (int k8 = 0; k8 < 8; ++k8) {
                    const bf16x8 pf = lds_tr2(pf0 + k8 * 8192, pf1 + k8 * 8192), v0 = lds_tr2(va0 + k8 * 4096, va1 + k8 * 4096), v1 = lds_tr2(vb0 + k8 * 4096, vb1 + k8 * 4096);
                    kv0 = MFMA(pf, v0, kv0); kv1 = MFMA(pf, v1, kv1); zz = MFMA(pf, ones, zz);
                }
                ST_SC1(((u32x4){pk_bf16(kv0[0], kv0[1]), pk_bf16(kv0[2], kv0[3]), pk_bf16(kv1[0], kv1[1]), pk_bf16(kv1[2], kv1[3])}), rs,
                       WS_PART + (unsigned)(((panel * 32 + 2 * j + hh) * 4 + ft) * 1024 + lane * 16));
                if (l15 == 0) ST_SC1(__builtin_bit_cast(u32x4, zz), rs, WS_PZ + (unsigned)(((panel * 32 + 2 * j + hh) * 64 + 16 * ft + 4 * g) * 4));
            }
            SCHED();
            u32x4 h0 = {0, 0, 0, 0};
            if (has_h) h0 = geglu_c(0);
            WAIT_V(0);
            LBAR();
            if (threadIdx.x == 0) flag_set(ws, FL_KV + j * 8 + panel, tag);
            STAMP(8);
            prefetchB();
            if (has_h) {
                ST_SC1(h0, rs, hoff); SCHED();
                geglu(1); geglu(2); geglu(3); geglu(4); geglu(5); geglu(6); geglu(7);
            }
            WAIT_V(0);
            LBAR();
            if (threadIdx.x == 0) flag_set(ws, FL_H + (panel * 4 + ks) * 8 + c8, tag);
            STAMP(9);
        } else {
            u32x4 lg[4], P[4], M[4];
            LBAR();
            logits(lg);
            soft_pm<4>(lg, P, M);
#pragma unroll
            for (int u = 0; u < 4; ++u) asm volatile("" : "+v"(P[u]), "+v"(M[u]));
            SCHED();
            STAMP(6);
            geglu(0); geglu(1); geglu(2); geglu(3); geglu(4); geglu(5);
            STAMP(7);
            if (wid == 0) flag_wait(ws, FL_KV + j * 8, 1, 8, tag, lane);
            LBAR();
            STAMP(8);
            {
                const int hh = wid >> 2, ft = wid & 3;
                f32x4 a0, a1, az;
                u32x4 pw[8]; f32x4 pz[8];
#pragma unroll
                for (int p = 0; p < 8; ++p) {
                    pw[p] = LD_SC1(rs, WS_PART + (unsigned)(((p * 32 + 2 * j + hh) * 4 + ft) * 1024 + lane * 16));
                    pz[p] = __builtin_bit_cast(f32x4, LD_SC1(rs, WS_PZ + (unsigned)(((p * 32 + 2 * j + hh) * 64 + 16 * ft + 4 * g) * 4)));
                }
                SCHED();
                geglu(6); geglu(7);
#pragma unroll
                for (int p = 0; p < 8; ++p) {
                    const f32x4 b0 = {lo_f(pw[p][0]), hi_f(pw[p][0]), lo_f(pw[p][1]), hi_f(pw[p][1])}, b1 = {lo_f(pw[p][2]), hi_f(pw[p][2]), lo_f(pw[p][3]), hi_f(pw[p][3])};
                    if (p == 0) { a0 = b0; a1 = b1; az = pz[0]; } else { a0 += b0; a1 += b1; az += pz[p]; }
                }
                lds_t kb = lds + E_V + ((hh * 2 + (ft >> 1)) * 3) * 1024 + lane * 16 + (ft & 1) * 8;
                *(LAS u32x2*)(kb) = (u32x2){pk_bf16(a0[0], a0[1]), pk_bf16(a0[2], a0[3])};
                *(LAS u32x2*)(kb + 1024) = (u32x2){pk_bf16(a1[0], a1[1]), pk_bf16(a1[2], a1[3])};
                *(LAS u32x2*)(kb + 2048) = (u32x2){pk_bf16(az[0], az[1]), pk_bf16(az[2], az[3])};
            }
            prefetchB();
            WAIT_V(4);
            LBAR();
            if (threadIdx.x == 0) flag_set(ws, FL_H + (panel * 4 + ks) * 8 + c8, tag);
            STAMP(9);
#pragma unroll
            for (int hh = 0; hh < 2; ++hh) {
                bf16x8 kf[2][3];
#pragma unroll
                for (int s = 0; s < 2; ++s)
#pragma unroll
                    for (int d = 0; d < 3; ++d) kf[s][d] = lds_b128(lds + E_V + ((hh * 2 + s) * 3 + d) * 1024 + lane * 16);
#pragma unroll
                for (int tb = 0; tb < 2; ++tb) {
                    const bf16x8 Pf = __builtin_bit_cast(bf16x8, P[hh * 2 + tb]), Mf = __builtin_bit_cast(bf16x8, M[hh * 2 + tb]);
                    const f32x4 z4 = {0.f, 0.f, 0.f, 0.f};
                    const f32x4 n0 = MFMA(kf[1][0], Mf, MFMA(kf[0][0], Pf, z4));
                    const f32x4 n1 = MFMA(kf[1][1], Mf, MFMA(kf[0][1], Pf, z4));
                    const f32x4 dn = MFMA(kf[1][2], Mf, MFMA(kf[0][2], Pf, z4));
                    const float rden = __builtin_amdgcn_rcpf(rbf(rbf(dn[0]) + 1e-6f));
                    ST_SC1(pack_frag(n0.lo * rden, n0.hi * rden, n1.lo * rden, n1.hi * rden), rs, WS_H + (unsigned)(((panel * NKC2 + 112 + 2 * j + hh) * 16 + 2 * wid + tb) * 1024 + frag_off(lane)));
                }
            }
        }
    }
    STAMP(10);
    STAMP_OUT(0, 0, 11);

    STAMP(0);
    if (wid == 0) flag_wait(ws, FL_H + (panel * 4 + ks) * 8, 1, 8, tag, lane);
    LBAR();
    STAMP(1);
    f32x4 c2[8][4];
    {
        const int wm = wc >> 1, wn = wc & 1;
        cptr sa = uni((cptr)ws + WS_H + ((size_t)(panel * NKC2 + 28 * ks + grp) << 14));
        const unsigned avoff = (unsigned)(wc * 4096 + lane * 16), al = ldsb + grp * 49152 + wc * 4096;
        auto issueA = [&](unsigned slot_off) {
            glds_s_sc1_first<0>(avoff, sa, al + slot_off); glds_s_sc1_next<1024>(avoff, sa); glds_s_sc1_next<2048>(avoff, sa); glds_s_sc1_next<3072>(avoff, sa);
            sa += 32768;
        };
        issueA(0); issueA(16384);
        unsigned bb[4];
        {
            const int q4 = (lane >> 2) & 3, p = lane & 3, kr = 8 * g + q4, sw = swzB(kr);
#pragma unroll
            for (int tl = 0; tl < 4; ++tl) bb[tl] = ldsb + G2_SB + grp * 24576 + wn * 4096 + kr * 128 + (((2 * tl + (p >> 1)) ^ sw) << 4) + (p & 1) * 8;
        }
        lds_t ap = lds + grp * 49152 + wm * 8192 + lane * 16;
#pragma unroll
        for (int m = 0; m < 8; ++m)
#pragma unroll
            for (int n = 0; n < 4; ++n) c2[m][n] = (f32x4){0.f, 0.f, 0.f, 0.f};
        u32x2 ofl = {0, 0};
        const unsigned fo = (FL_O + (panel * 4 + ks) * 4 + (lane & 3)) * 64;
        WAIT_V(4);
        LBAR();
        if (!kvt && threadIdx.x == 0) flag_set(ws, FL_O + (panel * 4 + ks) * 4 + (c8 >> 1), tag);
        if (grp == 1) BAR();
        STAMP(2);
        auto step = [&](auto uc, const int t) {
            constexpr int U = decltype(uc)::value, W = (U + 2) % 3;
            bf16x8 A[8], B[4];
#define FST(x)
            FST(f0);
            if (U == 0) {
                if (t == 12) {
                    asm volatile("" : "+v"(ofl));
                    const u64 v = ((u64)ofl[1] << 32) | ofl[0];
                    if (!__all(v == tag)) flag_wait(ws, FL_O + (panel * 4 + ks) * 4, 1, 4, tag, lane);
                    sa = uni((cptr)ws + WS_H + ((size_t)(panel * NKC2 + 112 + 8 * ks + grp) << 14));
                    sbp = uni((cptr)wo + ((size_t)(8 * ks + grp) << 16));
                }
                asm volatile("s_cmp_lg_u32 %3, 9\n\ts_cbranch_scc1 .Lofl_%=\n\tglobal_load_dwordx2 %0, %1, %2 sc1\n.Lofl_%=:" : "+v"(ofl) : "v"(fo), "s"(ws + WS_FLG), "s"(t) : "scc", "memory");
            }
            const bool more = U == 0 || t < 16;
            B[0] = lds_tr2_asm<U * 8192>(bb[0]); B[1] = lds_tr2_asm<U * 8192>(bb[1]);
            if (more) glds_s(bvoff, sbp, bl + W * 8192);
            B[2] = lds_tr2_asm<U * 8192>(bb[2]); B[3] = lds_tr2_asm<U * 8192>(bb[3]);
            if (more) { glds_s_o128(bvoff, sbp, bl + W * 8192 + 4096); sbp += 131072; }
            A[0] = lds_b128(ap + U * 16384); A[1] = lds_b128(ap + U * 16384 + 1024);
            if (more) { glds_s_sc1_first<0>(avoff, sa, al + W * 16384); glds_s_sc1_next<1024>(avoff, sa); }
            A[2] = lds_b128(ap + U * 16384 + 2048); A[3] = lds_b128(ap + U * 16384 + 3072); A[4] = lds_b128(ap + U * 16384 + 4096);
            if (more) { glds_s_sc1_next<2048>(avoff, sa); glds_s_sc1_next<3072>(avoff, sa); sa += 32768; }
            A[5] = lds_b128(ap + U * 16384 + 5120); A[6] = lds_b128(ap + U * 16384 + 6144); A[7] = lds_b128(ap + U * 16384 + 7168);
            FST(f1);
            WAIT_L0();
            FST(f2);
            if (!more) WAIT_V(0);
            SCHED(); BAR(); SCHED();
            FST(f3);
            __builtin_amdgcn_s_setprio(1);
#pragma unroll
            for (int m = 0; m < 8; ++m)
#pragma unroll
                for (int n = 0; n < 4; ++n) { const int nn = (m & 1) ? 3 - n : n; c2[m][nn] = MFMA(B[nn], A[m], c2[m][nn]); }
            __builtin_amdgcn_s_setprio(0);
            FST(f4);
            WAIT_V(6);
            FST(f5);
            SCHED(); BAR(); SCHED();
        };
#pragma unroll 1
        for (int t = 0; t < 18; t += 3) { step(std::integral_constant<int, 0>{}, t); step(std::integral_constant<int, 1>{}, t + 1); step(std::integral_constant<int, 2>{}, t + 2); }
        if (grp == 0) BAR();
    }
    STAMP(3);
    f32x4 c3[4][4];
    {
        int tz3 = threadIdx.x; asm volatile("" : "+v"(tz3));
        lds_t mine = lds + wid * 16384 + (tz3 & 63) * 16, theirs = lds + (wid ^ 4) * 16384 + (tz3 & 63) * 16;
        if (grp == 0) {
#pragma unroll
            for (int m = 0; m < 4; ++m)
#pragma unroll
                for (int n = 0; n < 4; ++n) { *(LAS f32x4*)(mine + (m * 4 + n) * 1024) = c2[4 + m][n]; c3[m][n] = c2[m][n]; }
        } else {
#pragma unroll
            for (int m = 0; m < 4; ++m)
#pragma unroll
                for (int n = 0; n < 4; ++n) { *(LAS f32x4*)(mine + (m * 4 + n) * 1024) = c2[m][n]; c3[m][n] = c2[4 + m][n]; }
        }
        LBAR();
#pragma unroll
        for (int m = 0; m < 4; ++m)
#pragma unroll
            for (int n = 0; n < 4; ++n) c3[m][n] += *(const LAS f32x4*)(theirs + (m * 4 + n) * 1024);
    }

    {
        int tz2 = threadIdx.x; asm volatile("" : "+v"(tz2));
        const int ln = tz2 & 63, tile = panel * 8 + c8, qd = 2 * ((wid & 3) >> 1) + grp, wn = wid & 1;
        if (qd != ks) {
            const unsigned base = WS_XCH + (unsigned)((((tile * 4 + ks) * 8 + wid) * 8) * 1024 + ln * 16);
#pragma unroll
            for (int m = 0; m < 4; ++m)
#pragma unroll
                for (int n2 = 0; n2 < 2; ++n2) {
                    const f32x4 a = c3[m][2 * n2], b = c3[m][2 * n2 + 1];
                    ST_SC1(((u32x4){pk_bf16(a[0], a[1]), pk_bf16(a[2], a[3]), pk_bf16(b[0], b[1]), pk_bf16(b[2], b[3])}), rs, base + (m * 2 + n2) * 1024);
                }
            STAMP(4);
            WAIT_V(0);
            if (ln == 0) flag_set(ws, FL_X + (tile * 4 + ks) * 8 + wid, tag);
            STAMP(5);
        } else {
            const int row0 = panel * 256 + qd * 64 + (ln & 15), col0 = c8 * 128 + wn * 64 + 8 * frag_kq(ln);
            const __amdgpu_buffer_rsrc_t ors = __builtin_amdgcn_make_buffer_rsrc((void*)out, 0, S_ * D_ * 2, 0x00020000);
            u32x4 xr[4][2];
#pragma unroll
            for (int m = 0; m < 4; ++m)
#pragma unroll
                for (int n2 = 0; n2 < 2; ++n2) xr[m][n2] = *(const u32x4*)(x + (size_t)(row0 + 16 * m) * D_ + col0 + 32 * n2);
            {
                const int src = (ks + 1 + (ln < 3 ? ln : 0)) & 3;
                GAS u64* p = flagp(ws, FL_X + (tile * 4 + src) * 8 + wid);
#pragma unroll 1
                for (unsigned it = 0; it < (1u << 21); ++it) {
                    const u64 v = __hip_atomic_load(p, __ATOMIC_RELAXED, __HIP_MEMORY_SCOPE_AGENT);
                    if (__all(v == tag)) break;
                    __builtin_amdgcn_s_sleep(1);
                }
                asm volatile("" ::: "memory");
            }
            STAMP(4);
#pragma unroll
            for (int s = 0; s < 4; ++s) {
                if (s == ks) continue;
                const unsigned base = WS_XCH + (unsigned)((((tile * 4 + s) * 8 + wid) * 8) * 1024 + ln * 16);
#pragma unroll
                for (int m = 0; m < 4; ++m)
#pragma unroll
                    for (int n2 = 0; n2 < 2; ++n2) {
                        const u32x4 w = LD_SC1(rs, base + (m * 2 + n2) * 1024);
                        c3[m][2 * n2] += (f32x4){lo_f(w[0]), hi_f(w[0]), lo_f(w[1]), hi_f(w[1])};
                        c3[m][2 * n2 + 1] += (f32x4){lo_f(w[2]), hi_f(w[2]), lo_f(w[3]), hi_f(w[3])};
                    }
            }
            STAMP(5);
#pragma unroll
            for (int m = 0; m < 4; ++m)
#pragma unroll
                for (int n2 = 0; n2 < 2; ++n2) {
                    float a[4], b[4];
#pragma unroll
                    for (int e = 0; e < 4; ++e) {
                        auto rr = __builtin_amdgcn_permlane16_swap(__float_as_uint(c3[m][2 * n2][e]), __float_as_uint(c3[m][2 * n2 + 1][e]), false, false);
                        a[e] = __uint_as_float(rr[0]); b[e] = __uint_as_float(rr[1]);
                    }
                    const u32x4 xv = xr[m][n2];
                    u32x4 o;
                    o[0] = pk_bf16(a[0] + lo_f(xv[0]), a[1] + hi_f(xv[0])); o[1] = pk_bf16(a[2] + lo_f(xv[1]), a[3] + hi_f(xv[1]));
                    o[2] = pk_bf16(b[0] + lo_f(xv[2]), b[1] + hi_f(xv[2])); o[3] = pk_bf16(b[2] + lo_f(xv[3]), b[3] + hi_f(xv[3]));
                    __builtin_amdgcn_raw_buffer_store_b128(o, ors, (unsigned)(((row0 + 16 * m) * D_ + col0 + 32 * n2) * 2), 0, 16);
                }
        }
        const int ln2 = ln;
        if (blockIdx.x == 0 && wid == 0) {
#pragma unroll 1
            for (int i = 0; i < 4; ++i) flag_wait(ws, FL_LN + 64 * i, 1, 64, tag, ln2);
            if (ln2 == 0) flag_set(ws, FL_EPOCH, tag);
        }
    }
    STAMP(6);
    STAMP_OUT(1, 0, 7);
}

extern "C" void kernel_launch(void* const* d_in, const int* in_sizes, int n_in, void* d_out, int out_size, void* d_ws, size_t ws_size, hipStream_t stream) {
    const u16* x = (const u16*)d_in[0]; const u16* wqkv = (const u16*)d_in[1]; const u16* wphi = (const u16*)d_in[2]; const u16* wo = (const u16*)d_in[3];
    const u16* wg = (const u16*)d_in[5]; const u16* wu = (const u16*)d_in[6]; const u16* wd = (const u16*)d_in[7];
    (void)hipFuncSetAttribute((const void*)k_fused, hipFuncAttributeMaxDynamicSharedMemorySize, L_TOTAL);
    k_fused<<<256, 512, L_TOTAL, stream>>>(x, wqkv, wphi, wo, wg, wu, wd, (unsigned char*)d_ws, (u16*)d_out);
}
```

```cpp
#include <hip/hip_runtime.h>
#include <type_traits>

typedef unsigned short u16;
typedef unsigned long long u64;
typedef short bf16x8 __attribute__((ext_vector_type(8)));
typedef short s16x4 __attribute__((ext_vector_type(4)));
typedef float f32x4 __attribute__((ext_vector_type(4)));
typedef float f32x2 __attribute__((ext_vector_type(2)));
typedef unsigned u32x4 __attribute__((ext_vector_type(4)));
typedef unsigned u32x2 __attribute__((ext_vector_type(2)));
#define LAS __attribute__((address_space(3)))
#define GAS __attribute__((address_space(1)))
typedef LAS unsigned char* lds_t;
typedef const unsigned char* cptr;

constexpr int S_ = 2048, D_ = 1024, F_ = 3584;
constexpr int CODE_LINES = 256;
constexpr int NKC2 = 144;

constexpr unsigned WS_XN   = 0;
constexpr unsigned WS_H    = WS_XN + S_ * D_ * 2;
constexpr unsigned WS_PART = WS_H + 8 * NKC2 * 16 * 1024;
constexpr unsigned WS_PZ   = WS_PART + 8 * 32 * 4 * 1024;
constexpr unsigned WS_XCH  = WS_PZ + 8 * 32 * 64 * 4;
constexpr unsigned WS_FLG  = WS_XCH + 64 * 4 * 8 * 8 * 1024;
constexpr unsigned FL_LN = 0, FL_KV = FL_LN + 256, FL_H = FL_KV + 128, FL_O = FL_H + 256, FL_X = FL_O + 128, FL_EPOCH = FL_X + 2048, FL_N = FL_EPOCH + 1;
constexpr unsigned WS_END  = WS_FLG + FL_N * 64;

constexpr int G1_SA = 16384, G1_STAGE = 36864;
constexpr int L_MISC = 158720, L_WPHI = 159744, L_TOTAL = 163840;
constexpr int E_KP = 0, E_V = 65536;
constexpr int G2_SB = 98304;

__device__ __forceinline__ unsigned pk_bf16(float lo, float hi) {
    typedef __bf16 bf2 __attribute__((ext_vector_type(2)));
    bf2 r = {(__bf16)lo, (__bf16)hi};
    return __builtin_bit_cast(unsigned, r);
}
__device__ __forceinline__ float rbf(float x) { return __uint_as_float(pk_bf16(x, x) & 0xffff0000u); }
__device__ __forceinline__ float lo_f(unsigned w) { return __uint_as_float(w << 16); }
__device__ __forceinline__ float hi_f(unsigned w) { return __uint_as_float(w & 0xffff0000u); }

#define STAMP_DECL
#define STAMP(i)
#define STAMP_ARG
#define STAMP_PASS
#define STAMP_OUT(kern, lo, n)

#define XW_CNT "18"
#define WAIT_V(n) asm volatile("s_waitcnt vmcnt(" #n ")" ::: "memory")
#define WAIT_L0() do { asm volatile("" ::: "memory"); __builtin_amdgcn_s_waitcnt(0xC07F); asm volatile("" ::: "memory"); } while (0)
#define BAR() __builtin_amdgcn_s_barrier()
#define LBAR() asm volatile("s_waitcnt lgkmcnt(0)\n\ts_barrier" ::: "memory")
#define SCHED() __builtin_amdgcn_sched_barrier(0)
#define MFMA(a, b, c) __builtin_amdgcn_mfma_f32_16x16x32_bf16(a, b, c, 0, 0, 0)

__device__ __forceinline__ cptr uni(cptr p) {
    const u64 v = (u64)p;
    return (cptr)(((u64)(unsigned)__builtin_amdgcn_readfirstlane((unsigned)(v >> 32)) << 32) | (unsigned)__builtin_amdgcn_readfirstlane((unsigned)v));
}
__device__ __forceinline__ void glds_v(cptr g, unsigned l) {
    asm volatile("s_mov_b32 m0, %1\n\ts_nop 0\n\tglobal_load_lds_dwordx4 %0, off" ::"v"(g), "s"(l) : "memory");
}
__device__ __forceinline__ void glds_s(unsigned voff, cptr sb, unsigned l) {
    asm volatile("s_nop 2\n\ts_mov_b32 m0, %2\n\ts_nop 0\n\tglobal_load_lds_dwordx4 %0, %1" ::"v"(voff), "s"(sb), "s"(l) : "memory");
}
__device__ __forceinline__ void glds_s_o128(unsigned voff, cptr sb, unsigned l) {
    asm volatile("s_mov_b32 m0, %2\n\ts_nop 0\n\tglobal_load_lds_dwordx4 %0, %1 offset:128" ::"v"(voff), "s"(sb), "s"(l - 128u) : "memory");
}
template <int OFF> __device__ __forceinline__ void glds_s_sc1_first(unsigned voff, cptr sb, unsigned l) {
    asm volatile("s_nop 2\n\ts_mov_b32 m0, %2\n\ts_nop 0\n\tglobal_load_lds_dwordx4 %0, %1 offset:%3 sc1" ::"v"(voff), "s"(sb), "s"(l), "i"(OFF) : "memory");
}
template <int OFF> __device__ __forceinline__ void glds_s_sc1_next(unsigned voff, cptr sb) {
    asm volatile("global_load_lds_dwordx4 %0, %1 offset:%2 sc1" ::"v"(voff), "s"(sb), "i"(OFF) : "memory");
}
template <int OFF> __device__ __forceinline__ void gld_sc1(u32x4& d, unsigned voff, cptr sb) {
    asm volatile("global_load_dwordx4 %0, %1, %2 offset:%3 sc1" : "+v"(d) : "v"(voff), "s"(sb), "i"(OFF) : "memory");
}
__device__ __forceinline__ bf16x8 lds_b128(lds_t p) { return *(const LAS bf16x8*)p; }
__device__ __forceinline__ bf16x8 lds_tr2(lds_t p0, lds_t p1) {
    s16x4 a = __builtin_amdgcn_ds_read_tr16_b64_v4i16((LAS s16x4*)p0);
    s16x4 b = __builtin_amdgcn_ds_read_tr16_b64_v4i16((LAS s16x4*)p1);
    return __builtin_shufflevector(a, b, 0, 1, 2, 3, 4, 5, 6, 7);
}
template <int OFF> __device__ __forceinline__ bf16x8 lds_tr2_asm(unsigned addr) {
    s16x4 a, b;
    asm volatile("ds_read_b64_tr_b16 %0, %2 offset:%3\n\tds_read_b64_tr_b16 %1, %2 offset:%4" : "=&v"(a), "=&v"(b) : "v"(addr), "i"(OFF), "i"(OFF + 512) : "memory");
    return __builtin_shufflevector(a, b, 0, 1, 2, 3, 4, 5, 6, 7);
}

__device__ __forceinline__ float x16(float v, float& o) { auto r = __builtin_amdgcn_permlane16_swap(__float_as_uint(v), __float_as_uint(v), false, false); o = __uint_as_float(r[1]); return __uint_as_float(r[0]); }
__device__ __forceinline__ float x32(float v, float& o) { auto r = __builtin_amdgcn_permlane32_swap(__float_as_uint(v), __float_as_uint(v), false, false); o = __uint_as_float(r[1]); return __uint_as_float(r[0]); }
__device__ __forceinline__ float sum_g(float v) { float o, a = x16(v, o); v = a + o; a = x32(v, o); return a + o; }
__device__ __forceinline__ float max_g(float v) { float o, a = x16(v, o); v = fmaxf(a, o); a = x32(v, o); return fmaxf(a, o); }
__device__ __forceinline__ float min_g(float v) { float o, a = x16(v, o); v = fminf(a, o); a = x32(v, o); return fminf(a, o); }
__device__ __forceinline__ float ror8(float v) { return __int_as_float(__builtin_amdgcn_update_dpp(0, __float_as_int(v), 0x128, 0xf, 0xf, false)); }

__device__ __forceinline__ int swzB(int k) { return (((k >> 1) & 1) << 1) | (((k >> 3) & 1) << 2); }
__device__ __forceinline__ int swzP(int k) { return ((k >> 2) & 1) | (((k >> 1) & 1) << 1) | (((k >> 3) & 1) << 2); }
__device__ __forceinline__ int swzF(int k) { return ((k >> 2) & 1) | ((k & 3) << 1) | (((k >> 3) & 1) << 3); }

__device__ __forceinline__ f32x2 gelu_mul2(f32x2 g, f32x2 u) {
    constexpr float C0 = -2.0f * 0.7978845608028654f * 1.4426950408889634f, C1 = C0 * 0.044715f;
    const f32x2 a = g * (g * g * C1 + C0);
    const f32x2 d = (f32x2){__builtin_amdgcn_exp2f(a[0]), __builtin_amdgcn_exp2f(a[1])} + 1.0f;
    return (g * u) * (f32x2){__builtin_amdgcn_rcpf(d[0]), __builtin_amdgcn_rcpf(d[1])};
}
__device__ __forceinline__ u32x4 pack_frag(const f32x2 a0, const f32x2 a1, const f32x2 b0, const f32x2 b1) {
    unsigned x0 = pk_bf16(a0[0], a0[1]), x1 = pk_bf16(a1[0], a1[1]), y0 = pk_bf16(b0[0], b0[1]), y1 = pk_bf16(b1[0], b1[1]);
    auto r0 = __builtin_amdgcn_permlane16_swap(x0, y0, false, false);
    auto r1 = __builtin_amdgcn_permlane16_swap(x1, y1, false, false);
    u32x4 o; o[0] = r0[0]; o[1] = r1[0]; o[2] = r0[1]; o[3] = r1[1];
    return o;
}
__device__ __forceinline__ int frag_kq(int lane) { const int g = lane >> 4; return ((g & 1) << 1) | (g >> 1); }
__device__ __forceinline__ int frag_off(int lane) { return (frag_kq(lane) * 16 + (lane & 15)) * 16; }

__device__ __forceinline__ float max3(float a, float b, float c) { float r; asm("v_max3_f32 %0, %1, %2, %3" : "=v"(r) : "v"(a), "v"(b), "v"(c)); return r; }
__device__ __forceinline__ float min3(float a, float b, float c) { float r; asm("v_min3_f32 %0, %1, %2, %3" : "=v"(r) : "v"(a), "v"(b), "v"(c)); return r; }
__device__ __forceinline__ float max2(float a, float b) { float r; asm("v_max_f32 %0, %1, %2" : "=v"(r) : "v"(a), "v"(b)); return r; }
template <int NU, class Op> __device__ __forceinline__ void red2_g(float (&a)[NU], float (&b)[NU], Op op) {
    float z[NU], y[NU];
#pragma unroll
    for (int u = 0; u < NU; ++u) {
        auto r = __builtin_amdgcn_permlane16_swap(__float_as_uint(a[u]), __float_as_uint(b[u]), false, false);
        z[u] = op(__uint_as_float(r[0]), __uint_as_float(r[1]));
    }
#pragma unroll
    for (int u = 0; u < NU; ++u) {
        auto q = __builtin_amdgcn_permlane32_swap(__float_as_uint(z[u]), __float_as_uint(z[u]), false, false);
        y[u] = op(__uint_as_float(q[0]), __uint_as_float(q[1]));
    }
#pragma unroll
    for (int u = 0; u < NU; ++u) {
        auto w = __builtin_amdgcn_permlane16_swap(__float_as_uint(y[u]), __float_as_uint(y[u]), false, false);
        a[u] = __uint_as_float(w[0]); b[u] = __uint_as_float(w[1]);
    }
}
template <int NU> __device__ __forceinline__ void soft_pm(const u32x4 (&w)[NU], u32x4 (&P)[NU], u32x4 (&M)[NU]) {
    constexpr float L2E = 1.4426950408889634f;
    f32x2 pv[NU][4]; float mx[NU], nm[NU];
#pragma unroll
    for (int u = 0; u < NU; ++u) {
#pragma unroll
        for (int i = 0; i < 4; ++i) pv[u][i] = (f32x2){lo_f(w[u][i]), hi_f(w[u][i])};
        mx[u] = max3(max3(max3(pv[u][0][0], pv[u][0][1], pv[u][1][0]), pv[u][1][1], pv[u][2][0]), max2(pv[u][2][1], pv[u][3][0]), pv[u][3][1]);
        nm[u] = -min3(min3(min3(pv[u][0][0], pv[u][0][1], pv[u][1][0]), pv[u][1][1], pv[u][2][0]), min3(pv[u][2][1], pv[u][3][0], pv[u][3][1]), pv[u][3][1]);
    }
    red2_g<NU>(mx, nm, [](float a, float b) { return max2(a, b); });
    f32x2 pp[NU][4], pm[NU][4]; float sp[NU], sm[NU];
#pragma unroll
    for (int u = 0; u < NU; ++u) {
        const float cp = -mx[u] * L2E, cm = -nm[u] * L2E;
#pragma unroll
        for (int i = 0; i < 4; ++i) {
            const f32x2 a = pv[u][i] * L2E + cp, b = pv[u][i] * (-L2E) + cm;
            pp[u][i] = (f32x2){__builtin_amdgcn_exp2f(a[0]), __builtin_amdgcn_exp2f(a[1])};
            pm[u][i] = (f32x2){__builtin_amdgcn_exp2f(b[0]), __builtin_amdgcn_exp2f(b[1])};
        }
        const f32x2 s2 = (pp[u][0] + pp[u][1]) + (pp[u][2] + pp[u][3]), m2 = (pm[u][0] + pm[u][1]) + (pm[u][2] + pm[u][3]);
        sp[u] = s2[0] + s2[1]; sm[u] = m2[0] + m2[1];
    }
    red2_g<NU>(sp, sm, [](float a, float b) { return a + b; });
#pragma unroll
    for (int u = 0; u < NU; ++u) {
        const float rp = __builtin_amdgcn_rcpf(sp[u]), rm = __builtin_amdgcn_rcpf(sm[u]);
#pragma unroll
        for (int i = 0; i < 4; ++i) { const f32x2 a = pp[u][i] * rp, b = pm[u][i] * rm; P[u][i] = pk_bf16(a[0], a[1]); M[u][i] = pk_bf16(b[0], b[1]); }
    }
}

__device__ __forceinline__ GAS u64* flagp(unsigned char* ws, unsigned i) { return (GAS u64*)(ws + WS_FLG + (size_t)i * 64); }
__device__ __forceinline__ void flag_set(unsigned char* ws, unsigned i, u64 tag) { __hip_atomic_store(flagp(ws, i), tag, __ATOMIC_RELAXED, __HIP_MEMORY_SCOPE_AGENT); }
__device__ __forceinline__ void flag_wait(unsigned char* ws, unsigned i0, unsigned st, int n, u64 tag, int lane) {
    GAS u64* p = flagp(ws, i0 + st * (unsigned)(lane < n ? lane : 0));
#pragma unroll 1
    for (unsigned it = 0; it < (1u << 21); ++it) {
        const u64 v = __hip_atomic_load(p, __ATOMIC_RELAXED, __HIP_MEMORY_SCOPE_AGENT);
        if (__all(v == tag)) break;
        __builtin_amdgcn_s_sleep(1);
    }
    asm volatile("" ::: "memory");
}
__device__ __forceinline__ __amdgpu_buffer_rsrc_t ws_rsrc(unsigned char* ws) { return __builtin_amdgcn_make_buffer_rsrc((void*)ws, 0, WS_END, 0x00020000); }
#define ST_SC1(v, rs, off) __builtin_amdgcn_raw_buffer_store_b128(v, rs, off, 0, 16)
#define LD_SC1(rs, off) __builtin_amdgcn_raw_buffer_load_b128(rs, off, 0, 16)

template <int GRP>
__device__ __forceinline__ void g1_mainloop(const unsigned ldsb, lds_t lds, f32x4 (&acc)[8][5], cptr (&bp)[5], const size_t ld4, cptr xn, const int wl, const int lane, const int (&ba)[5],
                                            cptr wd, cptr wo) {
    const size_t ldq = (size_t)32 * 3072 * 2, ldg = (size_t)32 * F_ * 2;
    const int aoff = GRP * 8192 + lane * 16;
    const int p0 = wl == 0 ? 0 : 5 * wl + 1;
    const unsigned av0 = p0 * 1024 + lane * 16, av1 = av0 + 4096;
    int so = 0, sl = 3 * G1_STAGE;
    cptr an = xn + 3 * 16384;
    unsigned touch = 0;
    cptr tp = wd + (size_t)(blockIdx.x * 224 + (lane & 15)) * 128;
    bf16x8 A[8], B[5];
#pragma unroll 1
    for (int t = 0; t < 32; ++t) {
        const bool more = t < 29;
        const unsigned lb = ldsb + sl + G1_SA + wl * 1024, la = ldsb + sl + p0 * 1024, rb = ldsb + so;
        if (more) {
            if (GRP == 0) {
                B[0] = lds_tr2_asm<0>(rb + ba[0]); B[1] = lds_tr2_asm<0>(rb + ba[1]);
                glds_v(bp[0], lb);
                B[2] = lds_tr2_asm<0>(rb + ba[2]); B[3] = lds_tr2_asm<0>(rb + ba[3]);
                glds_v(bp[1], lb + 4096);
                B[4] = lds_tr2_asm<0>(rb + ba[4]);
                A[0] = lds_b128(lds + so + aoff); A[1] = lds_b128(lds + so + aoff + 1024);
                glds_v(bp[2], lb + 8192);
                A[2] = lds_b128(lds + so + aoff + 2048); A[3] = lds_b128(lds + so + aoff + 3072); A[4] = lds_b128(lds + so + aoff + 4096);
                glds_v(bp[3], lb + 12288);
                A[5] = lds_b128(lds + so + aoff + 5120); A[6] = lds_b128(lds + so + aoff + 6144); A[7] = lds_b128(lds + so + aoff + 7168);
                glds_v(bp[4], lb + 16384);
                bp[0] += ldq; bp[1] += ldg; bp[2] += ldg; bp[3] += ldg; bp[4] += ld4;
                WAIT_L0(); WAIT_V(10);
            } else if (wl < 3) {
                B[0] = lds_tr2_asm<0>(rb + ba[0]); B[1] = lds_tr2_asm<0>(rb + ba[1]);
                glds_s_sc1_first<0>(av0, an, la);
                B[2] = lds_tr2_asm<0>(rb + ba[2]); B[3] = lds_tr2_asm<0>(rb + ba[3]);
                glds_s_sc1_next<1024>(av0, an);
                B[4] = lds_tr2_asm<0>(rb + ba[4]);
                A[0] = lds_b128(lds + so + aoff); A[1] = lds_b128(lds + so + aoff + 1024);
                glds_s_sc1_next<2048>(av0, an);
                A[2] = lds_b128(lds + so + aoff + 2048); A[3] = lds_b128(lds + so + aoff + 3072);
                glds_s_sc1_next<3072>(av0, an);
                A[4] = lds_b128(lds + so + aoff + 4096); A[5] = lds_b128(lds + so + aoff + 5120);
                glds_s_sc1_first<0>(av1, an, la + 4096);
                A[6] = lds_b128(lds + so + aoff + 6144); A[7] = lds_b128(lds + so + aoff + 7168);
                if (wl == 0) glds_s_sc1_next<1024>(av1, an);
                an += 16384;
                WAIT_L0();
                if (wl == 0) WAIT_V(12); else WAIT_V(10);
            } else {
                if (t < 18) {
                    asm volatile("global_load_dword %0, %1, off" : "+v"(touch) : "v"(tp) : "memory");
                    if (t == 13) tp = wo + (size_t)(blockIdx.x * 64 + (lane & 15)) * 128; else tp += 2048;
                }
#pragma unroll
                for (int n = 0; n < 5; ++n) B[n] = lds_tr2_asm<0>(rb + ba[n]);
#pragma unroll
                for (int m = 0; m < 8; ++m) A[m] = lds_b128(lds + so + aoff + m * 1024);
                WAIT_L0();
            }
        } else {
#pragma unroll
            for (int n = 0; n < 5; ++n) B[n] = lds_tr2_asm<0>(rb + ba[n]);
#pragma unroll
            for (int m = 0; m < 8; ++m) A[m] = lds_b128(lds + so + aoff + m * 1024);
            WAIT_L0(); WAIT_V(0);
        }
        so = (so == 3 * G1_STAGE) ? 0 : so + G1_STAGE;
        sl = (sl == 3 * G1_STAGE) ? 0 : sl + G1_STAGE;
        SCHED(); BAR(); SCHED();
        __builtin_amdgcn_s_setprio(1);
#pragma unroll
        for (int m = 0; m < 8; ++m)
#pragma unroll
            for (int n = 0; n < 5; ++n) { const int nn = (m & 1) ? 4 - n : n; acc[m][nn] = MFMA(B[nn], A[m], acc[m][nn]); }
        __builtin_amdgcn_s_setprio(0);
        SCHED(); BAR(); SCHED();
    }
    asm volatile("" ::"v"(touch));
}

__device__ __forceinline__ void g2_issueB(unsigned bvoff, cptr sb, unsigned l) {
    glds_s(bvoff, sb, l);
    glds_s_o128(bvoff, sb, l + 4096);
}

__global__ __launch_bounds__(512, 2) void k_fused(const u16* __restrict__ x, const u16* __restrict__ wqkv, const u16* __restrict__ wphi, const u16* __restrict__ wo,
                                                  const u16* __restrict__ wg, const u16* __restrict__ wu, const u16* __restrict__ wd,
                                                  unsigned char* __restrict__ ws, u16* __restrict__ out) {
    extern __shared__ __attribute__((aligned(16))) unsigned char shm[];
    lds_t lds = (lds_t)shm;
    const unsigned ldsb = (unsigned)(size_t)lds;
    const int wid = __builtin_amdgcn_readfirstlane(threadIdx.x >> 6);
    const int grp = wid >> 2, wc = wid & 3;
    const int x8 = blockIdx.x & 7, r = blockIdx.x >> 3, ks = x8 >> 1, panel = 4 * (x8 & 1) + (r >> 3), c8 = r & 7, j = 4 * ks + (c8 >> 1), kvt = c8 & 1;
    const __amdgpu_buffer_rsrc_t rs = ws_rsrc(ws);
    STAMP_DECL; STAMP(0);
    u64 tag;
    f32x4 acc[8][5];

    {
        const int lane = threadIdx.x & 63, g = lane >> 4;
        const int row = panel * 256 + (ks * 8 + c8) * 8 + (lane & 7);
        u32x4 xv[4]; u32x2 ev = {0, 0};
#pragma unroll
        for (int i = 0; i < 4; ++i) xv[i] = (u32x4){0, 0, 0, 0};
        {
            const unsigned zero = 0;
            asm volatile("global_load_dwordx2 %0, %1, %2 sc1" : "+v"(ev) : "v"(zero), "s"(ws + WS_FLG + FL_EPOCH * 64) : "memory");
        }
        unsigned tlbt = 0, codet = 0;
        cptr bp[5];
        const size_t ldq = (size_t)32 * 3072 * 2, ldg = (size_t)32 * F_ * 2, ld4 = kvt ? ldq : ldg;
        if (grp == 0) {
            const int gq = (j & 1) ? 7 * j + 3 : 7 * j, gp = (j & 1) ? 7 * j + 1 : 7 * j + 4, gh = (j & 1) ? 7 * j : 7 * j + 6;
            const int krow = 8 * wc + (lane >> 3), chunk = (lane & 7) ^ swzB(krow);
            const size_t rq = (size_t)krow * 3072 * 2 + chunk * 16, rg = (size_t)krow * F_ * 2 + chunk * 16;
            if (!kvt) {
                bp[0] = (cptr)(wqkv + 64 * j) + rq;
                bp[1] = (cptr)(wg + 32 * gq) + rg;      bp[2] = (cptr)(wg + 32 * gq + 64) + rg;
                bp[3] = (cptr)(wu + 32 * gq) + rg;      bp[4] = (cptr)(wu + 32 * gq + 64) + rg;
            } else {
                bp[0] = (cptr)(wqkv + 1024 + 64 * j) + rq;
                bp[1] = (cptr)(wg + 32 * gp) + rg;
                bp[2] = (chunk < 4 ? (cptr)(wg + 32 * gh) : (cptr)(wu + 32 * gh) - 64) + rg;
                bp[3] = (cptr)(wu + 32 * gp) + rg;
                bp[4] = (cptr)(wqkv + 2048 + 64 * j) + rq;
            }
#pragma unroll
            for (int s = 0; s < 3; ++s) {
#pragma unroll
                for (int c = 0; c < 5; ++c) glds_v(bp[c], ldsb + s * G1_STAGE + G1_SA + c * 4096 + wc * 1024);
                bp[0] += ldq; bp[1] += ldg; bp[2] += ldg; bp[3] += ldg; bp[4] += ld4;
            }
            glds_v((cptr)(wphi + (size_t)(2 * j) * 1024 + wc * 512 + lane * 8), ldsb + L_WPHI + wc * 1024);
            {
                const int e = r * 40 + wc * 10 + (lane < 10 ? lane : 9);
                cptr p;
                if (e < 96) p = (cptr)wqkv + (size_t)e * 65536;
                else if (e < 208) p = (cptr)wg + (size_t)(e - 96) * 65536;
                else if (e < 320) p = (cptr)wu + (size_t)(e - 208) * 65536;
                else if (e < 432) p = (cptr)wd + (size_t)(e - 320) * 65536;
                else if (e < 464) p = (cptr)wo + (size_t)(e - 432) * 65536;
                else if (e < 528) p = (cptr)out + (size_t)(e - 464) * 65536;
                else if (e < 592) p = (cptr)x + (size_t)(e - 528) * 65536;
                else p = (cptr)ws + (size_t)min(e - 592, (int)(WS_END / 65536)) * 65536;
                asm volatile("global_load_dword %0, %1, off sc1" : "+v"(tlbt) : "v"(p) : "memory");
            }
            {
                u64 pc; asm volatile("s_getpc_b64 %0" : "=s"(pc));
                const int ln_ = wc * 64 + lane;
                cptr p = (cptr)(pc & ~(u64)0x7f) + (ln_ < CODE_LINES ? ln_ : CODE_LINES - 1) * 128;
                asm volatile("global_load_dword %0, %1, off" : "+v"(codet) : "v"(p) : "memory");
            }
        } else {
#pragma unroll
            for (int c = 0; c < 5; ++c) bp[c] = nullptr;
            const unsigned xo = (unsigned)(row * D_ + (wc * 8 + (lane >> 3)) * 8) * 2u;
            asm volatile("global_load_dwordx4 %0, %4, %5\n\tglobal_load_dwordx4 %1, %4, %5 offset:512\n\tglobal_load_dwordx4 %2, %4, %5 offset:1024\n\tglobal_load_dwordx4 %3, %4, %5 offset:1536\n\t"
                         "s_waitcnt vmcnt(0)" : "+v"(xv[0]), "+v"(xv[1]), "+v"(xv[2]), "+v"(xv[3]) : "v"(xo), "s"(x) : "memory");
        }
        asm volatile("s_waitcnt vmcnt(" XW_CNT ")" : "+v"(ev)::"memory");
        {
            u64 z = (((u64)(unsigned)__builtin_amdgcn_readfirstlane(ev[1]) << 32) | (unsigned)__builtin_amdgcn_readfirstlane(ev[0])) + 0x9E3779B97F4A7C15ull;
            z = (z ^ (z >> 30)) * 0xBF58476D1CE4E5B9ull; z = (z ^ (z >> 27)) * 0x94D049BB133111EBull; tag = z ^ (z >> 31);
        }
        STAMP(1);
        LAS f32x2* red = (LAS f32x2*)(lds + L_MISC);
        float f[32];
        if (grp == 1) {
            float s = 0.f;
#pragma unroll
            for (int i = 0; i < 4; ++i)
#pragma unroll
                for (int k = 0; k < 4; ++k) { f[8 * i + 2 * k] = lo_f(xv[i][k]); f[8 * i + 2 * k + 1] = hi_f(xv[i][k]); }
            { f32x2 s2 = {0.f, 0.f};
#pragma unroll
              for (int i = 0; i < 16; ++i) s2 += (f32x2){f[2 * i], f[2 * i + 1]};
              s = s2[0] + s2[1]; }
            s += ror8(s); s = sum_g(s);
            const float mw = s * (1.0f / 256.0f);
            f32x2 q2 = {0.f, 0.f};
#pragma unroll
            for (int i = 0; i < 16; ++i) { const f32x2 d = (f32x2){f[2 * i], f[2 * i + 1]} - mw; q2 += d * d; }
            float q = q2[0] + q2[1];
            q += ror8(q); q = sum_g(q);
            if (lane < 8) red[wc * 8 + lane] = (f32x2){s, q};
        }
        LBAR();
        if (grp == 1) {
            f32x2 p[4];
#pragma unroll
            for (int w = 0; w < 4; ++w) p[w] = red[w * 8 + (lane & 7)];
            const float mean = ((p[0][0] + p[1][0]) + (p[2][0] + p[3][0])) * (1.0f / D_);
            float m2 = (p[0][1] + p[1][1]) + (p[2][1] + p[3][1]);
#pragma unroll
            for (int w = 0; w < 4; ++w) { const float dm = p[w][0] * (1.0f / 256.0f) - mean; m2 += 256.0f * dm * dm; }
            const float var = m2 * (1.0f / D_);
            const float m = rbf(mean), sd = rbf(sqrtf(rbf(rbf(var) + 1e-5f))), rsd = 1.0f / sd;
#pragma unroll
            for (int it = 0; it < 4; ++it) {
                const int pc = it * 32 + wc * 8 + (lane >> 3), kc = pc >> 2, kq = pc & 3;
                u32x4 o;
#pragma unroll
                for (int i = 0; i < 4; ++i) {
                    const unsigned w = pk_bf16(f[it * 8 + 2 * i] - m, f[it * 8 + 2 * i + 1] - m);
                    const f32x2 a = {lo_f(w), hi_f(w)}, q0 = a * rsd;
                    const f32x2 q1 = __builtin_elementwise_fma(__builtin_elementwise_fma(-q0, (f32x2){sd, sd}, a), (f32x2){rsd, rsd}, q0);
                    o[i] = pk_bf16(q1[0], q1[1]);
                }
                ST_SC1(o, rs, WS_XN + (unsigned)(((panel * 32 + kc) * 16 + ((row >> 4) & 15)) * 1024 + (kq * 16 + (row & 15)) * 16));
            }
            WAIT_V(0);
        }
        LBAR();
        STAMP(2);
        if (threadIdx.x == 256) flag_set(ws, FL_LN + panel * 32 + ks * 8 + c8, tag);
        if (wid == 0) flag_wait(ws, FL_LN + panel * 32, 1, 32, tag, lane);
        LBAR();
        STAMP(3);

        cptr xn = uni((cptr)ws + WS_XN + (size_t)panel * 32 * 16384);
        if (grp == 1 && wc < 3) {
            const int p0 = wc == 0 ? 0 : 5 * wc + 1;
            const unsigned av0 = p0 * 1024 + lane * 16, av1 = av0 + 4096;
#pragma unroll
            for (int s3 = 0; s3 < 3; ++s3) {
                const unsigned la = ldsb + s3 * G1_STAGE + p0 * 1024;
                glds_s_sc1_first<0>(av0, xn + s3 * 16384, la); glds_s_sc1_next<1024>(av0, xn + s3 * 16384);
                glds_s_sc1_next<2048>(av0, xn + s3 * 16384);   glds_s_sc1_next<3072>(av0, xn + s3 * 16384);
                glds_s_sc1_first<0>(av1, xn + s3 * 16384, la + 4096);
                if (wc == 0) glds_s_sc1_next<1024>(av1, xn + s3 * 16384);
            }
            if (wc == 0) WAIT_V(12); else WAIT_V(10);
        }
        int ba[5];
        {
            int cgA, tlA, cgB, tlB;
            if (!kvt) { cgA = 1 + (wc >> 1); tlA = 2 * (wc & 1); cgB = 3 + (wc >> 1); tlB = tlA; }
            else if (wc < 2) { cgA = 1; tlA = 2 * wc; cgB = 3; tlB = 2 * wc; }
            else if (wc == 2) { cgA = 2; tlA = 0; cgB = 2; tlB = 2; }
            else { cgA = 4; tlA = 0; cgB = 4; tlB = 2; }
            const int q4 = (lane >> 2) & 3, p = lane & 3, krow = 8 * g + q4, sw = swzB(krow);
            auto adr = [&](int cg, int tl) { return G1_SA + cg * 4096 + krow * 128 + (((2 * tl + (p >> 1)) ^ sw) << 4) + (p & 1) * 8; };
            ba[0] = adr(cgA, tlA); ba[1] = adr(cgA, tlA + 1); ba[2] = adr(cgB, tlB); ba[3] = adr(cgB, tlB + 1); ba[4] = adr(0, wc);
        }
#pragma unroll
        for (int m = 0; m < 8; ++m)
#pragma unroll
            for (int n = 0; n < 5; ++n) acc[m][n] = (f32x4){0.f, 0.f, 0.f, 0.f};
        if (grp == 0) asm volatile("s_waitcnt vmcnt(0)" : "+v"(tlbt), "+v"(codet)::"memory");
        BAR();
        STAMP(4);
        if (grp == 0) { g1_mainloop<0>(ldsb, lds, acc, bp, ld4, xn, wc, lane, ba, (cptr)wd, (cptr)wo); BAR(); }
        else          { BAR(); g1_mainloop<1>(ldsb, lds, acc, bp, ld4, xn, wc, lane, ba, (cptr)wd, (cptr)wo); }
        STAMP(5);
    }

    int tz = threadIdx.x; asm volatile("" : "+v"(tz));
    const int lane = tz & 63, g = lane >> 4, l15 = lane & 15;
    const unsigned bvoff = (unsigned)((8 * wc + (lane >> 3)) * 2048 + c8 * 256 + (((lane & 7) ^ swzB(8 * wc + (lane >> 3))) << 4));
    const unsigned bl = ldsb + G2_SB + grp * 24576 + wc * 1024;
    cptr sbp = uni((cptr)wd + ((size_t)(28 * ks + grp) << 16));
    auto prefetchB = [&]() { g2_issueB(bvoff, sbp, bl); g2_issueB(bvoff, sbp + 131072, bl + 8192); sbp += 262144; };
    {
        const int gq = (j & 1) ? 7 * j + 3 : 7 * j, gp = (j & 1) ? 7 * j + 1 : 7 * j + 4, gh = (j & 1) ? 7 * j : 7 * j + 6;
        {
            lds_t kp = lds + E_KP + (128 * grp + l15) * 256 + ((((wc >> 1) * 4 + (wc & 1) * 2 + (g >> 1)) ^ l15) << 4) + (g & 1) * 8;
#pragma unroll
            for (int m = 0; m < 8; ++m) *(LAS u32x2*)(kp + m * 4096) = (u32x2){pk_bf16(acc[m][4][0], acc[m][4][1]), pk_bf16(acc[m][4][2], acc[m][4][3])};
        }
        const bool has_h = !(kvt && wc == 3);
        const int hkc = !kvt ? gq + wc : (wc < 2 ? gp + wc : gh);
        const unsigned hoff = WS_H + (unsigned)(((panel * NKC2 + hkc) * 16 + 8 * grp) * 1024 + frag_off(lane));
        auto geglu_c = [&](int m) {
            const f32x2 a0 = gelu_mul2(acc[m][0].lo, acc[m][2].lo), a1 = gelu_mul2(acc[m][0].hi, acc[m][2].hi);
            const f32x2 b0 = gelu_mul2(acc[m][1].lo, acc[m][3].lo), b1 = gelu_mul2(acc[m][1].hi, acc[m][3].hi);
            return pack_frag(a0, a1, b0, b1);
        };
        auto geglu = [&](int m) { ST_SC1(geglu_c(m), rs, hoff + m * 1024); SCHED(); };
        const int q4 = (lane >> 2) & 3, p4 = lane & 3;
        auto logits = [&](u32x4 (&lg)[4]) {
            lds_t wp = lds + L_WPHI + (8 * g + q4) * 64 + p4 * 8;
            lds_t qp = lds + E_KP + (32 * wid + l15) * 256;
            bf16x8 wf[2][2], qf[4];
#pragma unroll
            for (int hh = 0; hh < 2; ++hh) {
                wf[hh][0] = lds_tr2(wp + hh * 2048, wp + hh * 2048 + 256); wf[hh][1] = lds_tr2(wp + hh * 2048 + 32, wp + hh * 2048 + 288);
                qf[hh * 2] = lds_b128(qp + (((hh * 4 + g) ^ l15) << 4)); qf[hh * 2 + 1] = lds_b128(qp + 4096 + (((hh * 4 + g) ^ l15) << 4));
            }
            SCHED();
            const f32x4 z4 = {0.f, 0.f, 0.f, 0.f};
            f32x4 p0[4], p1[4];
#pragma unroll
            for (int u = 0; u < 4; ++u) { p0[u] = MFMA(wf[u >> 1][0], qf[u], z4); p1[u] = MFMA(wf[u >> 1][1], qf[u], z4); }
            SCHED();
#pragma unroll
            for (int u = 0; u < 4; ++u) lg[u] = (u32x4){pk_bf16(p0[u][0], p0[u][1]), pk_bf16(p0[u][2], p0[u][3]), pk_bf16(p1[u][0], p1[u][1]), pk_bf16(p1[u][2], p1[u][3])};
            asm volatile("" : "+v"(lg[0]), "+v"(lg[1]), "+v"(lg[2]), "+v"(lg[3]));
        };
        if (kvt) {
            LBAR();
            if (wc == 3) {
                const int sw = swzP(l15);
                lds_t vp = lds + E_V + (128 * grp + l15) * 128 + (g & 1) * 8;
#pragma unroll
                for (int n = 0; n < 4; ++n) {
                    lds_t vn = vp + (((2 * n + (g >> 1)) ^ sw) << 4);
#pragma unroll
                    for (int m = 0; m < 8; ++m) *(LAS u32x2*)(vn + m * 2048) = (u32x2){pk_bf16(acc[m][n][0], acc[m][n][1]), pk_bf16(acc[m][n][2], acc[m][n][3])};
                }
            }
            STAMP(6);
            u32x4 lg[4], P[4], M[4];
            logits(lg);
            WAIT_L0();
            soft_pm<4>(lg, P, M);
            {
                const int sw = swzF(l15);
                lds_t pb = lds + E_KP + (32 * wid + l15) * 256 + (g & 1) * 8;
#pragma unroll
                for (int u = 0; u < 4; ++u) {
                    const int c0 = (u >> 1) * 8 + (g >> 1), tb = u & 1;
                    *(LAS u32x2*)(pb + tb * 4096 + (((c0 + 0) ^ sw) << 4)) = (u32x2){P[u][0], P[u][1]};
                    *(LAS u32x2*)(pb + tb * 4096 + (((c0 + 2) ^ sw) << 4)) = (u32x2){P[u][2], P[u][3]};
                    *(LAS u32x2*)(pb + tb * 4096 + (((c0 + 4) ^ sw) << 4)) = (u32x2){M[u][0], M[u][1]};
                    *(LAS u32x2*)(pb + tb * 4096 + (((c0 + 6) ^ sw) << 4)) = (u32x2){M[u][2], M[u][3]};
                }
            }
            LBAR();
            STAMP(7);
            {
                const int hh = wid >> 2, ft = wid & 3;
                f32x4 kv0 = {0.f, 0.f, 0.f, 0.f}, kv1 = kv0, zz = kv0;
                const bf16x8 ones = {0x3F80, 0x3F80, 0x3F80, 0x3F80, 0x3F80, 0x3F80, 0x3F80, 0x3F80};
                const int t0 = 8 * g + q4, t1 = t0 + 4, cp = hh * 8 + 2 * ft + (p4 >> 1), cv = hh * 4 + (p4 >> 1), hb8 = (p4 & 1) * 8;
                lds_t pf0 = lds + E_KP + t0 * 256 + ((cp ^ swzF(t0 & 15)) << 4) + hb8, pf1 = lds + E_KP + t1 * 256 + ((cp ^ swzF(t1 & 15)) << 4) + hb8;
                lds_t va0 = lds + E_V + t0 * 128 + ((cv ^ swzP(t0)) << 4) + hb8, va1 = lds + E_V + t1 * 128 + ((cv ^ swzP(t1)) << 4) + hb8;
                lds_t vb0 = lds + E_V + t0 * 128 + (((cv + 2) ^ swzP(t0)) << 4) + hb8, vb1 = lds + E_V + t1 * 128 + (((cv + 2) ^ swzP(t1)) << 4) + hb8;
#pragma unroll
                for (int k8 = 0; k8 < 8; ++k8) {
                    const bf16x8 pf = lds_tr2(pf0 + k8 * 8192, pf1 + k8 * 8192), v0 = lds_tr2(va0 + k8 * 4096, va1 + k8 * 4096), v1 = lds_tr2(vb0 + k8 * 4096, vb1 + k8 * 4096);
                    kv0 = MFMA(pf, v0, kv0); kv1 = MFMA(pf, v1, kv1); zz = MFMA(pf, ones, zz);
                }
                ST_SC1(((u32x4){pk_bf16(kv0[0], kv0[1]), pk_bf16(kv0[2], kv0[3]), pk_bf16(kv1[0], kv1[1]), pk_bf16(kv1[2], kv1[3])}), rs,
                       WS_PART + (unsigned)(((panel * 32 + 2 * j + hh) * 4 + ft) * 1024 + lane * 16));
                if (l15 == 0) ST_SC1(__builtin_bit_cast(u32x4, zz), rs, WS_PZ + (unsigned)(((panel * 32 + 2 * j + hh) * 64 + 16 * ft + 4 * g) * 4));
            }
            SCHED();
            u32x4 h0 = {0, 0, 0, 0};
            if (has_h) h0 = geglu_c(0);
            WAIT_V(0);
            LBAR();
            if (threadIdx.x == 0) flag_set(ws, FL_KV + j * 8 + panel, tag);
            STAMP(8);
            prefetchB();
            if (has_h) {
                ST_SC1(h0, rs, hoff); SCHED();
                geglu(1); geglu(2); geglu(3); geglu(4); geglu(5); geglu(6); geglu(7);
            }
            WAIT_V(0);
            LBAR();
            if (threadIdx.x == 0) flag_set(ws, FL_H + (panel * 4 + ks) * 8 + c8, tag);
            STAMP(9);
        } else {
            u32x4 lg[4], P[4], M[4];
            LBAR();
            logits(lg);
            soft_pm<4>(lg, P, M);
#pragma unroll
            for (int u = 0; u < 4; ++u) asm volatile("" : "+v"(P[u]), "+v"(M[u]));
            SCHED();
            STAMP(6);
#pragma unroll
            for (int m = 0; m < 8; ++m) geglu(m);
            prefetchB();
            WAIT_V(4);
            LBAR();
            if (threadIdx.x == 0) flag_set(ws, FL_H + (panel * 4 + ks) * 8 + c8, tag);
            STAMP(7);
            if (wid == 0) flag_wait(ws, FL_KV + j * 8, 1, 8, tag, lane);
            LBAR();
            STAMP(8);
            {
                const int hh = wid >> 2, ft = wid & 3;
                f32x4 a0, a1, az;
                u32x4 pw[8]; f32x4 pz[8];
#pragma unroll
                for (int p = 0; p < 8; ++p) {
                    pw[p] = LD_SC1(rs, WS_PART + (unsigned)(((p * 32 + 2 * j + hh) * 4 + ft) * 1024 + lane * 16));
                    pz[p] = __builtin_bit_cast(f32x4, LD_SC1(rs, WS_PZ + (unsigned)(((p * 32 + 2 * j + hh) * 64 + 16 * ft + 4 * g) * 4)));
                }
#pragma unroll
                for (int p = 0; p < 8; ++p) {
                    const f32x4 b0 = {lo_f(pw[p][0]), hi_f(pw[p][0]), lo_f(pw[p][1]), hi_f(pw[p][1])}, b1 = {lo_f(pw[p][2]), hi_f(pw[p][2]), lo_f(pw[p][3]), hi_f(pw[p][3])};
                    if (p == 0) { a0 = b0; a1 = b1; az = pz[0]; } else { a0 += b0; a1 += b1; az += pz[p]; }
                }
                lds_t kb = lds + E_V + ((hh * 2 + (ft >> 1)) * 3) * 1024 + lane * 16 + (ft & 1) * 8;
                *(LAS u32x2*)(kb) = (u32x2){pk_bf16(a0[0], a0[1]), pk_bf16(a0[2], a0[3])};
                *(LAS u32x2*)(kb + 1024) = (u32x2){pk_bf16(a1[0], a1[1]), pk_bf16(a1[2], a1[3])};
                *(LAS u32x2*)(kb + 2048) = (u32x2){pk_bf16(az[0], az[1]), pk_bf16(az[2], az[3])};
            }
            LBAR();
            STAMP(9);
#pragma unroll
            for (int hh = 0; hh < 2; ++hh) {
                bf16x8 kf[2][3];
#pragma unroll
                for (int s = 0; s < 2; ++s)
#pragma unroll
                    for (int d = 0; d < 3; ++d) kf[s][d] = lds_b128(lds + E_V + ((hh * 2 + s) * 3 + d) * 1024 + lane * 16);
#pragma unroll
                for (int tb = 0; tb < 2; ++tb) {
                    const bf16x8 Pf = __builtin_bit_cast(bf16x8, P[hh * 2 + tb]), Mf = __builtin_bit_cast(bf16x8, M[hh * 2 + tb]);
                    const f32x4 z4 = {0.f, 0.f, 0.f, 0.f};
                    const f32x4 n0 = MFMA(kf[1][0], Mf, MFMA(kf[0][0], Pf, z4));
                    const f32x4 n1 = MFMA(kf[1][1], Mf, MFMA(kf[0][1], Pf, z4));
                    const f32x4 dn = MFMA(kf[1][2], Mf, MFMA(kf[0][2], Pf, z4));
                    const float rden = __builtin_amdgcn_rcpf(rbf(rbf(dn[0]) + 1e-6f));
                    ST_SC1(pack_frag(n0.lo * rden, n0.hi * rden, n1.lo * rden, n1.hi * rden), rs, WS_H + (unsigned)(((panel * NKC2 + 112 + 2 * j + hh) * 16 + 2 * wid + tb) * 1024 + frag_off(lane)));
                }
            }
        }
    }
    STAMP(10);
    STAMP_OUT(0, 0, 11);

    STAMP(0);
    if (wid == 0) flag_wait(ws, FL_H + (panel * 4 + ks) * 8, 1, 8, tag, lane);
    LBAR();
    STAMP(1);
    f32x4 c2[8][4];
    {
        const int wm = wc >> 1, wn = wc & 1;
        cptr sa = uni((cptr)ws + WS_H + ((size_t)(panel * NKC2 + 28 * ks + grp) << 14));
        const unsigned avoff = (unsigned)(wc * 4096 + lane * 16), al = ldsb + grp * 49152 + wc * 4096;
        auto issueA = [&](unsigned slot_off) {
            glds_s_sc1_first<0>(avoff, sa, al + slot_off); glds_s_sc1_next<1024>(avoff, sa); glds_s_sc1_next<2048>(avoff, sa); glds_s_sc1_next<3072>(avoff, sa);
            sa += 32768;
        };
        issueA(0); issueA(16384);
        unsigned bb[4];
        {
            const int q4 = (lane >> 2) & 3, p = lane & 3, kr = 8 * g + q4, sw = swzB(kr);
#pragma unroll
            for (int tl = 0; tl < 4; ++tl) bb[tl] = ldsb + G2_SB + grp * 24576 + wn * 4096 + kr * 128 + (((2 * tl + (p >> 1)) ^ sw) << 4) + (p & 1) * 8;
        }
        lds_t ap = lds + grp * 49152 + wm * 8192 + lane * 16;
#pragma unroll
        for (int m = 0; m < 8; ++m)
#pragma unroll
            for (int n = 0; n < 4; ++n) c2[m][n] = (f32x4){0.f, 0.f, 0.f, 0.f};
        u32x2 ofl = {0, 0};
        const unsigned fo = (FL_O + (panel * 4 + ks) * 4 + (lane & 3)) * 64;
        WAIT_V(4);
        LBAR();
        if (!kvt && threadIdx.x == 0) flag_set(ws, FL_O + (panel * 4 + ks) * 4 + (c8 >> 1), tag);
        if (grp == 1) BAR();
        STAMP(2);
        auto step = [&](auto uc, const int t) {
            constexpr int U = decltype(uc)::value, W = (U + 2) % 3;
            bf16x8 A[8], B[4];
#define FST(x)
            FST(f0);
            if (U == 0) {
                if (t == 12) {
                    asm volatile("" : "+v"(ofl));
                    const u64 v = ((u64)ofl[1] << 32) | ofl[0];
                    if (!__all(v == tag)) flag_wait(ws, FL_O + (panel * 4 + ks) * 4, 1, 4, tag, lane);
                    sa = uni((cptr)ws + WS_H + ((size_t)(panel * NKC2 + 112 + 8 * ks + grp) << 14));
                    sbp = uni((cptr)wo + ((size_t)(8 * ks + grp) << 16));
                }
                asm volatile("s_cmp_lg_u32 %3, 9\n\ts_cbranch_scc1 .Lofl_%=\n\tglobal_load_dwordx2 %0, %1, %2 sc1\n.Lofl_%=:" : "+v"(ofl) : "v"(fo), "s"(ws + WS_FLG), "s"(t) : "scc", "memory");
            }
            const bool more = U == 0 || t < 16;
            B[0] = lds_tr2_asm<U * 8192>(bb[0]); B[1] = lds_tr2_asm<U * 8192>(bb[1]);
            if (more) glds_s(bvoff, sbp, bl + W * 8192);
            B[2] = lds_tr2_asm<U * 8192>(bb[2]); B[3] = lds_tr2_asm<U * 8192>(bb[3]);
            if (more) { glds_s_o128(bvoff, sbp, bl + W * 8192 + 4096); sbp += 131072; }
            A[0] = lds_b128(ap + U * 16384); A[1] = lds_b128(ap + U * 16384 + 1024);
            if (more) { glds_s_sc1_first<0>(avoff, sa, al + W * 16384); glds_s_sc1_next<1024>(avoff, sa); }
            A[2] = lds_b128(ap + U * 16384 + 2048); A[3] = lds_b128(ap + U * 16384 + 3072); A[4] = lds_b128(ap + U * 16384 + 4096);
            if (more) { glds_s_sc1_next<2048>(avoff, sa); glds_s_sc1_next<3072>(avoff, sa); sa += 32768; }
            A[5] = lds_b128(ap + U * 16384 + 5120); A[6] = lds_b128(ap + U * 16384 + 6144); A[7] = lds_b128(ap + U * 16384 + 7168);
            FST(f1);
            WAIT_L0();
            FST(f2);
            if (!more) WAIT_V(0);
            SCHED(); BAR(); SCHED();
            FST(f3);
            __builtin_amdgcn_s_setprio(1);
#pragma unroll
            for (int m = 0; m < 8; ++m)
#pragma unroll
                for (int n = 0; n < 4; ++n) { const int nn = (m & 1) ? 3 - n : n; c2[m][nn] = MFMA(B[nn], A[m], c2[m][nn]); }
            __builtin_amdgcn_s_setprio(0);
            FST(f4);
            WAIT_V(6);
            FST(f5);
            SCHED(); BAR(); SCHED();
        };
#pragma unroll 1
        for (int t = 0; t < 18; t += 3) { step(std::integral_constant<int, 0>{}, t); step(std::integral_constant<int, 1>{}, t + 1); step(std::integral_constant<int, 2>{}, t + 2); }
        if (grp == 0) BAR();
    }
    STAMP(3);
    {
        int tz2 = threadIdx.x; asm volatile("" : "+v"(tz2));
        const int ln = tz2 & 63, tile = panel * 8 + c8, wm = wc >> 1, wn = wc & 1;
        const int qk = 2 * wm + grp, qs = qk ^ 1;
        const bool publisher = qk != ks;
        const int mh = wid >> 2, nq = wid & 3;
        const int row0 = panel * 256 + ks * 64 + mh * 32 + (ln & 15), col0 = c8 * 128 + nq * 32 + 8 * frag_kq(ln);
        constexpr int L_OWN = 0, L_SEND = 65536;
        u32x4 xr0 = {0, 0, 0, 0}, xr1 = xr0;
        {
            const unsigned xo = (unsigned)(row0 * D_ + col0) * 2u;
            asm volatile("global_load_dwordx4 %0, %1, %2" : "+v"(xr0) : "v"(xo), "s"(x) : "memory");
            asm volatile("global_load_dwordx4 %0, %1, %2" : "+v"(xr1) : "v"(xo + 16 * D_ * 2), "s"(x) : "memory");
        }
        f32x4 hk[4][4], hs[4][4];
#pragma unroll
        for (int m = 0; m < 4; ++m)
#pragma unroll
            for (int n = 0; n < 4; ++n) { hk[m][n] = grp ? c2[4 + m][n] : c2[m][n]; hs[m][n] = grp ? c2[m][n] : c2[4 + m][n]; }
        auto dump_own = [&](const f32x4 (&v)[4][4]) {
#pragma unroll
            for (int m = 0; m < 4; ++m)
#pragma unroll
                for (int n = 0; n < 4; ++n) *(LAS f32x4*)(lds + L_OWN + ((grp * 4 + m) * 8 + 4 * wn + n) * 1024 + ln * 16) = v[m][n];
        };
        if (qs == ks) dump_own(hs);
        else {
#pragma unroll
            for (int m = 0; m < 4; ++m)
#pragma unroll
                for (int n2 = 0; n2 < 2; ++n2) {
                    const f32x4 a = hs[m][2 * n2], b = hs[m][2 * n2 + 1];
                    *(LAS u32x4*)(lds + L_SEND + (wid ^ 4) * 8192 + (m * 2 + n2) * 1024 + ln * 16) = (u32x4){pk_bf16(a[0], a[1]), pk_bf16(a[2], a[3]), pk_bf16(b[0], b[1]), pk_bf16(b[2], b[3])};
                }
        }
        if (qk == ks) dump_own(hk);
        LBAR();
        if (publisher) {
            const unsigned xbase = WS_XCH + (unsigned)((((tile * 4 + ks) * 8 + wid) * 8) * 1024 + ln * 16);
#pragma unroll
            for (int m = 0; m < 4; ++m)
#pragma unroll
                for (int n2 = 0; n2 < 2; ++n2) {
                    const u32x4 w = *(const LAS u32x4*)(lds + L_SEND + wid * 8192 + (m * 2 + n2) * 1024 + ln * 16);
                    const f32x4 a = hk[m][2 * n2] + (f32x4){lo_f(w[0]), hi_f(w[0]), lo_f(w[1]), hi_f(w[1])}, b = hk[m][2 * n2 + 1] + (f32x4){lo_f(w[2]), hi_f(w[2]), lo_f(w[3]), hi_f(w[3])};
                    ST_SC1(((u32x4){pk_bf16(a[0], a[1]), pk_bf16(a[2], a[3]), pk_bf16(b[0], b[1]), pk_bf16(b[2], b[3])}), rs, xbase + (m * 2 + n2) * 1024);
                }
        }
        f32x4 e[2][2];
#pragma unroll
        for (int a = 0; a < 2; ++a)
#pragma unroll
            for (int b = 0; b < 2; ++b)
                e[a][b] = *(const LAS f32x4*)(lds + L_OWN + ((2 * mh + a) * 8 + 2 * nq + b) * 1024 + ln * 16) + *(const LAS f32x4*)(lds + L_OWN + ((4 + 2 * mh + a) * 8 + 2 * nq + b) * 1024 + ln * 16);
        STAMP(4);
        asm volatile("s_waitcnt vmcnt(0)" : "+v"(xr0), "+v"(xr1)::"memory");
        if (publisher && ln == 0) flag_set(ws, FL_X + (tile * 4 + ks) * 8 + wid, tag);
        STAMP(5);
        const int wsrc = (ks & 1) * 4 + (ks >> 1) * 2 + (nq >> 1);
        {
            GAS u64* p = flagp(ws, FL_X + (tile * 4 + ((ks + 1 + (ln < 3 ? ln : 0)) & 3)) * 8 + wsrc);
#pragma unroll 1
            for (unsigned it = 0; it < (1u << 21); ++it) {
                const u64 v = __hip_atomic_load(p, __ATOMIC_RELAXED, __HIP_MEMORY_SCOPE_AGENT);
                if (__all(v == tag)) break;
                __builtin_amdgcn_s_sleep(1);
            }
            asm volatile("" ::: "memory");
        }
        u32x4 pw[3][2];
#pragma unroll
        for (int i = 0; i < 3; ++i) {
            const unsigned base = WS_XCH + (unsigned)((((tile * 4 + i + (i >= ks ? 1 : 0)) * 8 + wsrc) * 8 + (nq & 1) + 4 * mh) * 1024 + ln * 16);
            pw[i][0] = LD_SC1(rs, base); pw[i][1] = LD_SC1(rs, base + 2048);
        }
#pragma unroll
        for (int i = 0; i < 3; ++i)
#pragma unroll
            for (int a = 0; a < 2; ++a) {
                const u32x4 w = pw[i][a];
                e[a][0] += (f32x4){lo_f(w[0]), hi_f(w[0]), lo_f(w[1]), hi_f(w[1])};
                e[a][1] += (f32x4){lo_f(w[2]), hi_f(w[2]), lo_f(w[3]), hi_f(w[3])};
            }
        const __amdgpu_buffer_rsrc_t ors = __builtin_amdgcn_make_buffer_rsrc((void*)out, 0, S_ * D_ * 2, 0x00020000);
#pragma unroll
        for (int a = 0; a < 2; ++a) {
            float u[4], v[4];
#pragma unroll
            for (int q = 0; q < 4; ++q) {
                auto rr = __builtin_amdgcn_permlane16_swap(__float_as_uint(e[a][0][q]), __float_as_uint(e[a][1][q]), false, false);
                u[q] = __uint_as_float(rr[0]); v[q] = __uint_as_float(rr[1]);
            }
            const u32x4 xv = a ? xr1 : xr0;
            u32x4 o;
            o[0] = pk_bf16(u[0] + lo_f(xv[0]), u[1] + hi_f(xv[0])); o[1] = pk_bf16(u[2] + lo_f(xv[1]), u[3] + hi_f(xv[1]));
            o[2] = pk_bf16(v[0] + lo_f(xv[2]), v[1] + hi_f(xv[2])); o[3] = pk_bf16(v[2] + lo_f(xv[3]), v[3] + hi_f(xv[3]));
            __builtin_amdgcn_raw_buffer_store_b128(o, ors, (unsigned)(((row0 + 16 * a) * D_ + col0) * 2), 0, 16);
        }
        const int ln2 = ln;
        if (blockIdx.x == 0 && wid == 0) {
#pragma unroll 1
            for (int i = 0; i < 4; ++i) flag_wait(ws, FL_LN + 64 * i, 1, 64, tag, ln2);
            if (ln2 == 0) flag_set(ws, FL_EPOCH, tag);
        }
    }
    STAMP(6);
    STAMP_OUT(1, 0, 7);
}

extern "C" void kernel_launch(void* const* d_in, const int* in_sizes, int n_in, void* d_out, int out_size, void* d_ws, size_t ws_size, hipStream_t stream) {
    const u16* x = (const u16*)d_in[0]; const u16* wqkv = (const u16*)d_in[1]; const u16* wphi = (const u16*)d_in[2]; const u16* wo = (const u16*)d_in[3];
    const u16* wg = (const u16*)d_in[5]; const u16* wu = (const u16*)d_in[6]; const u16* wd = (const u16*)d_in[7];
    (void)hipFuncSetAttribute((const void*)k_fused, hipFuncAttributeMaxDynamicSharedMemorySize, L_TOTAL);
    k_fused<<<256, 512, L_TOTAL, stream>>>(x, wqkv, wphi, wo, wg, wu, wd, (unsigned char*)d_ws, (u16*)d_out);
}
```

```cpp
#include <hip/hip_runtime.h>
#include <type_traits>

typedef unsigned short u16;
typedef unsigned long long u64;
typedef short bf16x8 __attribute__((ext_vector_type(8)));
typedef short s16x4 __attribute__((ext_vector_type(4)));
typedef float f32x4 __attribute__((ext_vector_type(4)));
typedef float f32x2 __attribute__((ext_vector_type(2)));
typedef unsigned u32x4 __attribute__((ext_vector_type(4)));
typedef unsigned u32x2 __attribute__((ext_vector_type(2)));
#define LAS __attribute__((address_space(3)))
#define GAS __attribute__((address_space(1)))
typedef LAS unsigned char* lds_t;
typedef const unsigned char* cptr;

constexpr int S_ = 2048, D_ = 1024, F_ = 3584;
constexpr int CODE_LINES = 256;
constexpr int NKC2 = 144;

constexpr unsigned WS_XN   = 0;
constexpr unsigned WS_H    = WS_XN + S_ * D_ * 2;
constexpr unsigned WS_PART = WS_H + 8 * NKC2 * 16 * 1024;
constexpr unsigned WS_PZ   = WS_PART + 8 * 32 * 4 * 1024;
constexpr unsigned WS_XCH  = WS_PZ + 8 * 32 * 64 * 4;
constexpr unsigned WS_FLG  = WS_XCH + 64 * 4 * 8 * 8 * 1024;
constexpr unsigned FL_LN = 0, FL_KV = FL_LN + 256, FL_H = FL_KV + 128, FL_O = FL_H + 256, FL_X = FL_O + 128, FL_EPOCH = FL_X + 2048, FL_N = FL_EPOCH + 1;
constexpr unsigned WS_END  = WS_FLG + FL_N * 64;

constexpr int G1_SA = 16384, G1_STAGE = 36864;
constexpr int L_MISC = 158720, L_WPHI = 159744, L_TOTAL = 163840;
constexpr int L_KVL = 147456;
constexpr int E_KP = 0, E_V = 65536;
constexpr int G2_SB = 98304;

__device__ __forceinline__ unsigned pk_bf16(float lo, float hi) {
    typedef __bf16 bf2 __attribute__((ext_vector_type(2)));
    bf2 r = {(__bf16)lo, (__bf16)hi};
    return __builtin_bit_cast(unsigned, r);
}
__device__ __forceinline__ float rbf(float x) { return __uint_as_float(pk_bf16(x, x) & 0xffff0000u); }
__device__ __forceinline__ float lo_f(unsigned w) { return __uint_as_float(w << 16); }
__device__ __forceinline__ float hi_f(unsigned w) { return __uint_as_float(w & 0xffff0000u); }

#define STAMP_DECL
#define STAMP(i)
#define STAMP_ARG
#define STAMP_PASS
#define STAMP_OUT(kern, lo, n)

#define XW_CNT "18"
#define WAIT_V(n) asm volatile("s_waitcnt vmcnt(" #n ")" ::: "memory")
#define WAIT_L0() do { asm volatile("" ::: "memory"); __builtin_amdgcn_s_waitcnt(0xC07F); asm volatile("" ::: "memory"); } while (0)
#define BAR() __builtin_amdgcn_s_barrier()
#define LBAR() asm volatile("s_waitcnt lgkmcnt(0)\n\ts_barrier" ::: "memory")
#define SCHED() __builtin_amdgcn_sched_barrier(0)
#define MFMA(a, b, c) __builtin_amdgcn_mfma_f32_16x16x32_bf16(a, b, c, 0, 0, 0)
#define MFMA_SET(c, a, b) asm volatile("v_mfma_f32_16x16x32_bf16 %0, %1, %2, 0" : "=&v"(c) : "v"(a), "v"(b))
#define MFMA_ACC(c, a, b) asm volatile("v_mfma_f32_16x16x32_bf16 %0, %1, %2, %0" : "+v"(c) : "v"(a), "v"(b))

__device__ __forceinline__ cptr uni(cptr p) {
    const u64 v = (u64)p;
    return (cptr)(((u64)(unsigned)__builtin_amdgcn_readfirstlane((unsigned)(v >> 32)) << 32) | (unsigned)__builtin_amdgcn_readfirstlane((unsigned)v));
}
__device__ __forceinline__ void glds_v(cptr g, unsigned l) {
    asm volatile("s_mov_b32 m0, %1\n\ts_nop 0\n\tglobal_load_lds_dwordx4 %0, off" ::"v"(g), "s"(l) : "memory");
}
__device__ __forceinline__ void glds_s(unsigned voff, cptr sb, unsigned l) {
    asm volatile("s_nop 2\n\ts_mov_b32 m0, %2\n\ts_nop 0\n\tglobal_load_lds_dwordx4 %0, %1" ::"v"(voff), "s"(sb), "s"(l) : "memory");
}
__device__ __forceinline__ void glds_s_o128(unsigned voff, cptr sb, unsigned l) {
    asm volatile("s_mov_b32 m0, %2\n\ts_nop 0\n\tglobal_load_lds_dwordx4 %0, %1 offset:128" ::"v"(voff), "s"(sb), "s"(l - 128u) : "memory");
}
template <int OFF> __device__ __forceinline__ void glds_s_sc1_first(unsigned voff, cptr sb, unsigned l) {
    asm volatile("s_nop 2\n\ts_mov_b32 m0, %2\n\ts_nop 0\n\tglobal_load_lds_dwordx4 %0, %1 offset:%3 sc1" ::"v"(voff), "s"(sb), "s"(l), "i"(OFF) : "memory");
}
template <int OFF> __device__ __forceinline__ void glds_s_sc1_next(unsigned voff, cptr sb) {
    asm volatile("global_load_lds_dwordx4 %0, %1 offset:%2 sc1" ::"v"(voff), "s"(sb), "i"(OFF) : "memory");
}
template <int OFF> __device__ __forceinline__ void gld_sc1(u32x4& d, unsigned voff, cptr sb) {
    asm volatile("global_load_dwordx4 %0, %1, %2 offset:%3 sc1" : "+v"(d) : "v"(voff), "s"(sb), "i"(OFF) : "memory");
}
__device__ __forceinline__ bf16x8 lds_b128(lds_t p) { return *(const LAS bf16x8*)p; }
__device__ __forceinline__ bf16x8 lds_tr2(lds_t p0, lds_t p1) {
    s16x4 a = __builtin_amdgcn_ds_read_tr16_b64_v4i16((LAS s16x4*)p0);
    s16x4 b = __builtin_amdgcn_ds_read_tr16_b64_v4i16((LAS s16x4*)p1);
    return __builtin_shufflevector(a, b, 0, 1, 2, 3, 4, 5, 6, 7);
}
template <int OFF> __device__ __forceinline__ bf16x8 lds_tr2_asm(unsigned addr) {
    s16x4 a, b;
    asm volatile("ds_read_b64_tr_b16 %0, %2 offset:%3\n\tds_read_b64_tr_b16 %1, %2 offset:%4" : "=&v"(a), "=&v"(b) : "v"(addr), "i"(OFF), "i"(OFF + 512) : "memory");
    return __builtin_shufflevector(a, b, 0, 1, 2, 3, 4, 5, 6, 7);
}

__device__ __forceinline__ float x16(float v, float& o) { auto r = __builtin_amdgcn_permlane16_swap(__float_as_uint(v), __float_as_uint(v), false, false); o = __uint_as_float(r[1]); return __uint_as_float(r[0]); }
__device__ __forceinline__ float x32(float v, float& o) { auto r = __builtin_amdgcn_permlane32_swap(__float_as_uint(v), __float_as_uint(v), false, false); o = __uint_as_float(r[1]); return __uint_as_float(r[0]); }
__device__ __forceinline__ float sum_g(float v) { float o, a = x16(v, o); v = a + o; a = x32(v, o); return a + o; }
__device__ __forceinline__ float max_g(float v) { float o, a = x16(v, o); v = fmaxf(a, o); a = x32(v, o); return fmaxf(a, o); }
__device__ __forceinline__ float min_g(float v) { float o, a = x16(v, o); v = fminf(a, o); a = x32(v, o); return fminf(a, o); }
__device__ __forceinline__ float ror8(float v) { return __int_as_float(__builtin_amdgcn_update_dpp(0, __float_as_int(v), 0x128, 0xf, 0xf, false)); }

__device__ __forceinline__ int swzB(int k) { return (((k >> 1) & 1) << 1) | (((k >> 3) & 1) << 2); }
__device__ __forceinline__ int swzP(int k) { return ((k >> 2) & 1) | (((k >> 1) & 1) << 1) | (((k >> 3) & 1) << 2); }
__device__ __forceinline__ int swzF(int k) { return ((k >> 2) & 1) | ((k & 3) << 1) | (((k >> 3) & 1) << 3); }

__device__ __forceinline__ f32x2 gelu_mul2(f32x2 g, f32x2 u) {
    constexpr float C0 = -2.0f * 0.7978845608028654f * 1.4426950408889634f, C1 = C0 * 0.044715f;
    const f32x2 a = g * (g * g * C1 + C0);
    const f32x2 d = (f32x2){__builtin_amdgcn_exp2f(a[0]), __builtin_amdgcn_exp2f(a[1])} + 1.0f;
    return (g * u) * (f32x2){__builtin_amdgcn_rcpf(d[0]), __builtin_amdgcn_rcpf(d[1])};
}
__device__ __forceinline__ u32x4 pack_frag(const f32x2 a0, const f32x2 a1, const f32x2 b0, const f32x2 b1) {
    unsigned x0 = pk_bf16(a0[0], a0[1]), x1 = pk_bf16(a1[0], a1[1]), y0 = pk_bf16(b0[0], b0[1]), y1 = pk_bf16(b1[0], b1[1]);
    auto r0 = __builtin_amdgcn_permlane16_swap(x0, y0, false, false);
    auto r1 = __builtin_amdgcn_permlane16_swap(x1, y1, false, false);
    u32x4 o; o[0] = r0[0]; o[1] = r1[0]; o[2] = r0[1]; o[3] = r1[1];
    return o;
}
__device__ __forceinline__ int frag_kq(int lane) { const int g = lane >> 4; return ((g & 1) << 1) | (g >> 1); }
__device__ __forceinline__ int frag_off(int lane) { return (frag_kq(lane) * 16 + (lane & 15)) * 16; }

__device__ __forceinline__ float max3(float a, float b, float c) { float r; asm("v_max3_f32 %0, %1, %2, %3" : "=v"(r) : "v"(a), "v"(b), "v"(c)); return r; }
__device__ __forceinline__ float min3(float a, float b, float c) { float r; asm("v_min3_f32 %0, %1, %2, %3" : "=v"(r) : "v"(a), "v"(b), "v"(c)); return r; }
__device__ __forceinline__ float max2(float a, float b) { float r; asm("v_max_f32 %0, %1, %2" : "=v"(r) : "v"(a), "v"(b)); return r; }
template <int NU, class Op> __device__ __forceinline__ void red2_g(float (&a)[NU], float (&b)[NU], Op op) {
    float z[NU], y[NU];
#pragma unroll
    for (int u = 0; u < NU; ++u) {
        auto r = __builtin_amdgcn_permlane16_swap(__float_as_uint(a[u]), __float_as_uint(b[u]), false, false);
        z[u] = op(__uint_as_float(r[0]), __uint_as_float(r[1]));
    }
#pragma unroll
    for (int u = 0; u < NU; ++u) {
        auto q = __builtin_amdgcn_permlane32_swap(__float_as_uint(z[u]), __float_as_uint(z[u]), false, false);
        y[u] = op(__uint_as_float(q[0]), __uint_as_float(q[1]));
    }
#pragma unroll
    for (int u = 0; u < NU; ++u) {
        auto w = __builtin_amdgcn_permlane16_swap(__float_as_uint(y[u]), __float_as_uint(y[u]), false, false);
        a[u] = __uint_as_float(w[0]); b[u] = __uint_as_float(w[1]);
    }
}
template <int NU> __device__ __forceinline__ void soft_pm(const u32x4 (&w)[NU], u32x4 (&P)[NU], u32x4 (&M)[NU]) {
    constexpr float L2E = 1.4426950408889634f;
    f32x2 pv[NU][4]; float mx[NU], nm[NU];
#pragma unroll
    for (int u = 0; u < NU; ++u) {
#pragma unroll
        for (int i = 0; i < 4; ++i) pv[u][i] = (f32x2){lo_f(w[u][i]), hi_f(w[u][i])};
        mx[u] = max3(max3(max3(pv[u][0][0], pv[u][0][1], pv[u][1][0]), pv[u][1][1], pv[u][2][0]), max2(pv[u][2][1], pv[u][3][0]), pv[u][3][1]);
        nm[u] = -min3(min3(min3(pv[u][0][0], pv[u][0][1], pv[u][1][0]), pv[u][1][1], pv[u][2][0]), min3(pv[u][2][1], pv[u][3][0], pv[u][3][1]), pv[u][3][1]);
    }
    red2_g<NU>(mx, nm, [](float a, float b) { return max2(a, b); });
    f32x2 pp[NU][4], pm[NU][4]; float sp[NU], sm[NU];
#pragma unroll
    for (int u = 0; u < NU; ++u) {
        const float cp = -mx[u] * L2E, cm = -nm[u] * L2E;
#pragma unroll
        for (int i = 0; i < 4; ++i) {
            const f32x2 a = pv[u][i] * L2E + cp, b = pv[u][i] * (-L2E) + cm;
            pp[u][i] = (f32x2){__builtin_amdgcn_exp2f(a[0]), __builtin_amdgcn_exp2f(a[1])};
            pm[u][i] = (f32x2){__builtin_amdgcn_exp2f(b[0]), __builtin_amdgcn_exp2f(b[1])};
        }
        const f32x2 s2 = (pp[u][0] + pp[u][1]) + (pp[u][2] + pp[u][3]), m2 = (pm[u][0] + pm[u][1]) + (pm[u][2] + pm[u][3]);
        sp[u] = s2[0] + s2[1]; sm[u] = m2[0] + m2[1];
    }
    red2_g<NU>(sp, sm, [](float a, float b) { return a + b; });
#pragma unroll
    for (int u = 0; u < NU; ++u) {
        const float rp = __builtin_amdgcn_rcpf(sp[u]), rm = __builtin_amdgcn_rcpf(sm[u]);
#pragma unroll
        for (int i = 0; i < 4; ++i) { const f32x2 a = pp[u][i] * rp, b = pm[u][i] * rm; P[u][i] = pk_bf16(a[0], a[1]); M[u][i] = pk_bf16(b[0], b[1]); }
    }
}

__device__ __forceinline__ GAS u64* flagp(unsigned char* ws, unsigned i) { return (GAS u64*)(ws + WS_FLG + (size_t)i * 64); }
__device__ __forceinline__ void flag_set(unsigned char* ws, unsigned i, u64 tag) { __hip_atomic_store(flagp(ws, i), tag, __ATOMIC_RELAXED, __HIP_MEMORY_SCOPE_AGENT); }
__device__ __forceinline__ void flag_wait(unsigned char* ws, unsigned i0, unsigned st, int n, u64 tag, int lane) {
    GAS u64* p = flagp(ws, i0 + st * (unsigned)(lane < n ? lane : 0));
#pragma unroll 1
    for (unsigned it = 0; it < (1u << 21); ++it) {
        const u64 v = __hip_atomic_load(p, __ATOMIC_RELAXED, __HIP_MEMORY_SCOPE_AGENT);
        if (__all(v == tag)) break;
        __builtin_amdgcn_s_sleep(1);
    }
    asm volatile("" ::: "memory");
}
__device__ __forceinline__ __amdgpu_buffer_rsrc_t ws_rsrc(unsigned char* ws) { return __builtin_amdgcn_make_buffer_rsrc((void*)ws, 0, WS_END, 0x00020000); }
#define ST_SC1(v, rs, off) __builtin_amdgcn_raw_buffer_store_b128(v, rs, off, 0, 16)
#define LD_SC1(rs, off) __builtin_amdgcn_raw_buffer_load_b128(rs, off, 0, 16)

template <int GRP>
__device__ __forceinline__ void g1_mainloop(const unsigned ldsb, lds_t lds, f32x4 (&acc)[8][5], cptr (&bp)[5], const size_t ld4, cptr xn, const int wl, const int lane, const int (&ba)[5],
                                            cptr wd, cptr wo) {
    const size_t ldq = (size_t)32 * 3072 * 2, ldg = (size_t)32 * F_ * 2;
    const int aoff = GRP * 8192 + lane * 16;
    const int p0 = wl == 0 ? 0 : 5 * wl + 1;
    const unsigned av0 = p0 * 1024 + lane * 16, av1 = av0 + 4096;
    int so = 0, sl = 3 * G1_STAGE;
    cptr an = xn + 3 * 16384;
    unsigned touch = 0;
    cptr tp = wd + (size_t)(blockIdx.x * 224 + (lane & 15)) * 128;
    bf16x8 A[8], B[5];
#pragma unroll 1
    for (int t = 0; t < 32; ++t) {
        const bool more = t < 29;
        const unsigned lb = ldsb + sl + G1_SA + wl * 1024, la = ldsb + sl + p0 * 1024, rb = ldsb + so;
        if (more) {
            if (GRP == 0) {
                B[0] = lds_tr2_asm<0>(rb + ba[0]); B[1] = lds_tr2_asm<0>(rb + ba[1]);
                glds_v(bp[0], lb);
                B[2] = lds_tr2_asm<0>(rb + ba[2]); B[3] = lds_tr2_asm<0>(rb + ba[3]);
                glds_v(bp[1], lb + 4096);
                B[4] = lds_tr2_asm<0>(rb + ba[4]);
                A[0] = lds_b128(lds + so + aoff); A[1] = lds_b128(lds + so + aoff + 1024);
                glds_v(bp[2], lb + 8192);
                A[2] = lds_b128(lds + so + aoff + 2048); A[3] = lds_b128(lds + so + aoff + 3072); A[4] = lds_b128(lds + so + aoff + 4096);
                glds_v(bp[3], lb + 12288);
                A[5] = lds_b128(lds + so + aoff + 5120); A[6] = lds_b128(lds + so + aoff + 6144); A[7] = lds_b128(lds + so + aoff + 7168);
                glds_v(bp[4], lb + 16384);
                bp[0] += ldq; bp[1] += ldg; bp[2] += ldg; bp[3] += ldg; bp[4] += ld4;
                WAIT_L0(); WAIT_V(10);
            } else if (wl < 3) {
                B[0] = lds_tr2_asm<0>(rb + ba[0]); B[1] = lds_tr2_asm<0>(rb + ba[1]);
                glds_s_sc1_first<0>(av0, an, la);
                B[2] = lds_tr2_asm<0>(rb + ba[2]); B[3] = lds_tr2_asm<0>(rb + ba[3]);
                glds_s_sc1_next<1024>(av0, an);
                B[4] = lds_tr2_asm<0>(rb + ba[4]);
                A[0] = lds_b128(lds + so + aoff); A[1] = lds_b128(lds + so + aoff + 1024);
                glds_s_sc1_next<2048>(av0, an);
                A[2] = lds_b128(lds + so + aoff + 2048); A[3] = lds_b128(lds + so + aoff + 3072);
                glds_s_sc1_next<3072>(av0, an);
                A[4] = lds_b128(lds + so + aoff + 4096); A[5] = lds_b128(lds + so + aoff + 5120);
                glds_s_sc1_first<0>(av1, an, la + 4096);
                A[6] = lds_b128(lds + so + aoff + 6144); A[7] = lds_b128(lds + so + aoff + 7168);
                if (wl == 0) glds_s_sc1_next<1024>(av1, an);
                an += 16384;
                WAIT_L0();
                if (wl == 0) WAIT_V(12); else WAIT_V(10);
            } else {
                if (t < 18) {
                    asm volatile("global_load_dword %0, %1, off" : "+v"(touch) : "v"(tp) : "memory");
                    if (t == 13) tp = wo + (size_t)(blockIdx.x * 64 + (lane & 15)) * 128; else tp += 2048;
                }
#pragma unroll
                for (int n = 0; n < 5; ++n) B[n] = lds_tr2_asm<0>(rb + ba[n]);
#pragma unroll
                for (int m = 0; m < 8; ++m) A[m] = lds_b128(lds + so + aoff + m * 1024);
                WAIT_L0();
            }
        } else {
#pragma unroll
            for (int n = 0; n < 5; ++n) B[n] = lds_tr2_asm<0>(rb + ba[n]);
#pragma unroll
            for (int m = 0; m < 8; ++m) A[m] = lds_b128(lds + so + aoff + m * 1024);
            WAIT_L0(); WAIT_V(0);
        }
        so = (so == 3 * G1_STAGE) ? 0 : so + G1_STAGE;
        sl = (sl == 3 * G1_STAGE) ? 0 : sl + G1_STAGE;
        asm volatile("" : "+s"(so), "+s"(sl));
        SCHED(); BAR(); SCHED();
        __builtin_amdgcn_s_setprio(1);
#pragma unroll
        for (int m = 0; m < 8; ++m)
#pragma unroll
            for (int n = 0; n < 5; ++n) { const int nn = (m & 1) ? 4 - n : n; MFMA_ACC(acc[m][nn], B[nn], A[m]); }
        __builtin_amdgcn_s_setprio(0);
        SCHED(); BAR(); SCHED();
    }
    asm volatile("" ::"v"(touch));
}

__device__ __forceinline__ void g2_issueB(unsigned bvoff, cptr sb, unsigned l) {
    glds_s(bvoff, sb, l);
    glds_s_o128(bvoff, sb, l + 4096);
}

__global__ __launch_bounds__(512, 2) void k_fused(const u16* __restrict__ x, const u16* __restrict__ wqkv, const u16* __restrict__ wphi, const u16* __restrict__ wo,
                                                  const u16* __restrict__ wg, const u16* __restrict__ wu, const u16* __restrict__ wd,
                                                  unsigned char* __restrict__ ws, u16* __restrict__ out) {
    extern __shared__ __attribute__((aligned(16))) unsigned char shm[];
    lds_t lds = (lds_t)shm;
    const unsigned ldsb = (unsigned)(size_t)lds;
    const int wid = __builtin_amdgcn_readfirstlane(threadIdx.x >> 6);
    const int grp = wid >> 2, wc = wid & 3;
    const int x8 = blockIdx.x & 7, r = blockIdx.x >> 3, ks = x8 >> 1, panel = 4 * (x8 & 1) + (r >> 3), c8 = r & 7, j = 4 * ks + (c8 >> 1), kvt = c8 & 1;
    const __amdgpu_buffer_rsrc_t rs = ws_rsrc(ws);
    STAMP_DECL; STAMP(0);
    u64 tag;
    f32x4 acc[8][5];

    {
        const int lane = threadIdx.x & 63, g = lane >> 4;
        const int row = panel * 256 + (ks * 8 + c8) * 8 + (lane & 7);
        u32x4 xv[4]; u32x2 ev = {0, 0};
#pragma unroll
        for (int i = 0; i < 4; ++i) xv[i] = (u32x4){0, 0, 0, 0};
        {
            const unsigned zero = 0;
            asm volatile("global_load_dwordx2 %0, %1, %2 sc1" : "+v"(ev) : "v"(zero), "s"(ws + WS_FLG + FL_EPOCH * 64) : "memory");
        }
        unsigned tlbt = 0, codet = 0;
        cptr bp[5];
        const size_t ldq = (size_t)32 * 3072 * 2, ldg = (size_t)32 * F_ * 2, ld4 = kvt ? ldq : ldg;
        if (grp == 0) {
            const int gq = (j & 1) ? 7 * j + 3 : 7 * j, gp = (j & 1) ? 7 * j + 1 : 7 * j + 4, gh = (j & 1) ? 7 * j : 7 * j + 6;
            const int krow = 8 * wc + (lane >> 3), chunk = (lane & 7) ^ swzB(krow);
            const size_t rq = (size_t)krow * 3072 * 2 + chunk * 16, rg = (size_t)krow * F_ * 2 + chunk * 16;
            if (!kvt) {
                bp[0] = (cptr)(wqkv + 64 * j) + rq;
                bp[1] = (cptr)(wg + 32 * gq) + rg;      bp[2] = (cptr)(wg + 32 * gq + 64) + rg;
                bp[3] = (cptr)(wu + 32 * gq) + rg;      bp[4] = (cptr)(wu + 32 * gq + 64) + rg;
            } else {
                bp[0] = (cptr)(wqkv + 1024 + 64 * j) + rq;
                bp[1] = (cptr)(wg + 32 * gp) + rg;
                bp[2] = (chunk < 4 ? (cptr)(wg + 32 * gh) : (cptr)(wu + 32 * gh) - 64) + rg;
                bp[3] = (cptr)(wu + 32 * gp) + rg;
                bp[4] = (cptr)(wqkv + 2048 + 64 * j) + rq;
            }
#pragma unroll
            for (int s = 0; s < 3; ++s) {
#pragma unroll
                for (int c = 0; c < 5; ++c) glds_v(bp[c], ldsb + s * G1_STAGE + G1_SA + c * 4096 + wc * 1024);
                bp[0] += ldq; bp[1] += ldg; bp[2] += ldg; bp[3] += ldg; bp[4] += ld4;
            }
            glds_v((cptr)(wphi + (size_t)(2 * j) * 1024 + wc * 512 + lane * 8), ldsb + L_WPHI + wc * 1024);
            {
                const int e = r * 40 + wc * 10 + (lane < 10 ? lane : 9);
                cptr p;
                if (e < 96) p = (cptr)wqkv + (size_t)e * 65536;
                else if (e < 208) p = (cptr)wg + (size_t)(e - 96) * 65536;
                else if (e < 320) p = (cptr)wu + (size_t)(e - 208) * 65536;
                else if (e < 432) p = (cptr)wd + (size_t)(e - 320) * 65536;
                else if (e < 464) p = (cptr)wo + (size_t)(e - 432) * 65536;
                else if (e < 528) p = (cptr)out + (size_t)(e - 464) * 65536;
                else if (e < 592) p = (cptr)x + (size_t)(e - 528) * 65536;
                else p = (cptr)ws + (size_t)min(e - 592, (int)(WS_END / 65536)) * 65536;
                asm volatile("global_load_dword %0, %1, off sc1" : "+v"(tlbt) : "v"(p) : "memory");
            }
            {
                u64 pc; asm volatile("s_getpc_b64 %0" : "=s"(pc));
                const int ln_ = wc * 64 + lane;
                cptr p = (cptr)(pc & ~(u64)0x7f) + (ln_ < CODE_LINES ? ln_ : CODE_LINES - 1) * 128;
                asm volatile("global_load_dword %0, %1, off" : "+v"(codet) : "v"(p) : "memory");
            }
        } else {
#pragma unroll
            for (int c = 0; c < 5; ++c) bp[c] = nullptr;
            const unsigned xo = (unsigned)(row * D_ + (wc * 8 + (lane >> 3)) * 8) * 2u;
            asm volatile("global_load_dwordx4 %0, %4, %5\n\tglobal_load_dwordx4 %1, %4, %5 offset:512\n\tglobal_load_dwordx4 %2, %4, %5 offset:1024\n\tglobal_load_dwordx4 %3, %4, %5 offset:1536\n\t"
                         "s_waitcnt vmcnt(0)" : "+v"(xv[0]), "+v"(xv[1]), "+v"(xv[2]), "+v"(xv[3]) : "v"(xo), "s"(x) : "memory");
        }
        asm volatile("s_waitcnt vmcnt(" XW_CNT ")" : "+v"(ev)::"memory");
        {
            u64 z = (((u64)(unsigned)__builtin_amdgcn_readfirstlane(ev[1]) << 32) | (unsigned)__builtin_amdgcn_readfirstlane(ev[0])) + 0x9E3779B97F4A7C15ull;
            z = (z ^ (z >> 30)) * 0xBF58476D1CE4E5B9ull; z = (z ^ (z >> 27)) * 0x94D049BB133111EBull; tag = z ^ (z >> 31);
        }
        STAMP(1);
        LAS f32x2* red = (LAS f32x2*)(lds + L_MISC);
        float f[32];
        if (grp == 1) {
            float s = 0.f;
#pragma unroll
            for (int i = 0; i < 4; ++i)
#pragma unroll
                for (int k = 0; k < 4; ++k) { f[8 * i + 2 * k] = lo_f(xv[i][k]); f[8 * i + 2 * k + 1] = hi_f(xv[i][k]); }
            { f32x2 s2 = {0.f, 0.f};
#pragma unroll
              for (int i = 0; i < 16; ++i) s2 += (f32x2){f[2 * i], f[2 * i + 1]};
              s = s2[0] + s2[1]; }
            s += ror8(s); s = sum_g(s);
            const float mw = s * (1.0f / 256.0f);
            f32x2 q2 = {0.f, 0.f};
#pragma unroll
            for (int i = 0; i < 16; ++i) { const f32x2 d = (f32x2){f[2 * i], f[2 * i + 1]} - mw; q2 += d * d; }
            float q = q2[0] + q2[1];
            q += ror8(q); q = sum_g(q);
            if (lane < 8) red[wc * 8 + lane] = (f32x2){s, q};
        }
        LBAR();
        if (grp == 1) {
            f32x2 p[4];
#pragma unroll
            for (int w = 0; w < 4; ++w) p[w] = red[w * 8 + (lane & 7)];
            const float mean = ((p[0][0] + p[1][0]) + (p[2][0] + p[3][0])) * (1.0f / D_);
            float m2 = (p[0][1] + p[1][1]) + (p[2][1] + p[3][1]);
#pragma unroll
            for (int w = 0; w < 4; ++w) { const float dm = p[w][0] * (1.0f / 256.0f) - mean; m2 += 256.0f * dm * dm; }
            const float var = m2 * (1.0f / D_);
            const float m = rbf(mean), sd = rbf(sqrtf(rbf(rbf(var) + 1e-5f))), rsd = 1.0f / sd;
#pragma unroll
            for (int it = 0; it < 4; ++it) {
                const int pc = it * 32 + wc * 8 + (lane >> 3), kc = pc >> 2, kq = pc & 3;
                u32x4 o;
#pragma unroll
                for (int i = 0; i < 4; ++i) {
                    const unsigned w = pk_bf16(f[it * 8 + 2 * i] - m, f[it * 8 + 2 * i + 1] - m);
                    const f32x2 a = {lo_f(w), hi_f(w)}, q0 = a * rsd;
                    const f32x2 q1 = __builtin_elementwise_fma(__builtin_elementwise_fma(-q0, (f32x2){sd, sd}, a), (f32x2){rsd, rsd}, q0);
                    o[i] = pk_bf16(q1[0], q1[1]);
                }
                ST_SC1(o, rs, WS_XN + (unsigned)(((panel * 32 + kc) * 16 + ((row >> 4) & 15)) * 1024 + (kq * 16 + (row & 15)) * 16));
            }
            WAIT_V(0);
        }
        LBAR();
        STAMP(2);
        if (threadIdx.x == 256) flag_set(ws, FL_LN + panel * 32 + ks * 8 + c8, tag);
        if (wid == 0) flag_wait(ws, FL_LN + panel * 32, 1, 32, tag, lane);
        LBAR();
        STAMP(3);

        cptr xn = uni((cptr)ws + WS_XN + (size_t)panel * 32 * 16384);
        if (grp == 1 && wc < 3) {
            const int p0 = wc == 0 ? 0 : 5 * wc + 1;
            const unsigned av0 = p0 * 1024 + lane * 16, av1 = av0 + 4096;
#pragma unroll
            for (int s3 = 0; s3 < 3; ++s3) {
                const unsigned la = ldsb + s3 * G1_STAGE + p0 * 1024;
                glds_s_sc1_first<0>(av0, xn + s3 * 16384, la); glds_s_sc1_next<1024>(av0, xn + s3 * 16384);
                glds_s_sc1_next<2048>(av0, xn + s3 * 16384);   glds_s_sc1_next<3072>(av0, xn + s3 * 16384);
                glds_s_sc1_first<0>(av1, xn + s3 * 16384, la + 4096);
                if (wc == 0) glds_s_sc1_next<1024>(av1, xn + s3 * 16384);
            }
            if (wc == 0) WAIT_V(12); else WAIT_V(10);
        }
        int ba[5];
        {
            int cgA, tlA, cgB, tlB;
            if (!kvt) { cgA = 1 + (wc >> 1); tlA = 2 * (wc & 1); cgB = 3 + (wc >> 1); tlB = tlA; }
            else if (wc < 2) { cgA = 1; tlA = 2 * wc; cgB = 3; tlB = 2 * wc; }
            else if (wc == 2) { cgA = 2; tlA = 0; cgB = 2; tlB = 2; }
            else { cgA = 4; tlA = 0; cgB = 4; tlB = 2; }
            const int q4 = (lane >> 2) & 3, p = lane & 3, krow = 8 * g + q4, sw = swzB(krow);
            auto adr = [&](int cg, int tl) { return G1_SA + cg * 4096 + krow * 128 + (((2 * tl + (p >> 1)) ^ sw) << 4) + (p & 1) * 8; };
            ba[0] = adr(cgA, tlA); ba[1] = adr(cgA, tlA + 1); ba[2] = adr(cgB, tlB); ba[3] = adr(cgB, tlB + 1); ba[4] = adr(0, wc);
        }
#pragma unroll
        for (int m = 0; m < 8; ++m)
#pragma unroll
            for (int n = 0; n < 5; ++n) acc[m][n] = (f32x4){0.f, 0.f, 0.f, 0.f};
        if (grp == 0) asm volatile("s_waitcnt vmcnt(0)" : "+v"(tlbt), "+v"(codet)::"memory");
        BAR();
        STAMP(4);
        if (grp == 0) { g1_mainloop<0>(ldsb, lds, acc, bp, ld4, xn, wc, lane, ba, (cptr)wd, (cptr)wo); BAR(); }
        else          { BAR(); g1_mainloop<1>(ldsb, lds, acc, bp, ld4, xn, wc, lane, ba, (cptr)wd, (cptr)wo); }
        STAMP(5);
    }

    int tz = threadIdx.x; asm volatile("" : "+v"(tz));
    const int lane = tz & 63, g = lane >> 4, l15 = lane & 15;
    const unsigned bvoff = (unsigned)((8 * wc + (lane >> 3)) * 2048 + c8 * 256 + (((lane & 7) ^ swzB(8 * wc + (lane >> 3))) << 4));
    const unsigned bl = ldsb + G2_SB + grp * 24576 + wc * 1024;
    cptr sbp = uni((cptr)wd + ((size_t)(28 * ks + grp) << 16));
    cptr sa = uni((cptr)ws + WS_H + ((size_t)(panel * NKC2 + 28 * ks + grp) << 14));
    const unsigned al = ldsb + grp * 49152 + wc * 4096;
    auto startA = [&]() {
        int tz4 = threadIdx.x; asm volatile("" : "+v"(tz4));
        const unsigned avoff = (unsigned)(wc * 4096 + (tz4 & 63) * 16);
#pragma unroll
        for (int s2 = 0; s2 < 2; ++s2) {
            glds_s_sc1_first<0>(avoff, sa, al + s2 * 16384); glds_s_sc1_next<1024>(avoff, sa); glds_s_sc1_next<2048>(avoff, sa); glds_s_sc1_next<3072>(avoff, sa);
            sa += 32768;
        }
    };
    auto prefetchB = [&]() { g2_issueB(bvoff, sbp, bl); g2_issueB(bvoff, sbp + 131072, bl + 8192); sbp += 262144; };
    {
        const int gq = (j & 1) ? 7 * j + 3 : 7 * j, gp = (j & 1) ? 7 * j + 1 : 7 * j + 4, gh = (j & 1) ? 7 * j : 7 * j + 6;
        {
            lds_t kp = lds + E_KP + (128 * grp + l15) * 256 + ((((wc >> 1) * 4 + (wc & 1) * 2 + (g >> 1)) ^ l15) << 4) + (g & 1) * 8;
#pragma unroll
            for (int m = 0; m < 8; ++m) *(LAS u32x2*)(kp + m * 4096) = (u32x2){pk_bf16(acc[m][4][0], acc[m][4][1]), pk_bf16(acc[m][4][2], acc[m][4][3])};
        }
        const bool has_h = !(kvt && wc == 3);
        const int hkc = !kvt ? gq + wc : (wc < 2 ? gp + wc : gh);
        const unsigned hoff = WS_H + (unsigned)(((panel * NKC2 + hkc) * 16 + 8 * grp) * 1024 + frag_off(lane));
        auto geglu_c = [&](int m) {
            const f32x2 a0 = gelu_mul2(acc[m][0].lo, acc[m][2].lo), a1 = gelu_mul2(acc[m][0].hi, acc[m][2].hi);
            const f32x2 b0 = gelu_mul2(acc[m][1].lo, acc[m][3].lo), b1 = gelu_mul2(acc[m][1].hi, acc[m][3].hi);
            return pack_frag(a0, a1, b0, b1);
        };
        auto geglu = [&](int m) { ST_SC1(geglu_c(m), rs, hoff + m * 1024); SCHED(); };
        const int q4 = (lane >> 2) & 3, p4 = lane & 3;
        auto logits = [&](u32x4 (&lg)[4]) {
            lds_t wp = lds + L_WPHI + (8 * g + q4) * 64 + p4 * 8;
            lds_t qp = lds + E_KP + (32 * wid + l15) * 256;
            bf16x8 wf[2][2], qf[4];
#pragma unroll
            for (int hh = 0; hh < 2; ++hh) {
                wf[hh][0] = lds_tr2(wp + hh * 2048, wp + hh * 2048 + 256); wf[hh][1] = lds_tr2(wp + hh * 2048 + 32, wp + hh * 2048 + 288);
                qf[hh * 2] = lds_b128(qp + (((hh * 4 + g) ^ l15) << 4)); qf[hh * 2 + 1] = lds_b128(qp + 4096 + (((hh * 4 + g) ^ l15) << 4));
            }
            SCHED();
            const f32x4 z4 = {0.f, 0.f, 0.f, 0.f};
            f32x4 p0[4], p1[4];
#pragma unroll
            for (int u = 0; u < 4; ++u) { p0[u] = MFMA(wf[u >> 1][0], qf[u], z4); p1[u] = MFMA(wf[u >> 1][1], qf[u], z4); }
            SCHED();
#pragma unroll
            for (int u = 0; u < 4; ++u) lg[u] = (u32x4){pk_bf16(p0[u][0], p0[u][1]), pk_bf16(p0[u][2], p0[u][3]), pk_bf16(p1[u][0], p1[u][1]), pk_bf16(p1[u][2], p1[u][3])};
            asm volatile("" : "+v"(lg[0]), "+v"(lg[1]), "+v"(lg[2]), "+v"(lg[3]));
        };
        if (kvt) {
            LBAR();
            if (wc == 3) {
                const int sw = swzP(l15);
                lds_t vp = lds + E_V + (128 * grp + l15) * 128 + (g & 1) * 8;
#pragma unroll
                for (int n = 0; n < 4; ++n) {
                    lds_t vn = vp + (((2 * n + (g >> 1)) ^ sw) << 4);
#pragma unroll
                    for (int m = 0; m < 8; ++m) *(LAS u32x2*)(vn + m * 2048) = (u32x2){pk_bf16(acc[m][n][0], acc[m][n][1]), pk_bf16(acc[m][n][2], acc[m][n][3])};
                }
            }
            STAMP(6);
            u32x4 lg[4], P[4], M[4];
            logits(lg);
            WAIT_L0();
            soft_pm<4>(lg, P, M);
            {
                const int sw = swzF(l15);
                lds_t pb = lds + E_KP + (32 * wid + l15) * 256 + (g & 1) * 8;
#pragma unroll
                for (int u = 0; u < 4; ++u) {
                    const int c0 = (u >> 1) * 8 + (g >> 1), tb = u & 1;
                    *(LAS u32x2*)(pb + tb * 4096 + (((c0 + 0) ^ sw) << 4)) = (u32x2){P[u][0], P[u][1]};
                    *(LAS u32x2*)(pb + tb * 4096 + (((c0 + 2) ^ sw) << 4)) = (u32x2){P[u][2], P[u][3]};
                    *(LAS u32x2*)(pb + tb * 4096 + (((c0 + 4) ^ sw) << 4)) = (u32x2){M[u][0], M[u][1]};
                    *(LAS u32x2*)(pb + tb * 4096 + (((c0 + 6) ^ sw) << 4)) = (u32x2){M[u][2], M[u][3]};
                }
            }
            LBAR();
            STAMP(7);
            {
                const int hh = wid >> 2, ft = wid & 3;
                f32x4 kv0 = {0.f, 0.f, 0.f, 0.f}, kv1 = kv0, zz = kv0;
                const bf16x8 ones = {0x3F80, 0x3F80, 0x3F80, 0x3F80, 0x3F80, 0x3F80, 0x3F80, 0x3F80};
                const int t0 = 8 * g + q4, t1 = t0 + 4, cp = hh * 8 + 2 * ft + (p4 >> 1), cv = hh * 4 + (p4 >> 1), hb8 = (p4 & 1) * 8;
                lds_t pf0 = lds + E_KP + t0 * 256 + ((cp ^ swzF(t0 & 15)) << 4) + hb8, pf1 = lds + E_KP + t1 * 256 + ((cp ^ swzF(t1 & 15)) << 4) + hb8;
                lds_t va0 = lds + E_V + t0 * 128 + ((cv ^ swzP(t0)) << 4) + hb8, va1 = lds + E_V + t1 * 128 + ((cv ^ swzP(t1)) << 4) + hb8;
                lds_t vb0 = lds + E_V + t0 * 128 + (((cv + 2) ^ swzP(t0)) << 4) + hb8, vb1 = lds + E_V + t1 * 128 + (((cv + 2) ^ swzP(t1)) << 4) + hb8;
#pragma unroll
                for (int k8 = 0; k8 < 8; ++k8) {
                    const bf16x8 pf = lds_tr2(pf0 + k8 * 8192, pf1 + k8 * 8192), v0 = lds_tr2(va0 + k8 * 4096, va1 + k8 * 4096), v1 = lds_tr2(vb0 + k8 * 4096, vb1 + k8 * 4096);
                    kv0 = MFMA(pf, v0, kv0); kv1 = MFMA(pf, v1, kv1); zz = MFMA(pf, ones, zz);
                }
                ST_SC1(((u32x4){pk_bf16(kv0[0], kv0[1]), pk_bf16(kv0[2], kv0[3]), pk_bf16(kv1[0], kv1[1]), pk_bf16(kv1[2], kv1[3])}), rs,
                       WS_PART + (unsigned)(((panel * 32 + 2 * j + hh) * 4 + ft) * 1024 + lane * 16));
                if (l15 == 0) ST_SC1(__builtin_bit_cast(u32x4, zz), rs, WS_PZ + (unsigned)(((panel * 32 + 2 * j + hh) * 64 + 16 * ft + 4 * g) * 4));
            }
            SCHED();
            u32x4 h0 = {0, 0, 0, 0};
            if (has_h) h0 = geglu_c(0);
            WAIT_V(0);
            LBAR();
            if (threadIdx.x == 0) flag_set(ws, FL_KV + j * 8 + panel, tag);
            STAMP(8);
            prefetchB();
            if (has_h) {
                ST_SC1(h0, rs, hoff); SCHED();
                geglu(1); geglu(2); geglu(3); geglu(4); geglu(5); geglu(6); geglu(7);
            }
            WAIT_V(0);
            LBAR();
            if (threadIdx.x == 0) flag_set(ws, FL_H + (panel * 4 + ks) * 8 + c8, tag);
            STAMP(9);
        } else {
            u32x4 lg[4], P[4], M[4];
            LBAR();
            logits(lg);
            soft_pm<4>(lg, P, M);
#pragma unroll
            for (int u = 0; u < 4; ++u) asm volatile("" : "+v"(P[u]), "+v"(M[u]));
            SCHED();
            STAMP(6);
#pragma unroll
            for (int m = 0; m < 8; ++m) geglu(m);
            prefetchB();
            WAIT_V(4);
            LBAR();
            if (threadIdx.x == 0) flag_set(ws, FL_H + (panel * 4 + ks) * 8 + c8, tag);
            STAMP(7);
            if (wid == 0) flag_wait(ws, FL_KV + j * 8, 1, 8, tag, lane);
            if (blockIdx.x == 0 && wid == 1) {
                GAS u64* p = flagp(ws, FL_LN + lane);
#pragma unroll 1
                for (unsigned it = 0; it < (1u << 21); ++it) {
                    const u64 v0 = __hip_atomic_load(p, __ATOMIC_RELAXED, __HIP_MEMORY_SCOPE_AGENT), v1 = __hip_atomic_load(p + 64 * 8, __ATOMIC_RELAXED, __HIP_MEMORY_SCOPE_AGENT);
                    const u64 v2 = __hip_atomic_load(p + 128 * 8, __ATOMIC_RELAXED, __HIP_MEMORY_SCOPE_AGENT), v3 = __hip_atomic_load(p + 192 * 8, __ATOMIC_RELAXED, __HIP_MEMORY_SCOPE_AGENT);
                    if (__all(v0 == tag && v1 == tag && v2 == tag && v3 == tag)) { if (lane == 0) flag_set(ws, FL_EPOCH, tag); break; }
                    __builtin_amdgcn_s_sleep(1);
                }
            }
            LBAR();
            STAMP(8);
            u32x2 hfl = {0, 0};
            {
                const int hh = wid >> 2, ft = wid & 3;
                asm volatile("global_load_dwordx2 %0, %1, %2 sc1" : "+v"(hfl) : "v"((FL_H + (panel * 4 + ks) * 8 + (lane & 7)) * 64), "s"(ws + WS_FLG) : "memory");
                f32x4 a0, a1, az;
                u32x4 pw[8]; f32x4 pz[8];
#pragma unroll
                for (int p = 0; p < 8; ++p) {
                    pw[p] = LD_SC1(rs, WS_PART + (unsigned)(((p * 32 + 2 * j + hh) * 4 + ft) * 1024 + lane * 16));
                    pz[p] = __builtin_bit_cast(f32x4, LD_SC1(rs, WS_PZ + (unsigned)(((p * 32 + 2 * j + hh) * 64 + 16 * ft + 4 * g) * 4)));
                }
#pragma unroll
                for (int p = 0; p < 8; ++p) {
                    const f32x4 b0 = {lo_f(pw[p][0]), hi_f(pw[p][0]), lo_f(pw[p][1]), hi_f(pw[p][1])}, b1 = {lo_f(pw[p][2]), hi_f(pw[p][2]), lo_f(pw[p][3]), hi_f(pw[p][3])};
                    if (p == 0) { a0 = b0; a1 = b1; az = pz[0]; } else { a0 += b0; a1 += b1; az += pz[p]; }
                }
                lds_t kb = lds + L_KVL + ((hh * 2 + (ft >> 1)) * 3) * 1024 + lane * 16 + (ft & 1) * 8;
                *(LAS u32x2*)(kb) = (u32x2){pk_bf16(a0[0], a0[1]), pk_bf16(a0[2], a0[3])};
                *(LAS u32x2*)(kb + 1024) = (u32x2){pk_bf16(a1[0], a1[1]), pk_bf16(a1[2], a1[3])};
                *(LAS u32x2*)(kb + 2048) = (u32x2){pk_bf16(az[0], az[1]), pk_bf16(az[2], az[3])};
            }
            LBAR();
            STAMP(9);
            {
                asm volatile("" : "+v"(hfl));
                const u64 v = ((u64)hfl[1] << 32) | hfl[0];
                if (!__all(v == tag)) flag_wait(ws, FL_H + (panel * 4 + ks) * 8, 1, 8, tag, lane);
                startA();
            }
#pragma unroll
            for (int hh = 0; hh < 2; ++hh) {
                bf16x8 kf[2][3];
#pragma unroll
                for (int s = 0; s < 2; ++s)
#pragma unroll
                    for (int d = 0; d < 3; ++d) kf[s][d] = lds_b128(lds + L_KVL + ((hh * 2 + s) * 3 + d) * 1024 + lane * 16);
#pragma unroll
                for (int tb = 0; tb < 2; ++tb) {
                    const bf16x8 Pf = __builtin_bit_cast(bf16x8, P[hh * 2 + tb]), Mf = __builtin_bit_cast(bf16x8, M[hh * 2 + tb]);
                    const f32x4 z4 = {0.f, 0.f, 0.f, 0.f};
                    const f32x4 n0 = MFMA(kf[1][0], Mf, MFMA(kf[0][0], Pf, z4));
                    const f32x4 n1 = MFMA(kf[1][1], Mf, MFMA(kf[0][1], Pf, z4));
                    const f32x4 dn = MFMA(kf[1][2], Mf, MFMA(kf[0][2], Pf, z4));
                    const float rden = __builtin_amdgcn_rcpf(rbf(rbf(dn[0]) + 1e-6f));
                    ST_SC1(pack_frag(n0.lo * rden, n0.hi * rden, n1.lo * rden, n1.hi * rden), rs, WS_H + (unsigned)(((panel * NKC2 + 112 + 2 * j + hh) * 16 + 2 * wid + tb) * 1024 + frag_off(lane)));
                }
            }
        }
    }
    STAMP(10);
    STAMP_OUT(0, 0, 11);

    STAMP(0);
    if (kvt) {
        if (wid == 0) flag_wait(ws, FL_H + (panel * 4 + ks) * 8, 1, 8, tag, lane);
        LBAR();
        startA();
    }
    STAMP(1);
    f32x4 c2[8][4];
    {
        int tz3 = threadIdx.x; asm volatile("" : "+v"(tz3));
        const int lane = tz3 & 63, g = lane >> 4;
        const unsigned avoff = (unsigned)(wc * 4096 + lane * 16);
        const unsigned bvoff = (unsigned)((8 * wc + (lane >> 3)) * 2048 + c8 * 256 + (((lane & 7) ^ swzB(8 * wc + (lane >> 3))) << 4));
        const int wm = wc >> 1, wn = wc & 1;
        unsigned bb[4];
        {
            const int q4 = (lane >> 2) & 3, p = lane & 3, kr = 8 * g + q4, sw = swzB(kr);
#pragma unroll
            for (int tl = 0; tl < 4; ++tl) bb[tl] = ldsb + G2_SB + grp * 24576 + wn * 4096 + kr * 128 + (((2 * tl + (p >> 1)) ^ sw) << 4) + (p & 1) * 8;
        }
        lds_t ap = lds + grp * 49152 + wm * 8192 + lane * 16;
        u32x2 ofl = {0, 0};
        const unsigned fo = (FL_O + (panel * 4 + ks) * 4 + (lane & 3)) * 64;
        if (kvt) WAIT_V(4); else WAIT_V(8);
        LBAR();
        if (grp == 1) BAR();
        STAMP(2);
        auto step = [&](auto uc, auto fc, const int t) {
            constexpr int U = decltype(uc)::value, W = (U + 2) % 3;
            constexpr bool FIRST = decltype(fc)::value;
            bf16x8 A[8], B[4];
#define FST(x)
            FST(f0);
            if (U == 0) {
                if (t == 12) {
                    asm volatile("" : "+v"(ofl));
                    const u64 v = ((u64)ofl[1] << 32) | ofl[0];
                    if (!__all(v == tag)) flag_wait(ws, FL_O + (panel * 4 + ks) * 4, 1, 4, tag, lane);
                    sa = uni((cptr)ws + WS_H + ((size_t)(panel * NKC2 + 112 + 8 * ks + grp) << 14));
                    sbp = uni((cptr)wo + ((size_t)(8 * ks + grp) << 16));
                }
                asm volatile("s_cmp_lg_u32 %3, 9\n\ts_cbranch_scc1 .Lofl_%=\n\tglobal_load_dwordx2 %0, %1, %2 sc1\n.Lofl_%=:" : "+v"(ofl) : "v"(fo), "s"(ws + WS_FLG), "s"(t) : "scc", "memory");
            }
            if (U == 2 && t == 2 && !kvt && threadIdx.x == 0) flag_set(ws, FL_O + (panel * 4 + ks) * 4 + (c8 >> 1), tag);
            const bool more = U == 0 || t < 16;
            B[0] = lds_tr2_asm<U * 8192>(bb[0]); B[1] = lds_tr2_asm<U * 8192>(bb[1]);
            if (more) glds_s(bvoff, sbp, bl + W * 8192);
            B[2] = lds_tr2_asm<U * 8192>(bb[2]); B[3] = lds_tr2_asm<U * 8192>(bb[3]);
            if (more) { glds_s_o128(bvoff, sbp, bl + W * 8192 + 4096); sbp += 131072; }
            A[0] = lds_b128(ap + U * 16384); A[1] = lds_b128(ap + U * 16384 + 1024);
            if (more) { glds_s_sc1_first<0>(avoff, sa, al + W * 16384); glds_s_sc1_next<1024>(avoff, sa); }
            A[2] = lds_b128(ap + U * 16384 + 2048); A[3] = lds_b128(ap + U * 16384 + 3072); A[4] = lds_b128(ap + U * 16384 + 4096);
            if (more) { glds_s_sc1_next<2048>(avoff, sa); glds_s_sc1_next<3072>(avoff, sa); sa += 32768; }
            A[5] = lds_b128(ap + U * 16384 + 5120); A[6] = lds_b128(ap + U * 16384 + 6144); A[7] = lds_b128(ap + U * 16384 + 7168);
            FST(f1);
            WAIT_L0();
            FST(f2);
            if (!more) WAIT_V(0);
            SCHED(); BAR(); SCHED();
            FST(f3);
            __builtin_amdgcn_s_setprio(1);
#pragma unroll
            for (int m = 0; m < 8; ++m)
#pragma unroll
                for (int n = 0; n < 4; ++n) { const int nn = (m & 1) ? 3 - n : n; if (FIRST) MFMA_SET(c2[m][nn], B[nn], A[m]); else MFMA_ACC(c2[m][nn], B[nn], A[m]); }
            __builtin_amdgcn_s_setprio(0);
            FST(f4);
            WAIT_V(6);
            FST(f5);
            SCHED(); BAR(); SCHED();
        };
        step(std::integral_constant<int, 0>{}, std::true_type{}, 0);
#pragma unroll 1
        for (int t = 1; t < 18; t += 3) {
            step(std::integral_constant<int, 1>{}, std::false_type{}, t); step(std::integral_constant<int, 2>{}, std::false_type{}, t + 1);
            if (t < 16) step(std::integral_constant<int, 0>{}, std::false_type{}, t + 2);
        }
        if (grp == 0) BAR();
    }
    STAMP(3);
    {
        int tz2 = threadIdx.x; asm volatile("" : "+v"(tz2));
        const int ln = tz2 & 63, tile = panel * 8 + c8, wm = wc >> 1, wn = wc & 1;
        const int qk = 2 * wm + grp, qs = qk ^ 1;
        const bool publisher = qk != ks;
        const int mh = wid >> 2, nq = wid & 3;
        const int row0 = panel * 256 + ks * 64 + mh * 32 + (ln & 15), col0 = c8 * 128 + nq * 32 + 8 * frag_kq(ln);
        constexpr int L_OWN = 0, L_SEND = 65536;
        u32x4 xr0 = {0, 0, 0, 0}, xr1 = xr0;
        {
            const unsigned xo = (unsigned)(row0 * D_ + col0) * 2u;
            asm volatile("global_load_dwordx4 %0, %1, %2" : "+v"(xr0) : "v"(xo), "s"(x) : "memory");
            asm volatile("global_load_dwordx4 %0, %1, %2" : "+v"(xr1) : "v"(xo + 16 * D_ * 2), "s"(x) : "memory");
        }
        f32x4 hk[4][4], hs[4][4];
#pragma unroll
        for (int m = 0; m < 4; ++m)
#pragma unroll
            for (int n = 0; n < 4; ++n) { hk[m][n] = grp ? c2[4 + m][n] : c2[m][n]; hs[m][n] = grp ? c2[m][n] : c2[4 + m][n]; }
        auto dump_own = [&](const f32x4 (&v)[4][4]) {
#pragma unroll
            for (int m = 0; m < 4; ++m)
#pragma unroll
                for (int n = 0; n < 4; ++n) *(LAS f32x4*)(lds + L_OWN + ((grp * 4 + m) * 8 + 4 * wn + n) * 1024 + ln * 16) = v[m][n];
        };
        if (qs == ks) dump_own(hs);
        else {
#pragma unroll
            for (int m = 0; m < 4; ++m)
#pragma unroll
                for (int n2 = 0; n2 < 2; ++n2) {
                    const f32x4 a = hs[m][2 * n2], b = hs[m][2 * n2 + 1];
                    *(LAS u32x4*)(lds + L_SEND + (wid ^ 4) * 8192 + (m * 2 + n2) * 1024 + ln * 16) = (u32x4){pk_bf16(a[0], a[1]), pk_bf16(a[2], a[3]), pk_bf16(b[0], b[1]), pk_bf16(b[2], b[3])};
                }
        }
        if (qk == ks) dump_own(hk);
        LBAR();
        if (publisher) {
            const unsigned xbase = WS_XCH + (unsigned)((((tile * 4 + ks) * 8 + wid) * 8) * 1024 + ln * 16);
#pragma unroll
            for (int m = 0; m < 4; ++m)
#pragma unroll
                for (int n2 = 0; n2 < 2; ++n2) {
                    const u32x4 w = *(const LAS u32x4*)(lds + L_SEND + wid * 8192 + (m * 2 + n2) * 1024 + ln * 16);
                    const f32x4 a = hk[m][2 * n2] + (f32x4){lo_f(w[0]), hi_f(w[0]), lo_f(w[1]), hi_f(w[1])}, b = hk[m][2 * n2 + 1] + (f32x4){lo_f(w[2]), hi_f(w[2]), lo_f(w[3]), hi_f(w[3])};
                    ST_SC1(((u32x4){pk_bf16(a[0], a[1]), pk_bf16(a[2], a[3]), pk_bf16(b[0], b[1]), pk_bf16(b[2], b[3])}), rs, xbase + (m * 2 + n2) * 1024);
                }
        }
        f32x4 e[2][2];
#pragma unroll
        for (int a = 0; a < 2; ++a)
#pragma unroll
            for (int b = 0; b < 2; ++b)
                e[a][b] = *(const LAS f32x4*)(lds + L_OWN + ((2 * mh + a) * 8 + 2 * nq + b) * 1024 + ln * 16) + *(const LAS f32x4*)(lds + L_OWN + ((4 + 2 * mh + a) * 8 + 2 * nq + b) * 1024 + ln * 16);
        STAMP(4);
        asm volatile("s_waitcnt vmcnt(0)" : "+v"(xr0), "+v"(xr1)::"memory");
        if (publisher && ln == 0) flag_set(ws, FL_X + (tile * 4 + ks) * 8 + wid, tag);
        STAMP(5);
        const int wsrc = (ks & 1) * 4 + (ks >> 1) * 2 + (nq >> 1);
        {
            GAS u64* p = flagp(ws, FL_X + (tile * 4 + ((ks + 1 + (ln < 3 ? ln : 0)) & 3)) * 8 + wsrc);
#pragma unroll 1
            for (unsigned it = 0; it < (1u << 21); ++it) {
                const u64 v = __hip_atomic_load(p, __ATOMIC_RELAXED, __HIP_MEMORY_SCOPE_AGENT);
                if (__all(v == tag)) break;
                __builtin_amdgcn_s_sleep(1);
            }
            asm volatile("" ::: "memory");
        }
        u32x4 pw[3][2];
#pragma unroll
        for (int i = 0; i < 3; ++i) {
            const unsigned base = WS_XCH + (unsigned)((((tile * 4 + i + (i >= ks ? 1 : 0)) * 8 + wsrc) * 8 + (nq & 1) + 4 * mh) * 1024 + ln * 16);
            pw[i][0] = LD_SC1(rs, base); pw[i][1] = LD_SC1(rs, base + 2048);
        }
#pragma unroll
        for (int i = 0; i < 3; ++i)
#pragma unroll
            for (int a = 0; a < 2; ++a) {
                const u32x4 w = pw[i][a];
                e[a][0] += (f32x4){lo_f(w[0]), hi_f(w[0]), lo_f(w[1]), hi_f(w[1])};
                e[a][1] += (f32x4){lo_f(w[2]), hi_f(w[2]), lo_f(w[3]), hi_f(w[3])};
            }
        const __amdgpu_buffer_rsrc_t ors = __builtin_amdgcn_make_buffer_rsrc((void*)out, 0, S_ * D_ * 2, 0x00020000);
#pragma unroll
        for (int a = 0; a < 2; ++a) {
            float u[4], v[4];
#pragma unroll
            for (int q = 0; q < 4; ++q) {
                auto rr = __builtin_amdgcn_permlane16_swap(__float_as_uint(e[a][0][q]), __float_as_uint(e[a][1][q]), false, false);
                u[q] = __uint_as_float(rr[0]); v[q] = __uint_as_float(rr[1]);
            }
            const u32x4 xv = a ? xr1 : xr0;
            u32x4 o;
            o[0] = pk_bf16(u[0] + lo_f(xv[0]), u[1] + hi_f(xv[0])); o[1] = pk_bf16(u[2] + lo_f(xv[1]), u[3] + hi_f(xv[1]));
            o[2] = pk_bf16(v[0] + lo_f(xv[2]), v[1] + hi_f(xv[2])); o[3] = pk_bf16(v[2] + lo_f(xv[3]), v[3] + hi_f(xv[3]));
            __builtin_amdgcn_raw_buffer_store_b128(o, ors, (unsigned)(((row0 + 16 * a) * D_ + col0) * 2), 0, 16);
        }
    }
    STAMP(6);
    STAMP_OUT(1, 0, 7);
}

extern "C" void kernel_launch(void* const* d_in, const int* in_sizes, int n_in, void* d_out, int out_size, void* d_ws, size_t ws_size, hipStream_t stream) {
    const u16* x = (const u16*)d_in[0]; const u16* wqkv = (const u16*)d_in[1]; const u16* wphi = (const u16*)d_in[2]; const u16* wo = (const u16*)d_in[3];
    const u16* wg = (const u16*)d_in[5]; const u16* wu = (const u16*)d_in[6]; const u16* wd = (const u16*)d_in[7];
    (void)hipFuncSetAttribute((const void*)k_fused, hipFuncAttributeMaxDynamicSharedMemorySize, L_TOTAL);
    k_fused<<<256, 512, L_TOTAL, stream>>>(x, wqkv, wphi, wo, wg, wu, wd, (unsigned char*)d_ws, (u16*)d_out);
}
```

```cpp
#include <hip/hip_runtime.h>
#include <type_traits>

typedef unsigned short u16;
typedef unsigned long long u64;
typedef short bf16x8 __attribute__((ext_vector_type(8)));
typedef short s16x4 __attribute__((ext_vector_type(4)));
typedef float f32x4 __attribute__((ext_vector_type(4)));
typedef float f32x2 __attribute__((ext_vector_type(2)));
typedef unsigned u32x4 __attribute__((ext_vector_type(4)));
typedef unsigned u32x2 __attribute__((ext_vector_type(2)));
#define LAS __attribute__((address_space(3)))
#define GAS __attribute__((address_space(1)))
typedef LAS unsigned char* lds_t;
typedef const unsigned char* cptr;

constexpr int S_ = 2048, D_ = 1024, F_ = 3584;
constexpr int CODE_LINES = 256;
constexpr int NKC2 = 144;

constexpr unsigned WS_XN   = 0;
constexpr unsigned WS_H    = WS_XN + S_ * D_ * 2;
constexpr unsigned WS_PART = WS_H + 8 * NKC2 * 16 * 1024;
constexpr unsigned WS_PZ   = WS_PART + 8 * 32 * 4 * 1024;
constexpr unsigned WS_XCH  = WS_PZ + 8 * 32 * 64 * 4;
constexpr unsigned WS_FLG  = WS_XCH + 64 * 4 * 8 * 8 * 1024;
constexpr unsigned FL_LN = 0, FL_KV = FL_LN + 256, FL_H = FL_KV + 128, FL_O = FL_H + 256, FL_X = FL_O + 128, FL_EPOCH = FL_X + 2048, FL_N = FL_EPOCH + 1;
constexpr unsigned WS_END  = WS_FLG + FL_N * 64;

constexpr int G1_SA = 16384, G1_STAGE = 36864;
constexpr int L_MISC = 158720, L_WPHI = 159744, L_TOTAL = 163840;
constexpr int L_KVL = 147456;
constexpr int E_KP = 0, E_V = 65536;
constexpr int G2_SB = 98304;

__device__ __forceinline__ unsigned pk_bf16(float lo, float hi) {
    typedef __bf16 bf2 __attribute__((ext_vector_type(2)));
    bf2 r = {(__bf16)lo, (__bf16)hi};
    return __builtin_bit_cast(unsigned, r);
}
__device__ __forceinline__ float rbf(float x) { return __uint_as_float(pk_bf16(x, x) & 0xffff0000u); }
__device__ __forceinline__ float lo_f(unsigned w) { return __uint_as_float(w << 16); }
__device__ __forceinline__ float hi_f(unsigned w) { return __uint_as_float(w & 0xffff0000u); }

#define STAMP_DECL
#define STAMP(i)
#define STAMP_ARG
#define STAMP_PASS
#define STAMP_OUT(kern, lo, n)

#define XW_CNT "18"
#define JIT(k)
#define WAIT_V(n) asm volatile("s_waitcnt vmcnt(" #n ")" ::: "memory")
#define WAIT_L0() do { asm volatile("" ::: "memory"); __builtin_amdgcn_s_waitcnt(0xC07F); asm volatile("" ::: "memory"); } while (0)
#define BAR() __builtin_amdgcn_s_barrier()
#define LBAR() asm volatile("s_waitcnt lgkmcnt(0)\n\ts_barrier" ::: "memory")
#define SCHED() __builtin_amdgcn_sched_barrier(0)
#define MFMA(a, b, c) __builtin_amdgcn_mfma_f32_16x16x32_bf16(a, b, c, 0, 0, 0)
#define MFMA_SET(c, a, b) asm volatile("v_mfma_f32_16x16x32_bf16 %0, %1, %2, 0" : "=&v"(c) : "v"(a), "v"(b))
#define MFMA_ACC(c, a, b) asm volatile("v_mfma_f32_16x16x32_bf16 %0, %1, %2, %0" : "+v"(c) : "v"(a), "v"(b))

__device__ __forceinline__ cptr uni(cptr p) {
    const u64 v = (u64)p;
    return (cptr)(((u64)(unsigned)__builtin_amdgcn_readfirstlane((unsigned)(v >> 32)) << 32) | (unsigned)__builtin_amdgcn_readfirstlane((unsigned)v));
}
__device__ __forceinline__ void glds_v(cptr g, unsigned l) {
    asm volatile("s_mov_b32 m0, %1\n\ts_nop 0\n\tglobal_load_lds_dwordx4 %0, off" ::"v"(g), "s"(l) : "memory");
}
__device__ __forceinline__ void glds_s(unsigned voff, cptr sb, unsigned l) {
    asm volatile("s_nop 2\n\ts_mov_b32 m0, %2\n\ts_nop 0\n\tglobal_load_lds_dwordx4 %0, %1" ::"v"(voff), "s"(sb), "s"(l) : "memory");
}
__device__ __forceinline__ void glds_s_o128(unsigned voff, cptr sb, unsigned l) {
    asm volatile("s_mov_b32 m0, %2\n\ts_nop 0\n\tglobal_load_lds_dwordx4 %0, %1 offset:128" ::"v"(voff), "s"(sb), "s"(l - 128u) : "memory");
}
template <int OFF> __device__ __forceinline__ void glds_s_sc1_first(unsigned voff, cptr sb, unsigned l) {
    asm volatile("s_nop 2\n\ts_mov_b32 m0, %2\n\ts_nop 0\n\tglobal_load_lds_dwordx4 %0, %1 offset:%3 sc1" ::"v"(voff), "s"(sb), "s"(l), "i"(OFF) : "memory");
}
template <int OFF> __device__ __forceinline__ void glds_s_sc1_next(unsigned voff, cptr sb) {
    asm volatile("global_load_lds_dwordx4 %0, %1 offset:%2 sc1" ::"v"(voff), "s"(sb), "i"(OFF) : "memory");
}
template <int OFF> __device__ __forceinline__ void gld_sc1(u32x4& d, unsigned voff, cptr sb) {
    asm volatile("global_load_dwordx4 %0, %1, %2 offset:%3 sc1" : "+v"(d) : "v"(voff), "s"(sb), "i"(OFF) : "memory");
}
__device__ __forceinline__ bf16x8 lds_b128(lds_t p) { return *(const LAS bf16x8*)p; }
__device__ __forceinline__ bf16x8 lds_tr2(lds_t p0, lds_t p1) {
    s16x4 a = __builtin_amdgcn_ds_read_tr16_b64_v4i16((LAS s16x4*)p0);
    s16x4 b = __builtin_amdgcn_ds_read_tr16_b64_v4i16((LAS s16x4*)p1);
    return __builtin_shufflevector(a, b, 0, 1, 2, 3, 4, 5, 6, 7);
}
template <int OFF> __device__ __forceinline__ bf16x8 lds_tr2_asm(unsigned addr) {
    s16x4 a, b;
    asm volatile("ds_read_b64_tr_b16 %0, %2 offset:%3\n\tds_read_b64_tr_b16 %1, %2 offset:%4" : "=&v"(a), "=&v"(b) : "v"(addr), "i"(OFF), "i"(OFF + 512) : "memory");
    return __builtin_shufflevector(a, b, 0, 1, 2, 3, 4, 5, 6, 7);
}

__device__ __forceinline__ float x16(float v, float& o) { auto r = __builtin_amdgcn_permlane16_swap(__float_as_uint(v), __float_as_uint(v), false, false); o = __uint_as_float(r[1]); return __uint_as_float(r[0]); }
__device__ __forceinline__ float x32(float v, float& o) { auto r = __builtin_amdgcn_permlane32_swap(__float_as_uint(v), __float_as_uint(v), false, false); o = __uint_as_float(r[1]); return __uint_as_float(r[0]); }
__device__ __forceinline__ float sum_g(float v) { float o, a = x16(v, o); v = a + o; a = x32(v, o); return a + o; }
__device__ __forceinline__ float max_g(float v) { float o, a = x16(v, o); v = fmaxf(a, o); a = x32(v, o); return fmaxf(a, o); }
__device__ __forceinline__ float min_g(float v) { float o, a = x16(v, o); v = fminf(a, o); a = x32(v, o); return fminf(a, o); }
__device__ __forceinline__ float ror8(float v) { return __int_as_float(__builtin_amdgcn_update_dpp(0, __float_as_int(v), 0x128, 0xf, 0xf, false)); }

__device__ __forceinline__ int swzB(int k) { return (((k >> 1) & 1) << 1) | (((k >> 3) & 1) << 2); }
__device__ __forceinline__ int swzP(int k) { return ((k >> 2) & 1) | (((k >> 1) & 1) << 1) | (((k >> 3) & 1) << 2); }
__device__ __forceinline__ int swzF(int k) { return ((k >> 2) & 1) | ((k & 3) << 1) | (((k >> 3) & 1) << 3); }

__device__ __forceinline__ f32x2 gelu_mul2(f32x2 g, f32x2 u) {
    constexpr float C0 = -2.0f * 0.7978845608028654f * 1.4426950408889634f, C1 = C0 * 0.044715f;
    const f32x2 a = g * (g * g * C1 + C0);
    const f32x2 d = (f32x2){__builtin_amdgcn_exp2f(a[0]), __builtin_amdgcn_exp2f(a[1])} + 1.0f;
    return (g * u) * (f32x2){__builtin_amdgcn_rcpf(d[0]), __builtin_amdgcn_rcpf(d[1])};
}
__device__ __forceinline__ u32x4 pack_frag(const f32x2 a0, const f32x2 a1, const f32x2 b0, const f32x2 b1) {
    unsigned x0 = pk_bf16(a0[0], a0[1]), x1 = pk_bf16(a1[0], a1[1]), y0 = pk_bf16(b0[0], b0[1]), y1 = pk_bf16(b1[0], b1[1]);
    auto r0 = __builtin_amdgcn_permlane16_swap(x0, y0, false, false);
    auto r1 = __builtin_amdgcn_permlane16_swap(x1, y1, false, false);
    u32x4 o; o[0] = r0[0]; o[1] = r1[0]; o[2] = r0[1]; o[3] = r1[1];
    return o;
}
__device__ __forceinline__ int frag_kq(int lane) { const int g = lane >> 4; return ((g & 1) << 1) | (g >> 1); }
__device__ __forceinline__ int frag_off(int lane) { return (frag_kq(lane) * 16 + (lane & 15)) * 16; }

__device__ __forceinline__ float max3(float a, float b, float c) { float r; asm("v_max3_f32 %0, %1, %2, %3" : "=v"(r) : "v"(a), "v"(b), "v"(c)); return r; }
__device__ __forceinline__ float min3(float a, float b, float c) { float r; asm("v_min3_f32 %0, %1, %2, %3" : "=v"(r) : "v"(a), "v"(b), "v"(c)); return r; }
__device__ __forceinline__ float max2(float a, float b) { float r; asm("v_max_f32 %0, %1, %2" : "=v"(r) : "v"(a), "v"(b)); return r; }
template <int NU, class Op> __device__ __forceinline__ void red2_g(float (&a)[NU], float (&b)[NU], Op op) {
    float z[NU], y[NU];
#pragma unroll
    for (int u = 0; u < NU; ++u) {
        auto r = __builtin_amdgcn_permlane16_swap(__float_as_uint(a[u]), __float_as_uint(b[u]), false, false);
        z[u] = op(__uint_as_float(r[0]), __uint_as_float(r[1]));
    }
#pragma unroll
    for (int u = 0; u < NU; ++u) {
        auto q = __builtin_amdgcn_permlane32_swap(__float_as_uint(z[u]), __float_as_uint(z[u]), false, false);
        y[u] = op(__uint_as_float(q[0]), __uint_as_float(q[1]));
    }
#pragma unroll
    for (int u = 0; u < NU; ++u) {
        auto w = __builtin_amdgcn_permlane16_swap(__float_as_uint(y[u]), __float_as_uint(y[u]), false, false);
        a[u] = __uint_as_float(w[0]); b[u] = __uint_as_float(w[1]);
    }
}
template <int NU> __device__ __forceinline__ void soft_pm(const u32x4 (&w)[NU], u32x4 (&P)[NU], u32x4 (&M)[NU]) {
    constexpr float L2E = 1.4426950408889634f;
    f32x2 pv[NU][4]; float mx[NU], nm[NU];
#pragma unroll
    for (int u = 0; u < NU; ++u) {
#pragma unroll
        for (int i = 0; i < 4; ++i) pv[u][i] = (f32x2){lo_f(w[u][i]), hi_f(w[u][i])};
        mx[u] = max3(max3(max3(pv[u][0][0], pv[u][0][1], pv[u][1][0]), pv[u][1][1], pv[u][2][0]), max2(pv[u][2][1], pv[u][3][0]), pv[u][3][1]);
        nm[u] = -min3(min3(min3(pv[u][0][0], pv[u][0][1], pv[u][1][0]), pv[u][1][1], pv[u][2][0]), min3(pv[u][2][1], pv[u][3][0], pv[u][3][1]), pv[u][3][1]);
    }
    red2_g<NU>(mx, nm, [](float a, float b) { return max2(a, b); });
    f32x2 pp[NU][4], pm[NU][4]; float sp[NU], sm[NU];
#pragma unroll
    for (int u = 0; u < NU; ++u) {
        const float cp = -mx[u] * L2E, cm = -nm[u] * L2E;
#pragma unroll
        for (int i = 0; i < 4; ++i) {
            const f32x2 a = pv[u][i] * L2E + cp, b = pv[u][i] * (-L2E) + cm;
            pp[u][i] = (f32x2){__builtin_amdgcn_exp2f(a[0]), __builtin_amdgcn_exp2f(a[1])};
            pm[u][i] = (f32x2){__builtin_amdgcn_exp2f(b[0]), __builtin_amdgcn_exp2f(b[1])};
        }
        const f32x2 s2 = (pp[u][0] + pp[u][1]) + (pp[u][2] + pp[u][3]), m2 = (pm[u][0] + pm[u][1]) + (pm[u][2] + pm[u][3]);
        sp[u] = s2[0] + s2[1]; sm[u] = m2[0] + m2[1];
    }
    red2_g<NU>(sp, sm, [](float a, float b) { return a + b; });
#pragma unroll
    for (int u = 0; u < NU; ++u) {
        const float rp = __builtin_amdgcn_rcpf(sp[u]), rm = __builtin_amdgcn_rcpf(sm[u]);
#pragma unroll
        for (int i = 0; i < 4; ++i) { const f32x2 a = pp[u][i] * rp, b = pm[u][i] * rm; P[u][i] = pk_bf16(a[0], a[1]); M[u][i] = pk_bf16(b[0], b[1]); }
    }
}

__device__ __forceinline__ GAS u64* flagp(unsigned char* ws, unsigned i) { return (GAS u64*)(ws + WS_FLG + (size_t)i * 64); }
__device__ __forceinline__ void flag_set(unsigned char* ws, unsigned i, u64 tag) { __hip_atomic_store(flagp(ws, i), tag, __ATOMIC_RELAXED, __HIP_MEMORY_SCOPE_AGENT); }
__device__ __forceinline__ void flag_wait(unsigned char* ws, unsigned i0, unsigned st, int n, u64 tag, int lane) {
    GAS u64* p = flagp(ws, i0 + st * (unsigned)(lane < n ? lane : 0));
#pragma unroll 1
    for (unsigned it = 0; it < (1u << 21); ++it) {
        const u64 v = __hip_atomic_load(p, __ATOMIC_RELAXED, __HIP_MEMORY_SCOPE_AGENT);
        if (__all(v == tag)) break;
        __builtin_amdgcn_s_sleep(1);
    }
    asm volatile("" ::: "memory");
}
__device__ __forceinline__ __amdgpu_buffer_rsrc_t ws_rsrc(unsigned char* ws) { return __builtin_amdgcn_make_buffer_rsrc((void*)ws, 0, WS_END, 0x00020000); }
#define ST_SC1(v, rs, off) __builtin_amdgcn_raw_buffer_store_b128(v, rs, off, 0, 16)
#define LD_SC1(rs, off) __builtin_amdgcn_raw_buffer_load_b128(rs, off, 0, 16)

template <int GRP>
__device__ __forceinline__ void g1_mainloop(const unsigned ldsb, lds_t lds, f32x4 (&acc)[8][5], cptr (&bp)[5], const size_t ld4, cptr xn, const int wl, const int lane, const int (&ba)[5],
                                            cptr wd, cptr wo) {
    const size_t ldq = (size_t)32 * 3072 * 2, ldg = (size_t)32 * F_ * 2;
    const int aoff = GRP * 8192 + lane * 16;
    const int p0 = wl == 0 ? 0 : 5 * wl + 1;
    const unsigned av0 = p0 * 1024 + lane * 16, av1 = av0 + 4096;
    int so = 0, sl = 3 * G1_STAGE;
    cptr an = xn + 3 * 16384;
    unsigned touch = 0;
    cptr tp = wd + (size_t)(blockIdx.x * 224 + (lane & 15)) * 128;
    bf16x8 A[8], B[5];
#pragma unroll 1
    for (int t = 0; t < 32; ++t) {
        const bool more = t < 29;
        const unsigned lb = ldsb + sl + G1_SA + wl * 1024, la = ldsb + sl + p0 * 1024, rb = ldsb + so;
        if (more) {
            if (GRP == 0) {
                B[0] = lds_tr2_asm<0>(rb + ba[0]); B[1] = lds_tr2_asm<0>(rb + ba[1]);
                glds_v(bp[0], lb);
                B[2] = lds_tr2_asm<0>(rb + ba[2]); B[3] = lds_tr2_asm<0>(rb + ba[3]);
                glds_v(bp[1], lb + 4096);
                B[4] = lds_tr2_asm<0>(rb + ba[4]);
                A[0] = lds_b128(lds + so + aoff); A[1] = lds_b128(lds + so + aoff + 1024);
                glds_v(bp[2], lb + 8192);
                A[2] = lds_b128(lds + so + aoff + 2048); A[3] = lds_b128(lds + so + aoff + 3072); A[4] = lds_b128(lds + so + aoff + 4096);
                glds_v(bp[3], lb + 12288);
                A[5] = lds_b128(lds + so + aoff + 5120); A[6] = lds_b128(lds + so + aoff + 6144); A[7] = lds_b128(lds + so + aoff + 7168);
                glds_v(bp[4], lb + 16384);
                bp[0] += ldq; bp[1] += ldg; bp[2] += ldg; bp[3] += ldg; bp[4] += ld4;
                WAIT_L0(); WAIT_V(10);
            } else if (wl < 3) {
                B[0] = lds_tr2_asm<0>(rb + ba[0]); B[1] = lds_tr2_asm<0>(rb + ba[1]);
                glds_s_sc1_first<0>(av0, an, la);
                B[2] = lds_tr2_asm<0>(rb + ba[2]); B[3] = lds_tr2_asm<0>(rb + ba[3]);
                glds_s_sc1_next<1024>(av0, an);
                B[4] = lds_tr2_asm<0>(rb + ba[4]);
                A[0] = lds_b128(lds + so + aoff); A[1] = lds_b128(lds + so + aoff + 1024);
                glds_s_sc1_next<2048>(av0, an);
                A[2] = lds_b128(lds + so + aoff + 2048); A[3] = lds_b128(lds + so + aoff + 3072);
                glds_s_sc1_next<3072>(av0, an);
                A[4] = lds_b128(lds + so + aoff + 4096); A[5] = lds_b128(lds + so + aoff + 5120);
                glds_s_sc1_first<0>(av1, an, la + 4096);
                A[6] = lds_b128(lds + so + aoff + 6144); A[7] = lds_b128(lds + so + aoff + 7168);
                if (wl == 0) glds_s_sc1_next<1024>(av1, an);
                an += 16384;
                WAIT_L0();
                if (wl == 0) WAIT_V(12); else WAIT_V(10);
            } else {
                if (t < 18) {
                    asm volatile("global_load_dword %0, %1, off" : "+v"(touch) : "v"(tp) : "memory");
                    if (t == 13) tp = wo + (size_t)(blockIdx.x * 64 + (lane & 15)) * 128; else tp += 2048;
                }
#pragma unroll
                for (int n = 0; n < 5; ++n) B[n] = lds_tr2_asm<0>(rb + ba[n]);
#pragma unroll
                for (int m = 0; m < 8; ++m) A[m] = lds_b128(lds + so + aoff + m * 1024);
                WAIT_L0();
            }
        } else {
#pragma unroll
            for (int n = 0; n < 5; ++n) B[n] = lds_tr2_asm<0>(rb + ba[n]);
#pragma unroll
            for (int m = 0; m < 8; ++m) A[m] = lds_b128(lds + so + aoff + m * 1024);
            WAIT_L0(); WAIT_V(0);
        }
        so = (so == 3 * G1_STAGE) ? 0 : so + G1_STAGE;
        sl = (sl == 3 * G1_STAGE) ? 0 : sl + G1_STAGE;
        asm volatile("" : "+s"(so), "+s"(sl));
        SCHED(); BAR(); SCHED();
        __builtin_amdgcn_s_setprio(1);
#pragma unroll
        for (int m = 0; m < 8; ++m)
#pragma unroll
            for (int n = 0; n < 5; ++n) { const int nn = (m & 1) ? 4 - n : n; MFMA_ACC(acc[m][nn], B[nn], A[m]); }
        __builtin_amdgcn_s_setprio(0);
        SCHED(); BAR(); SCHED();
    }
    asm volatile("" ::"v"(touch));
}

__device__ __forceinline__ void g2_issueB(unsigned bvoff, cptr sb, unsigned l) {
    glds_s(bvoff, sb, l);
    glds_s_o128(bvoff, sb, l + 4096);
}

__global__ __launch_bounds__(512, 2) void k_fused(const u16* __restrict__ x, const u16* __restrict__ wqkv, const u16* __restrict__ wphi, const u16* __restrict__ wo,
                                                  const u16* __restrict__ wg, const u16* __restrict__ wu, const u16* __restrict__ wd,
                                                  unsigned char* __restrict__ ws, u16* __restrict__ out) {
    extern __shared__ __attribute__((aligned(16))) unsigned char shm[];
    lds_t lds = (lds_t)shm;
    const unsigned ldsb = (unsigned)(size_t)lds;
    const int wid = __builtin_amdgcn_readfirstlane(threadIdx.x >> 6);
    const int grp = wid >> 2, wc = wid & 3;
    const int x8 = blockIdx.x & 7, r = blockIdx.x >> 3, ks = x8 >> 1, panel = 4 * (x8 & 1) + (r >> 3), c8 = r & 7, j = 4 * ks + (c8 >> 1), kvt = c8 & 1;
    const __amdgpu_buffer_rsrc_t rs = ws_rsrc(ws);
    STAMP_DECL; STAMP(0);
    u64 tag;
    f32x4 acc[8][5];

    {
        const int lane = threadIdx.x & 63, g = lane >> 4;
        const int row = panel * 256 + (ks * 8 + c8) * 8 + (lane & 7);
        u32x4 xv[4]; u32x2 ev = {0, 0};
#pragma unroll
        for (int i = 0; i < 4; ++i) xv[i] = (u32x4){0, 0, 0, 0};
        {
            const unsigned zero = 0;
            asm volatile("global_load_dwordx2 %0, %1, %2 sc1" : "+v"(ev) : "v"(zero), "s"(ws + WS_FLG + FL_EPOCH * 64) : "memory");
        }
        unsigned tlbt = 0, codet = 0;
        cptr bp[5];
        const size_t ldq = (size_t)32 * 3072 * 2, ldg = (size_t)32 * F_ * 2, ld4 = kvt ? ldq : ldg;
        if (grp == 0) {
            const int gq = (j & 1) ? 7 * j + 3 : 7 * j, gp = (j & 1) ? 7 * j + 1 : 7 * j + 4, gh = (j & 1) ? 7 * j : 7 * j + 6;
            const int krow = 8 * wc + (lane >> 3), chunk = (lane & 7) ^ swzB(krow);
            const size_t rq = (size_t)krow * 3072 * 2 + chunk * 16, rg = (size_t)krow * F_ * 2 + chunk * 16;
            if (!kvt) {
                bp[0] = (cptr)(wqkv + 64 * j) + rq;
                bp[1] = (cptr)(wg + 32 * gq) + rg;      bp[2] = (cptr)(wg + 32 * gq + 64) + rg;
                bp[3] = (cptr)(wu + 32 * gq) + rg;      bp[4] = (cptr)(wu + 32 * gq + 64) + rg;
            } else {
                bp[0] = (cptr)(wqkv + 1024 + 64 * j) + rq;
                bp[1] = (cptr)(wg + 32 * gp) + rg;
                bp[2] = (chunk < 4 ? (cptr)(wg + 32 * gh) : (cptr)(wu + 32 * gh) - 64) + rg;
                bp[3] = (cptr)(wu + 32 * gp) + rg;
                bp[4] = (cptr)(wqkv + 2048 + 64 * j) + rq;
            }
#pragma unroll
            for (int s = 0; s < 3; ++s) {
#pragma unroll
                for (int c = 0; c < 5; ++c) glds_v(bp[c], ldsb + s * G1_STAGE + G1_SA + c * 4096 + wc * 1024);
                bp[0] += ldq; bp[1] += ldg; bp[2] += ldg; bp[3] += ldg; bp[4] += ld4;
            }
            glds_v((cptr)(wphi + (size_t)(2 * j) * 1024 + wc * 512 + lane * 8), ldsb + L_WPHI + wc * 1024);
            {
                const int e = r * 40 + wc * 10 + (lane < 10 ? lane : 9);
                cptr p;
                if (e < 96) p = (cptr)wqkv + (size_t)e * 65536;
                else if (e < 208) p = (cptr)wg + (size_t)(e - 96) * 65536;
                else if (e < 320) p = (cptr)wu + (size_t)(e - 208) * 65536;
                else if (e < 432) p = (cptr)wd + (size_t)(e - 320) * 65536;
                else if (e < 464) p = (cptr)wo + (size_t)(e - 432) * 65536;
                else if (e < 528) p = (cptr)out + (size_t)(e - 464) * 65536;
                else if (e < 592) p = (cptr)x + (size_t)(e - 528) * 65536;
                else p = (cptr)ws + (size_t)min(e - 592, (int)(WS_END / 65536)) * 65536;
                asm volatile("global_load_dword %0, %1, off sc1" : "+v"(tlbt) : "v"(p) : "memory");
            }
            {
                u64 pc; asm volatile("s_getpc_b64 %0" : "=s"(pc));
                const int ln_ = wc * 64 + lane;
                cptr p = (cptr)(pc & ~(u64)0x7f) + (ln_ < CODE_LINES ? ln_ : CODE_LINES - 1) * 128;
                asm volatile("global_load_dword %0, %1, off" : "+v"(codet) : "v"(p) : "memory");
            }
        } else {
#pragma unroll
            for (int c = 0; c < 5; ++c) bp[c] = nullptr;
            const unsigned xo = (unsigned)(row * D_ + (wc * 8 + (lane >> 3)) * 8) * 2u;
            asm volatile("global_load_dwordx4 %0, %4, %5\n\tglobal_load_dwordx4 %1, %4, %5 offset:512\n\tglobal_load_dwordx4 %2, %4, %5 offset:1024\n\tglobal_load_dwordx4 %3, %4, %5 offset:1536\n\t"
                         "s_waitcnt vmcnt(0)" : "+v"(xv[0]), "+v"(xv[1]), "+v"(xv[2]), "+v"(xv[3]) : "v"(xo), "s"(x) : "memory");
        }
        asm volatile("s_waitcnt vmcnt(" XW_CNT ")" : "+v"(ev)::"memory");
        {
            u64 z = (((u64)(unsigned)__builtin_amdgcn_readfirstlane(ev[1]) << 32) | (unsigned)__builtin_amdgcn_readfirstlane(ev[0])) + 0x9E3779B97F4A7C15ull;
            z = (z ^ (z >> 30)) * 0xBF58476D1CE4E5B9ull; z = (z ^ (z >> 27)) * 0x94D049BB133111EBull; tag = z ^ (z >> 31);
        }
        STAMP(1);
        JIT(1);
        LAS f32x2* red = (LAS f32x2*)(lds + L_MISC);
        float f[32];
        if (grp == 1) {
            float s = 0.f;
#pragma unroll
            for (int i = 0; i < 4; ++i)
#pragma unroll
                for (int k = 0; k < 4; ++k) { f[8 * i + 2 * k] = lo_f(xv[i][k]); f[8 * i + 2 * k + 1] = hi_f(xv[i][k]); }
            { f32x2 s4[4];
#pragma unroll
              for (int i = 0; i < 4; ++i) s4[i] = (f32x2){f[2 * i], f[2 * i + 1]};
#pragma unroll
              for (int i = 4; i < 16; ++i) s4[i & 3] += (f32x2){f[2 * i], f[2 * i + 1]};
              const f32x2 s2 = (s4[0] + s4[1]) + (s4[2] + s4[3]);
              s = s2[0] + s2[1]; }
            s += ror8(s); s = sum_g(s);
            const float mw = s * (1.0f / 256.0f);
            f32x2 dv[16], q4[4];
#pragma unroll
            for (int i = 0; i < 16; ++i) dv[i] = (f32x2){f[2 * i], f[2 * i + 1]} - mw;
            SCHED();
#pragma unroll
            for (int i = 0; i < 4; ++i) q4[i] = dv[i] * dv[i];
#pragma unroll
            for (int i = 4; i < 16; ++i) q4[i & 3] = __builtin_elementwise_fma(dv[i], dv[i], q4[i & 3]);
            const f32x2 q2 = (q4[0] + q4[1]) + (q4[2] + q4[3]);
            float q = q2[0] + q2[1];
            q += ror8(q); q = sum_g(q);
            if (lane < 8) red[wc * 8 + lane] = (f32x2){s, q};
        }
        LBAR();
        if (grp == 1) {
            f32x2 p[4];
#pragma unroll
            for (int w = 0; w < 4; ++w) p[w] = red[w * 8 + (lane & 7)];
            const float mean = ((p[0][0] + p[1][0]) + (p[2][0] + p[3][0])) * (1.0f / D_);
            float m2 = (p[0][1] + p[1][1]) + (p[2][1] + p[3][1]);
#pragma unroll
            for (int w = 0; w < 4; ++w) { const float dm = p[w][0] * (1.0f / 256.0f) - mean; m2 += 256.0f * dm * dm; }
            const float var = m2 * (1.0f / D_);
            const float m = rbf(mean), sd = rbf(sqrtf(rbf(rbf(var) + 1e-5f))), rsd = 1.0f / sd;
            unsigned w[16];
#pragma unroll
            for (int i = 0; i < 16; ++i) { const f32x2 d = (f32x2){f[2 * i], f[2 * i + 1]} - m; w[i] = pk_bf16(d[0], d[1]); }
            asm volatile("" : "+v"(w[0]), "+v"(w[1]), "+v"(w[2]), "+v"(w[3]), "+v"(w[4]), "+v"(w[5]), "+v"(w[6]), "+v"(w[7]));
            asm volatile("" : "+v"(w[8]), "+v"(w[9]), "+v"(w[10]), "+v"(w[11]), "+v"(w[12]), "+v"(w[13]), "+v"(w[14]), "+v"(w[15]));
            const unsigned xo = WS_XN + (unsigned)((panel * 32 * 16 + ((row >> 4) & 15)) * 1024 + (row & 15) * 16);
#pragma unroll
            for (int it = 0; it < 4; ++it) {
                const int pc = it * 32 + wc * 8 + (lane >> 3), kc = pc >> 2, kq = pc & 3;
                f32x2 a[4], q0[4];
#pragma unroll
                for (int i = 0; i < 4; ++i) { a[i] = (f32x2){lo_f(w[4 * it + i]), hi_f(w[4 * it + i])}; q0[i] = a[i] * rsd; }
#pragma unroll
                for (int i = 0; i < 4; ++i) a[i] = __builtin_elementwise_fma(-q0[i], (f32x2){sd, sd}, a[i]);
#pragma unroll
                for (int i = 0; i < 4; ++i) q0[i] = __builtin_elementwise_fma(a[i], (f32x2){rsd, rsd}, q0[i]);
                const u32x4 o = {pk_bf16(q0[0][0], q0[0][1]), pk_bf16(q0[1][0], q0[1][1]), pk_bf16(q0[2][0], q0[2][1]), pk_bf16(q0[3][0], q0[3][1])};
                ST_SC1(o, rs, xo + (unsigned)(kc * 16384 + kq * 256));
                SCHED();
            }
            WAIT_V(0);
        }
        LBAR();
        STAMP(2);
        if (threadIdx.x == 256) flag_set(ws, FL_LN + panel * 32 + ks * 8 + c8, tag);
        if (wid == 0) flag_wait(ws, FL_LN + panel * 32, 1, 32, tag, lane);
        LBAR();
        STAMP(3);
        JIT(3);

        cptr xn = uni((cptr)ws + WS_XN + (size_t)panel * 32 * 16384);
        if (grp == 1 && wc < 3) {
            const int p0 = wc == 0 ? 0 : 5 * wc + 1;
            const unsigned av0 = p0 * 1024 + lane * 16, av1 = av0 + 4096;
#pragma unroll
            for (int s3 = 0; s3 < 3; ++s3) {
                const unsigned la = ldsb + s3 * G1_STAGE + p0 * 1024;
                glds_s_sc1_first<0>(av0, xn + s3 * 16384, la); glds_s_sc1_next<1024>(av0, xn + s3 * 16384);
                glds_s_sc1_next<2048>(av0, xn + s3 * 16384);   glds_s_sc1_next<3072>(av0, xn + s3 * 16384);
                glds_s_sc1_first<0>(av1, xn + s3 * 16384, la + 4096);
                if (wc == 0) glds_s_sc1_next<1024>(av1, xn + s3 * 16384);
            }
            if (wc == 0) WAIT_V(12); else WAIT_V(10);
        }
        int ba[5];
        {
            int cgA, tlA, cgB, tlB;
            if (!kvt) { cgA = 1 + (wc >> 1); tlA = 2 * (wc & 1); cgB = 3 + (wc >> 1); tlB = tlA; }
            else if (wc < 2) { cgA = 1; tlA = 2 * wc; cgB = 3; tlB = 2 * wc; }
            else if (wc == 2) { cgA = 2; tlA = 0; cgB = 2; tlB = 2; }
            else { cgA = 4; tlA = 0; cgB = 4; tlB = 2; }
            const int q4 = (lane >> 2) & 3, p = lane & 3, krow = 8 * g + q4, sw = swzB(krow);
            auto adr = [&](int cg, int tl) { return G1_SA + cg * 4096 + krow * 128 + (((2 * tl + (p >> 1)) ^ sw) << 4) + (p & 1) * 8; };
            ba[0] = adr(cgA, tlA); ba[1] = adr(cgA, tlA + 1); ba[2] = adr(cgB, tlB); ba[3] = adr(cgB, tlB + 1); ba[4] = adr(0, wc);
        }
#pragma unroll
        for (int m = 0; m < 8; ++m)
#pragma unroll
            for (int n = 0; n < 5; ++n) acc[m][n] = (f32x4){0.f, 0.f, 0.f, 0.f};
        if (grp == 0) asm volatile("s_waitcnt vmcnt(0)" : "+v"(tlbt), "+v"(codet)::"memory");
        BAR();
        STAMP(4);
        if (grp == 0) { g1_mainloop<0>(ldsb, lds, acc, bp, ld4, xn, wc, lane, ba, (cptr)wd, (cptr)wo); BAR(); }
        else          { BAR(); g1_mainloop<1>(ldsb, lds, acc, bp, ld4, xn, wc, lane, ba, (cptr)wd, (cptr)wo); }
        STAMP(5);
    }

    JIT(5);
    int tz = threadIdx.x; asm volatile("" : "+v"(tz));
    const int lane = tz & 63, g = lane >> 4, l15 = lane & 15;
    const unsigned bvoff = (unsigned)((8 * wc + (lane >> 3)) * 2048 + c8 * 256 + (((lane & 7) ^ swzB(8 * wc + (lane >> 3))) << 4));
    const unsigned bl = ldsb + G2_SB + grp * 24576 + wc * 1024;
    cptr sbp = uni((cptr)wd + ((size_t)(28 * ks + grp) << 16));
    cptr sa = uni((cptr)ws + WS_H + ((size_t)(panel * NKC2 + 28 * ks + grp) << 14));
    const unsigned al = ldsb + grp * 49152 + wc * 4096;
    auto startA = [&](int s2) {
        int tz4 = threadIdx.x; asm volatile("" : "+v"(tz4));
        const unsigned avoff = (unsigned)(wc * 4096 + (tz4 & 63) * 16);
        glds_s_sc1_first<0>(avoff, sa, al + s2 * 16384); glds_s_sc1_next<1024>(avoff, sa); glds_s_sc1_next<2048>(avoff, sa); glds_s_sc1_next<3072>(avoff, sa);
        sa += 32768;
    };
    auto prefetchB = [&]() { g2_issueB(bvoff, sbp, bl); g2_issueB(bvoff, sbp + 131072, bl + 8192); sbp += 262144; };
    {
        const int gq = (j & 1) ? 7 * j + 3 : 7 * j, gp = (j & 1) ? 7 * j + 1 : 7 * j + 4, gh = (j & 1) ? 7 * j : 7 * j + 6;
        {
            lds_t kp = lds + E_KP + (128 * grp + l15) * 256 + ((((wc >> 1) * 4 + (wc & 1) * 2 + (g >> 1)) ^ l15) << 4) + (g & 1) * 8;
#pragma unroll
            for (int m = 0; m < 8; ++m) *(LAS u32x2*)(kp + m * 4096) = (u32x2){pk_bf16(acc[m][4][0], acc[m][4][1]), pk_bf16(acc[m][4][2], acc[m][4][3])};
        }
        const bool has_h = !(kvt && wc == 3);
        const int hkc = !kvt ? gq + wc : (wc < 2 ? gp + wc : gh);
        const unsigned hoff = WS_H + (unsigned)(((panel * NKC2 + hkc) * 16 + 8 * grp) * 1024 + frag_off(lane));
        auto geglu_c = [&](int m) {
            const f32x2 a0 = gelu_mul2(acc[m][0].lo, acc[m][2].lo), a1 = gelu_mul2(acc[m][0].hi, acc[m][2].hi);
            const f32x2 b0 = gelu_mul2(acc[m][1].lo, acc[m][3].lo), b1 = gelu_mul2(acc[m][1].hi, acc[m][3].hi);
            return pack_frag(a0, a1, b0, b1);
        };
        auto geglu = [&](int m) { ST_SC1(geglu_c(m), rs, hoff + m * 1024); SCHED(); };
        const int q4 = (lane >> 2) & 3, p4 = lane & 3;
        auto logits = [&](u32x4 (&lg)[4]) {
            lds_t wp = lds + L_WPHI + (8 * g + q4) * 64 + p4 * 8;
            lds_t qp = lds + E_KP + (32 * wid + l15) * 256;
            bf16x8 wf[2][2], qf[4];
#pragma unroll
            for (int hh = 0; hh < 2; ++hh) {
                wf[hh][0] = lds_tr2(wp + hh * 2048, wp + hh * 2048 + 256); wf[hh][1] = lds_tr2(wp + hh * 2048 + 32, wp + hh * 2048 + 288);
                qf[hh * 2] = lds_b128(qp + (((hh * 4 + g) ^ l15) << 4)); qf[hh * 2 + 1] = lds_b128(qp + 4096 + (((hh * 4 + g) ^ l15) << 4));
            }
            SCHED();
            const f32x4 z4 = {0.f, 0.f, 0.f, 0.f};
            f32x4 p0[4], p1[4];
#pragma unroll
            for (int u = 0; u < 4; ++u) { p0[u] = MFMA(wf[u >> 1][0], qf[u], z4); p1[u] = MFMA(wf[u >> 1][1], qf[u], z4); }
            SCHED();
#pragma unroll
            for (int u = 0; u < 4; ++u) lg[u] = (u32x4){pk_bf16(p0[u][0], p0[u][1]), pk_bf16(p0[u][2], p0[u][3]), pk_bf16(p1[u][0], p1[u][1]), pk_bf16(p1[u][2], p1[u][3])};
            asm volatile("" : "+v"(lg[0]), "+v"(lg[1]), "+v"(lg[2]), "+v"(lg[3]));
        };
        if (kvt) {
            LBAR();
            if (wc == 3) {
                const int sw = swzP(l15);
                lds_t vp = lds + E_V + (128 * grp + l15) * 128 + (g & 1) * 8;
#pragma unroll
                for (int n = 0; n < 4; ++n) {
                    lds_t vn = vp + (((2 * n + (g >> 1)) ^ sw) << 4);
#pragma unroll
                    for (int m = 0; m < 8; ++m) *(LAS u32x2*)(vn + m * 2048) = (u32x2){pk_bf16(acc[m][n][0], acc[m][n][1]), pk_bf16(acc[m][n][2], acc[m][n][3])};
                }
            }
            STAMP(6);
            u32x4 lg[4], P[4], M[4];
            logits(lg);
            WAIT_L0();
            soft_pm<4>(lg, P, M);
            {
                const int sw = swzF(l15);
                lds_t pb = lds + E_KP + (32 * wid + l15) * 256 + (g & 1) * 8;
#pragma unroll
                for (int u = 0; u < 4; ++u) {
                    const int c0 = (u >> 1) * 8 + (g >> 1), tb = u & 1;
                    *(LAS u32x2*)(pb + tb * 4096 + (((c0 + 0) ^ sw) << 4)) = (u32x2){P[u][0], P[u][1]};
                    *(LAS u32x2*)(pb + tb * 4096 + (((c0 + 2) ^ sw) << 4)) = (u32x2){P[u][2], P[u][3]};
                    *(LAS u32x2*)(pb + tb * 4096 + (((c0 + 4) ^ sw) << 4)) = (u32x2){M[u][0], M[u][1]};
                    *(LAS u32x2*)(pb + tb * 4096 + (((c0 + 6) ^ sw) << 4)) = (u32x2){M[u][2], M[u][3]};
                }
            }
            LBAR();
            STAMP(7);
            JIT(7);
            {
                const int hh = wid >> 2, ft = wid & 3;
                f32x4 kv0 = {0.f, 0.f, 0.f, 0.f}, kv1 = kv0, zz = kv0;
                const bf16x8 ones = {0x3F80, 0x3F80, 0x3F80, 0x3F80, 0x3F80, 0x3F80, 0x3F80, 0x3F80};
                const int t0 = 8 * g + q4, t1 = t0 + 4, cp = hh * 8 + 2 * ft + (p4 >> 1), cv = hh * 4 + (p4 >> 1), hb8 = (p4 & 1) * 8;
                lds_t pf0 = lds + E_KP + t0 * 256 + ((cp ^ swzF(t0 & 15)) << 4) + hb8, pf1 = lds + E_KP + t1 * 256 + ((cp ^ swzF(t1 & 15)) << 4) + hb8;
                lds_t va0 = lds + E_V + t0 * 128 + ((cv ^ swzP(t0)) << 4) + hb8, va1 = lds + E_V + t1 * 128 + ((cv ^ swzP(t1)) << 4) + hb8;
                lds_t vb0 = lds + E_V + t0 * 128 + (((cv + 2) ^ swzP(t0)) << 4) + hb8, vb1 = lds + E_V + t1 * 128 + (((cv + 2) ^ swzP(t1)) << 4) + hb8;
#pragma unroll
                for (int k8 = 0; k8 < 8; ++k8) {
                    const bf16x8 pf = lds_tr2(pf0 + k8 * 8192, pf1 + k8 * 8192), v0 = lds_tr2(va0 + k8 * 4096, va1 + k8 * 4096), v1 = lds_tr2(vb0 + k8 * 4096, vb1 + k8 * 4096);
                    kv0 = MFMA(pf, v0, kv0); kv1 = MFMA(pf, v1, kv1); zz = MFMA(pf, ones, zz);
                }
                ST_SC1(((u32x4){pk_bf16(kv0[0], kv0[1]), pk_bf16(kv0[2], kv0[3]), pk_bf16(kv1[0], kv1[1]), pk_bf16(kv1[2], kv1[3])}), rs,
                       WS_PART + (unsigned)(((panel * 32 + 2 * j + hh) * 4 + ft) * 1024 + lane * 16));
                if (l15 == 0) ST_SC1(__builtin_bit_cast(u32x4, zz), rs, WS_PZ + (unsigned)(((panel * 32 + 2 * j + hh) * 64 + 16 * ft + 4 * g) * 4));
            }
            SCHED();
            u32x4 h0 = {0, 0, 0, 0};
            if (has_h) h0 = geglu_c(0);
            WAIT_V(0);
            LBAR();
            if (threadIdx.x == 0) flag_set(ws, FL_KV + j * 8 + panel, tag);
            STAMP(8);
            JIT(9);
            prefetchB();
            if (has_h) {
                ST_SC1(h0, rs, hoff); SCHED();
                geglu(1); geglu(2); geglu(3); geglu(4); geglu(5); geglu(6); geglu(7);
            }
            WAIT_V(0);
            LBAR();
            if (threadIdx.x == 0) flag_set(ws, FL_H + (panel * 4 + ks) * 8 + c8, tag);
            STAMP(9);
        } else {
            u32x4 lg[4], P[4], M[4];
            LBAR();
            logits(lg);
            soft_pm<4>(lg, P, M);
#pragma unroll
            for (int u = 0; u < 4; ++u) asm volatile("" : "+v"(P[u]), "+v"(M[u]));
            SCHED();
            STAMP(6);
            JIT(11);
#pragma unroll
            for (int m = 0; m < 8; ++m) geglu(m);
            prefetchB();
            WAIT_V(4);
            LBAR();
            if (threadIdx.x == 0) flag_set(ws, FL_H + (panel * 4 + ks) * 8 + c8, tag);
            STAMP(7);
            if (wid == 0) flag_wait(ws, FL_KV + j * 8, 1, 8, tag, lane);
            if (blockIdx.x == 0 && wid == 1) {
                GAS u64* p = flagp(ws, FL_LN + lane);
#pragma unroll 1
                for (unsigned it = 0; it < (1u << 21); ++it) {
                    const u64 v0 = __hip_atomic_load(p, __ATOMIC_RELAXED, __HIP_MEMORY_SCOPE_AGENT), v1 = __hip_atomic_load(p + 64 * 8, __ATOMIC_RELAXED, __HIP_MEMORY_SCOPE_AGENT);
                    const u64 v2 = __hip_atomic_load(p + 128 * 8, __ATOMIC_RELAXED, __HIP_MEMORY_SCOPE_AGENT), v3 = __hip_atomic_load(p + 192 * 8, __ATOMIC_RELAXED, __HIP_MEMORY_SCOPE_AGENT);
                    if (__all(v0 == tag && v1 == tag && v2 == tag && v3 == tag)) { if (lane == 0) flag_set(ws, FL_EPOCH, tag); break; }
                    __builtin_amdgcn_s_sleep(1);
                }
            }
            LBAR();
            STAMP(8);
            u32x2 hfl = {0, 0};
            {
                const int hh = wid >> 2, ft = wid & 3;
                asm volatile("global_load_dwordx2 %0, %1, %2 sc1" : "+v"(hfl) : "v"((FL_H + (panel * 4 + ks) * 8 + (lane & 7)) * 64), "s"(ws + WS_FLG) : "memory");
                f32x4 a0, a1, az;
                u32x4 pw[8]; f32x4 pz[8];
#pragma unroll
                for (int p = 0; p < 8; ++p) {
                    pw[p] = LD_SC1(rs, WS_PART + (unsigned)(((p * 32 + 2 * j + hh) * 4 + ft) * 1024 + lane * 16));
                    pz[p] = __builtin_bit_cast(f32x4, LD_SC1(rs, WS_PZ + (unsigned)(((p * 32 + 2 * j + hh) * 64 + 16 * ft + 4 * g) * 4)));
                }
#pragma unroll
                for (int p = 0; p < 8; ++p) {
                    const f32x4 b0 = {lo_f(pw[p][0]), hi_f(pw[p][0]), lo_f(pw[p][1]), hi_f(pw[p][1])}, b1 = {lo_f(pw[p][2]), hi_f(pw[p][2]), lo_f(pw[p][3]), hi_f(pw[p][3])};
                    if (p == 0) { a0 = b0; a1 = b1; az = pz[0]; } else { a0 += b0; a1 += b1; az += pz[p]; }
                }
                lds_t kb = lds + L_KVL + ((hh * 2 + (ft >> 1)) * 3) * 1024 + lane * 16 + (ft & 1) * 8;
                *(LAS u32x2*)(kb) = (u32x2){pk_bf16(a0[0], a0[1]), pk_bf16(a0[2], a0[3])};
                *(LAS u32x2*)(kb + 1024) = (u32x2){pk_bf16(a1[0], a1[1]), pk_bf16(a1[2], a1[3])};
                *(LAS u32x2*)(kb + 2048) = (u32x2){pk_bf16(az[0], az[1]), pk_bf16(az[2], az[3])};
            }
            LBAR();
            STAMP(9);
            JIT(13);
            {
                asm volatile("" : "+v"(hfl));
                const u64 v = ((u64)hfl[1] << 32) | hfl[0];
                if (!__all(v == tag)) flag_wait(ws, FL_H + (panel * 4 + ks) * 8, 1, 8, tag, lane);
                startA(0); startA(1);
            }
#pragma unroll
            for (int hh = 0; hh < 2; ++hh) {
                bf16x8 kf[2][3];
#pragma unroll
                for (int s = 0; s < 2; ++s)
#pragma unroll
                    for (int d = 0; d < 3; ++d) kf[s][d] = lds_b128(lds + L_KVL + ((hh * 2 + s) * 3 + d) * 1024 + lane * 16);
#pragma unroll
                for (int tb = 0; tb < 2; ++tb) {
                    const bf16x8 Pf = __builtin_bit_cast(bf16x8, P[hh * 2 + tb]), Mf = __builtin_bit_cast(bf16x8, M[hh * 2 + tb]);
                    const f32x4 z4 = {0.f, 0.f, 0.f, 0.f};
                    const f32x4 n0 = MFMA(kf[1][0], Mf, MFMA(kf[0][0], Pf, z4));
                    const f32x4 n1 = MFMA(kf[1][1], Mf, MFMA(kf[0][1], Pf, z4));
                    const f32x4 dn = MFMA(kf[1][2], Mf, MFMA(kf[0][2], Pf, z4));
                    const float rden = __builtin_amdgcn_rcpf(rbf(rbf(dn[0]) + 1e-6f));
                    ST_SC1(pack_frag(n0.lo * rden, n0.hi * rden, n1.lo * rden, n1.hi * rden), rs, WS_H + (unsigned)(((panel * NKC2 + 112 + 2 * j + hh) * 16 + 2 * wid + tb) * 1024 + frag_off(lane)));
                }
            }
        }
    }
    STAMP(10);
    STAMP_OUT(0, 0, 11);

    STAMP(0);
    if (kvt) {
        if (wid == 0) flag_wait(ws, FL_H + (panel * 4 + ks) * 8, 1, 8, tag, lane);
        LBAR();
        startA(0); startA(1);
    }
    STAMP(1);
    f32x4 c2[8][4];
    {
        int tz3 = threadIdx.x; asm volatile("" : "+v"(tz3));
        const int lane = tz3 & 63, g = lane >> 4;
        const unsigned avoff = (unsigned)(wc * 4096 + lane * 16);
        const unsigned bvoff = (unsigned)((8 * wc + (lane >> 3)) * 2048 + c8 * 256 + (((lane & 7) ^ swzB(8 * wc + (lane >> 3))) << 4));
        const int wm = wc >> 1, wn = wc & 1;
        unsigned bb[4];
        {
            const int q4 = (lane >> 2) & 3, p = lane & 3, kr = 8 * g + q4, sw = swzB(kr);
#pragma unroll
            for (int tl = 0; tl < 4; ++tl) bb[tl] = ldsb + G2_SB + grp * 24576 + wn * 4096 + kr * 128 + (((2 * tl + (p >> 1)) ^ sw) << 4) + (p & 1) * 8;
        }
        lds_t ap = lds + grp * 49152 + wm * 8192 + lane * 16;
        u32x2 ofl = {0, 0};
        const unsigned fo = (FL_O + (panel * 4 + ks) * 4 + (lane & 3)) * 64;
        if (kvt) WAIT_V(4); else WAIT_V(8);
        LBAR();
        if (grp == 1) BAR();
        STAMP(2);
        auto step = [&](auto uc, auto fc, const int t) {
            constexpr int U = decltype(uc)::value, W = (U + 2) % 3;
            constexpr bool FIRST = decltype(fc)::value;
            bf16x8 A[8], B[4];
#define FST(x)
            FST(f0);
            if (U == 0) {
                if (t == 12) {
                    asm volatile("" : "+v"(ofl));
                    const u64 v = ((u64)ofl[1] << 32) | ofl[0];
                    if (!__all(v == tag)) flag_wait(ws, FL_O + (panel * 4 + ks) * 4, 1, 4, tag, lane);
                    sa = uni((cptr)ws + WS_H + ((size_t)(panel * NKC2 + 112 + 8 * ks + grp) << 14));
                    sbp = uni((cptr)wo + ((size_t)(8 * ks + grp) << 16));
                }
                asm volatile("s_cmp_lg_u32 %3, 9\n\ts_cbranch_scc1 .Lofl_%=\n\tglobal_load_dwordx2 %0, %1, %2 sc1\n.Lofl_%=:" : "+v"(ofl) : "v"(fo), "s"(ws + WS_FLG), "s"(t) : "scc", "memory");
            }
            if (U == 2 && t == 2 && !kvt && threadIdx.x == 0) flag_set(ws, FL_O + (panel * 4 + ks) * 4 + (c8 >> 1), tag);
            const bool more = U == 0 || t < 16;
            B[0] = lds_tr2_asm<U * 8192>(bb[0]); B[1] = lds_tr2_asm<U * 8192>(bb[1]);
            if (more) glds_s(bvoff, sbp, bl + W * 8192);
            B[2] = lds_tr2_asm<U * 8192>(bb[2]); B[3] = lds_tr2_asm<U * 8192>(bb[3]);
            if (more) { glds_s_o128(bvoff, sbp, bl + W * 8192 + 4096); sbp += 131072; }
            A[0] = lds_b128(ap + U * 16384); A[1] = lds_b128(ap + U * 16384 + 1024);
            if (more) { glds_s_sc1_first<0>(avoff, sa, al + W * 16384); glds_s_sc1_next<1024>(avoff, sa); }
            A[2] = lds_b128(ap + U * 16384 + 2048); A[3] = lds_b128(ap + U * 16384 + 3072); A[4] = lds_b128(ap + U * 16384 + 4096);
            if (more) { glds_s_sc1_next<2048>(avoff, sa); glds_s_sc1_next<3072>(avoff, sa); sa += 32768; }
            A[5] = lds_b128(ap + U * 16384 + 5120); A[6] = lds_b128(ap + U * 16384 + 6144); A[7] = lds_b128(ap + U * 16384 + 7168);
            FST(f1);
            WAIT_L0();
            FST(f2);
            if (!more) WAIT_V(0);
            SCHED(); BAR(); SCHED();
            FST(f3);
            __builtin_amdgcn_s_setprio(1);
    #pragma unroll
            for (int m = 0; m < 8; ++m)
#pragma unroll
                for (int n = 0; n < 4; ++n) { const int nn = (m & 1) ? 3 - n : n; if (FIRST) MFMA_SET(c2[m][nn], B[nn], A[m]); else MFMA_ACC(c2[m][nn], B[nn], A[m]); }
            __builtin_amdgcn_s_setprio(0);
            FST(f4);
            WAIT_V(6);
            FST(f5);
            SCHED(); BAR(); SCHED();
        };
        step(std::integral_constant<int, 0>{}, std::true_type{}, 0);
#pragma unroll 1
        for (int t = 1; t < 18; t += 3) {
            step(std::integral_constant<int, 1>{}, std::false_type{}, t); step(std::integral_constant<int, 2>{}, std::false_type{}, t + 1);
            if (t < 16) step(std::integral_constant<int, 0>{}, std::false_type{}, t + 2);
        }
        if (grp == 0) BAR();
    }
    STAMP(3);
    {
        int tz2 = threadIdx.x; asm volatile("" : "+v"(tz2));
        const int ln = tz2 & 63, tile = panel * 8 + c8, wm = wc >> 1, wn = wc & 1;
        const int qk = 2 * wm + grp, qs = qk ^ 1;
        const bool publisher = qk != ks;
        const int mh = wid >> 2, nq = wid & 3;
        const int row0 = panel * 256 + ks * 64 + mh * 32 + (ln & 15), col0 = c8 * 128 + nq * 32 + 8 * frag_kq(ln);
        constexpr int L_OWN = 0, L_SEND = 65536;
        u32x4 xr0 = {0, 0, 0, 0}, xr1 = xr0;
        {
            const unsigned xo = (unsigned)(row0 * D_ + col0) * 2u;
            asm volatile("global_load_dwordx4 %0, %1, %2" : "+v"(xr0) : "v"(xo), "s"(x) : "memory");
            asm volatile("global_load_dwordx4 %0, %1, %2" : "+v"(xr1) : "v"(xo + 16 * D_ * 2), "s"(x) : "memory");
        }
        f32x4 hk[4][4], hs[4][4];
#pragma unroll
        for (int m = 0; m < 4; ++m)
#pragma unroll
            for (int n = 0; n < 4; ++n) { hk[m][n] = grp ? c2[4 + m][n] : c2[m][n]; hs[m][n] = grp ? c2[m][n] : c2[4 + m][n]; }
        auto dump_own = [&](const f32x4 (&v)[4][4]) {
#pragma unroll
            for (int m = 0; m < 4; ++m)
#pragma unroll
                for (int n = 0; n < 4; ++n) *(LAS f32x4*)(lds + L_OWN + ((grp * 4 + m) * 8 + 4 * wn + n) * 1024 + ln * 16) = v[m][n];
        };
        if (qs == ks) dump_own(hs);
        else {
#pragma unroll
            for (int m = 0; m < 4; ++m)
#pragma unroll
                for (int n2 = 0; n2 < 2; ++n2) {
                    const f32x4 a = hs[m][2 * n2], b = hs[m][2 * n2 + 1];
                    *(LAS u32x4*)(lds + L_SEND + (wid ^ 4) * 8192 + (m * 2 + n2) * 1024 + ln * 16) = (u32x4){pk_bf16(a[0], a[1]), pk_bf16(a[2], a[3]), pk_bf16(b[0], b[1]), pk_bf16(b[2], b[3])};
                }
        }
        if (qk == ks) dump_own(hk);
        LBAR();
        if (publisher) {
            const unsigned xbase = WS_XCH + (unsigned)((((tile * 4 + ks) * 8 + wid) * 8) * 1024 + ln * 16);
#pragma unroll
            for (int m = 0; m < 4; ++m)
#pragma unroll
                for (int n2 = 0; n2 < 2; ++n2) {
                    const u32x4 w = *(const LAS u32x4*)(lds + L_SEND + wid * 8192 + (m * 2 + n2) * 1024 + ln * 16);
                    const f32x4 a = hk[m][2 * n2] + (f32x4){lo_f(w[0]), hi_f(w[0]), lo_f(w[1]), hi_f(w[1])}, b = hk[m][2 * n2 + 1] + (f32x4){lo_f(w[2]), hi_f(w[2]), lo_f(w[3]), hi_f(w[3])};
                    ST_SC1(((u32x4){pk_bf16(a[0], a[1]), pk_bf16(a[2], a[3]), pk_bf16(b[0], b[1]), pk_bf16(b[2], b[3])}), rs, xbase + (m * 2 + n2) * 1024);
                }
        }
        f32x4 e[2][2];
#pragma unroll
        for (int a = 0; a < 2; ++a)
#pragma unroll
            for (int b = 0; b < 2; ++b)
                e[a][b] = *(const LAS f32x4*)(lds + L_OWN + ((2 * mh + a) * 8 + 2 * nq + b) * 1024 + ln * 16) + *(const LAS f32x4*)(lds + L_OWN + ((4 + 2 * mh + a) * 8 + 2 * nq + b) * 1024 + ln * 16);
        STAMP(4);
        asm volatile("s_waitcnt vmcnt(0)" : "+v"(xr0), "+v"(xr1)::"memory");
        if (publisher && ln == 0) flag_set(ws, FL_X + (tile * 4 + ks) * 8 + wid, tag);
        STAMP(5);
        JIT(19);
        const int wsrc = (ks & 1) * 4 + (ks >> 1) * 2 + (nq >> 1);
        {
            GAS u64* p = flagp(ws, FL_X + (tile * 4 + ((ks + 1 + (ln < 3 ? ln : 0)) & 3)) * 8 + wsrc);
#pragma unroll 1
            for (unsigned it = 0; it < (1u << 21); ++it) {
                const u64 v = __hip_atomic_load(p, __ATOMIC_RELAXED, __HIP_MEMORY_SCOPE_AGENT);
                if (__all(v == tag)) break;
                __builtin_amdgcn_s_sleep(1);
            }
            asm volatile("" ::: "memory");
        }
        u32x4 pw[3][2];
#pragma unroll
        for (int i = 0; i < 3; ++i) {
            const unsigned base = WS_XCH + (unsigned)((((tile * 4 + i + (i >= ks ? 1 : 0)) * 8 + wsrc) * 8 + (nq & 1) + 4 * mh) * 1024 + ln * 16);
            pw[i][0] = LD_SC1(rs, base); pw[i][1] = LD_SC1(rs, base + 2048);
        }
#pragma unroll
        for (int i = 0; i < 3; ++i)
#pragma unroll
            for (int a = 0; a < 2; ++a) {
                const u32x4 w = pw[i][a];
                e[a][0] += (f32x4){lo_f(w[0]), hi_f(w[0]), lo_f(w[1]), hi_f(w[1])};
                e[a][1] += (f32x4){lo_f(w[2]), hi_f(w[2]), lo_f(w[3]), hi_f(w[3])};
            }
        const __amdgpu_buffer_rsrc_t ors = __builtin_amdgcn_make_buffer_rsrc((void*)out, 0, S_ * D_ * 2, 0x00020000);
#pragma unroll
        for (int a = 0; a < 2; ++a) {
            float u[4], v[4];
#pragma unroll
            for (int q = 0; q < 4; ++q) {
                auto rr = __builtin_amdgcn_permlane16_swap(__float_as_uint(e[a][0][q]), __float_as_uint(e[a][1][q]), false, false);
                u[q] = __uint_as_float(rr[0]); v[q] = __uint_as_float(rr[1]);
            }
            const u32x4 xv = a ? xr1 : xr0;
            u32x4 o;
            o[0] = pk_bf16(u[0] + lo_f(xv[0]), u[1] + hi_f(xv[0])); o[1] = pk_bf16(u[2] + lo_f(xv[1]), u[3] + hi_f(xv[1]));
            o[2] = pk_bf16(v[0] + lo_f(xv[2]), v[1] + hi_f(xv[2])); o[3] = pk_bf16(v[2] + lo_f(xv[3]), v[3] + hi_f(xv[3]));
            __builtin_amdgcn_raw_buffer_store_b128(o, ors, (unsigned)(((row0 + 16 * a) * D_ + col0) * 2), 0, 16);
        }
    }
    STAMP(6);
    STAMP_OUT(1, 0, 7);
}

extern "C" void kernel_launch(void* const* d_in, const int* in_sizes, int n_in, void* d_out, int out_size, void* d_ws, size_t ws_size, hipStream_t stream) {
    const u16* x = (const u16*)d_in[0]; const u16* wqkv = (const u16*)d_in[1]; const u16* wphi = (const u16*)d_in[2]; const u16* wo = (const u16*)d_in[3];
    const u16* wg = (const u16*)d_in[5]; const u16* wu = (const u16*)d_in[6]; const u16* wd = (const u16*)d_in[7];
    (void)hipFuncSetAttribute((const void*)k_fused, hipFuncAttributeMaxDynamicSharedMemorySize, L_TOTAL);
    k_fused<<<256, 512, L_TOTAL, stream>>>(x, wqkv, wphi, wo, wg, wu, wd, (unsigned char*)d_ws, (u16*)d_out);
}
```

```cpp
#define PAD_DIAG
#define PAD_GLOBAL 1
#include <hip/hip_runtime.h>
#include <type_traits>

typedef unsigned short u16;
typedef unsigned long long u64;
typedef short bf16x8 __attribute__((ext_vector_type(8)));
typedef short s16x4 __attribute__((ext_vector_type(4)));
typedef float f32x4 __attribute__((ext_vector_type(4)));
typedef float f32x2 __attribute__((ext_vector_type(2)));
typedef unsigned u32x4 __attribute__((ext_vector_type(4)));
typedef unsigned u32x2 __attribute__((ext_vector_type(2)));
#define LAS __attribute__((address_space(3)))
#define GAS __attribute__((address_space(1)))
typedef LAS unsigned char* lds_t;
typedef const unsigned char* cptr;

constexpr int S_ = 2048, D_ = 1024, F_ = 3584;
constexpr int CODE_LINES = 256;
constexpr int NKC2 = 144;

constexpr unsigned WS_XN   = 0;
constexpr unsigned WS_H    = WS_XN + S_ * D_ * 2;
constexpr unsigned WS_PART = WS_H + 8 * NKC2 * 16 * 1024;
constexpr unsigned WS_PZ   = WS_PART + 8 * 32 * 4 * 1024;
constexpr unsigned WS_XCH  = WS_PZ + 8 * 32 * 64 * 4;
constexpr unsigned WS_FLG  = WS_XCH + 64 * 4 * 8 * 8 * 1024;
constexpr unsigned FL_LN = 0, FL_KV = FL_LN + 256, FL_H = FL_KV + 128, FL_O = FL_H + 256, FL_X = FL_O + 128, FL_EPOCH = FL_X + 2048, FL_N = FL_EPOCH + 1;
constexpr unsigned WS_END  = WS_FLG + FL_N * 64;

constexpr int G1_SA = 16384, G1_STAGE = 36864;
constexpr int L_MISC = 158720, L_WPHI = 159744, L_TOTAL = 163840;
constexpr int L_KVL = 147456;
constexpr int E_KP = 0, E_V = 65536;
constexpr int G2_SB = 98304;

__device__ __forceinline__ unsigned pk_bf16(float lo, float hi) {
    typedef __bf16 bf2 __attribute__((ext_vector_type(2)));
    bf2 r = {(__bf16)lo, (__bf16)hi};
    return __builtin_bit_cast(unsigned, r);
}
__device__ __forceinline__ float rbf(float x) { return __uint_as_float(pk_bf16(x, x) & 0xffff0000u); }
__device__ __forceinline__ float lo_f(unsigned w) { return __uint_as_float(w << 16); }
__device__ __forceinline__ float hi_f(unsigned w) { return __uint_as_float(w & 0xffff0000u); }

#define STAMP_DECL
#define STAMP(i)
#define STAMP_ARG
#define STAMP_PASS
#define STAMP_OUT(kern, lo, n)

#define XW_CNT "18"
#define JIT(k)
#define WAIT_V(n) asm volatile("s_waitcnt vmcnt(" #n ")" ::: "memory")
#define WAIT_L0() do { asm volatile("" ::: "memory"); __builtin_amdgcn_s_waitcnt(0xC07F); asm volatile("" ::: "memory"); } while (0)
#define BAR() __builtin_amdgcn_s_barrier()
#define LBAR() asm volatile("s_waitcnt lgkmcnt(0)\n\ts_barrier" ::: "memory")
#define SCHED() __builtin_amdgcn_sched_barrier(0)
#define MFMA(a, b, c) __builtin_amdgcn_mfma_f32_16x16x32_bf16(a, b, c, 0, 0, 0)
#define MFMA_SET(c, a, b) asm volatile("v_mfma_f32_16x16x32_bf16 %0, %1, %2, 0" : "=&v"(c) : "v"(a), "v"(b))
#define MFMA_ACC(c, a, b) asm volatile("v_mfma_f32_16x16x32_bf16 %0, %1, %2, %0" : "+v"(c) : "v"(a), "v"(b))

__device__ __forceinline__ cptr uni(cptr p) {
    const u64 v = (u64)p;
    return (cptr)(((u64)(unsigned)__builtin_amdgcn_readfirstlane((unsigned)(v >> 32)) << 32) | (unsigned)__builtin_amdgcn_readfirstlane((unsigned)v));
}
__device__ __forceinline__ void glds_v(cptr g, unsigned l) {
    asm volatile("s_mov_b32 m0, %1\n\ts_nop 0\n\tglobal_load_lds_dwordx4 %0, off" ::"v"(g), "s"(l) : "memory");
}
__device__ __forceinline__ void glds_s(unsigned voff, cptr sb, unsigned l) {
    asm volatile("s_nop 2\n\ts_mov_b32 m0, %2\n\ts_nop 0\n\tglobal_load_lds_dwordx4 %0, %1" ::"v"(voff), "s"(sb), "s"(l) : "memory");
}
__device__ __forceinline__ void glds_s_o128(unsigned voff, cptr sb, unsigned l) {
    asm volatile("s_mov_b32 m0, %2\n\ts_nop 0\n\tglobal_load_lds_dwordx4 %0, %1 offset:128" ::"v"(voff), "s"(sb), "s"(l - 128u) : "memory");
}
template <int OFF> __device__ __forceinline__ void glds_s_sc1_first(unsigned voff, cptr sb, unsigned l) {
    asm volatile("s_nop 2\n\ts_mov_b32 m0, %2\n\ts_nop 0\n\tglobal_load_lds_dwordx4 %0, %1 offset:%3 sc1" ::"v"(voff), "s"(sb), "s"(l), "i"(OFF) : "memory");
}
template <int OFF> __device__ __forceinline__ void glds_s_sc1_next(unsigned voff, cptr sb) {
    asm volatile("global_load_lds_dwordx4 %0, %1 offset:%2 sc1" ::"v"(voff), "s"(sb), "i"(OFF) : "memory");
}
template <int OFF> __device__ __forceinline__ void gld_sc1(u32x4& d, unsigned voff, cptr sb) {
    asm volatile("global_load_dwordx4 %0, %1, %2 offset:%3 sc1" : "+v"(d) : "v"(voff), "s"(sb), "i"(OFF) : "memory");
}
__device__ __forceinline__ bf16x8 lds_b128(lds_t p) { return *(const LAS bf16x8*)p; }
__device__ __forceinline__ bf16x8 lds_tr2(lds_t p0, lds_t p1) {
    s16x4 a = __builtin_amdgcn_ds_read_tr16_b64_v4i16((LAS s16x4*)p0);
    s16x4 b = __builtin_amdgcn_ds_read_tr16_b64_v4i16((LAS s16x4*)p1);
    return __builtin_shufflevector(a, b, 0, 1, 2, 3, 4, 5, 6, 7);
}
template <int OFF> __device__ __forceinline__ bf16x8 lds_tr2_asm(unsigned addr) {
    s16x4 a, b;
    asm volatile("ds_read_b64_tr_b16 %0, %2 offset:%3\n\tds_read_b64_tr_b16 %1, %2 offset:%4" : "=&v"(a), "=&v"(b) : "v"(addr), "i"(OFF), "i"(OFF + 512) : "memory");
    return __builtin_shufflevector(a, b, 0, 1, 2, 3, 4, 5, 6, 7);
}

__device__ __forceinline__ float x16(float v, float& o) { auto r = __builtin_amdgcn_permlane16_swap(__float_as_uint(v), __float_as_uint(v), false, false); o = __uint_as_float(r[1]); return __uint_as_float(r[0]); }
__device__ __forceinline__ float x32(float v, float& o) { auto r = __builtin_amdgcn_permlane32_swap(__float_as_uint(v), __float_as_uint(v), false, false); o = __uint_as_float(r[1]); return __uint_as_float(r[0]); }
__device__ __forceinline__ float sum_g(float v) { float o, a = x16(v, o); v = a + o; a = x32(v, o); return a + o; }
__device__ __forceinline__ float max_g(float v) { float o, a = x16(v, o); v = fmaxf(a, o); a = x32(v, o); return fmaxf(a, o); }
__device__ __forceinline__ float min_g(float v) { float o, a = x16(v, o); v = fminf(a, o); a = x32(v, o); return fminf(a, o); }
__device__ __forceinline__ float ror8(float v) { return __int_as_float(__builtin_amdgcn_update_dpp(0, __float_as_int(v), 0x128, 0xf, 0xf, false)); }

__device__ __forceinline__ int swzB(int k) { return (((k >> 1) & 1) << 1) | (((k >> 3) & 1) << 2); }
__device__ __forceinline__ int swzP(int k) { return ((k >> 2) & 1) | (((k >> 1) & 1) << 1) | (((k >> 3) & 1) << 2); }
__device__ __forceinline__ int swzF(int k) { return ((k >> 2) & 1) | ((k & 3) << 1) | (((k >> 3) & 1) << 3); }

__device__ __forceinline__ f32x2 gelu_mul2(f32x2 g, f32x2 u) {
    constexpr float C0 = -2.0f * 0.7978845608028654f * 1.4426950408889634f, C1 = C0 * 0.044715f;
    const f32x2 a = g * (g * g * C1 + C0);
    const f32x2 d = (f32x2){__builtin_amdgcn_exp2f(a[0]), __builtin_amdgcn_exp2f(a[1])} + 1.0f;
    return (g * u) * (f32x2){__builtin_amdgcn_rcpf(d[0]), __builtin_amdgcn_rcpf(d[1])};
}
__device__ __forceinline__ u32x4 pack_frag(const f32x2 a0, const f32x2 a1, const f32x2 b0, const f32x2 b1) {
    unsigned x0 = pk_bf16(a0[0], a0[1]), x1 = pk_bf16(a1[0], a1[1]), y0 = pk_bf16(b0[0], b0[1]), y1 = pk_bf16(b1[0], b1[1]);
    auto r0 = __builtin_amdgcn_permlane16_swap(x0, y0, false, false);
    auto r1 = __builtin_amdgcn_permlane16_swap(x1, y1, false, false);
    u32x4 o; o[0] = r0[0]; o[1] = r1[0]; o[2] = r0[1]; o[3] = r1[1];
    return o;
}
__device__ __forceinline__ int frag_kq(int lane) { const int g = lane >> 4; return ((g & 1) << 1) | (g >> 1); }
__device__ __forceinline__ int frag_off(int lane) { return (frag_kq(lane) * 16 + (lane & 15)) * 16; }

__device__ __forceinline__ float max3(float a, float b, float c) { float r; asm("v_max3_f32 %0, %1, %2, %3" : "=v"(r) : "v"(a), "v"(b), "v"(c)); return r; }
__device__ __forceinline__ float min3(float a, float b, float c) { float r; asm("v_min3_f32 %0, %1, %2, %3" : "=v"(r) : "v"(a), "v"(b), "v"(c)); return r; }
__device__ __forceinline__ float max2(float a, float b) { float r; asm("v_max_f32 %0, %1, %2" : "=v"(r) : "v"(a), "v"(b)); return r; }
template <int NU, class Op> __device__ __forceinline__ void red2_g(float (&a)[NU], float (&b)[NU], Op op) {
    float z[NU], y[NU];
#pragma unroll
    for (int u = 0; u < NU; ++u) {
        auto r = __builtin_amdgcn_permlane16_swap(__float_as_uint(a[u]), __float_as_uint(b[u]), false, false);
        z[u] = op(__uint_as_float(r[0]), __uint_as_float(r[1]));
    }
#pragma unroll
    for (int u = 0; u < NU; ++u) {
        auto q = __builtin_amdgcn_permlane32_swap(__float_as_uint(z[u]), __float_as_uint(z[u]), false, false);
        y[u] = op(__uint_as_float(q[0]), __uint_as_float(q[1]));
    }
#pragma unroll
    for (int u = 0; u < NU; ++u) {
        auto w = __builtin_amdgcn_permlane16_swap(__float_as_uint(y[u]), __float_as_uint(y[u]), false, false);
        a[u] = __uint_as_float(w[0]); b[u] = __uint_as_float(w[1]);
    }
}
template <int NU> __device__ __forceinline__ void soft_pm(const u32x4 (&w)[NU], u32x4 (&P)[NU], u32x4 (&M)[NU]) {
    constexpr float L2E = 1.4426950408889634f;
    f32x2 pv[NU][4]; float mx[NU], nm[NU];
#pragma unroll
    for (int u = 0; u < NU; ++u) {
#pragma unroll
        for (int i = 0; i < 4; ++i) pv[u][i] = (f32x2){lo_f(w[u][i]), hi_f(w[u][i])};
        mx[u] = max3(max3(max3(pv[u][0][0], pv[u][0][1], pv[u][1][0]), pv[u][1][1], pv[u][2][0]), max2(pv[u][2][1], pv[u][3][0]), pv[u][3][1]);
        nm[u] = -min3(min3(min3(pv[u][0][0], pv[u][0][1], pv[u][1][0]), pv[u][1][1], pv[u][2][0]), min3(pv[u][2][1], pv[u][3][0], pv[u][3][1]), pv[u][3][1]);
    }
    red2_g<NU>(mx, nm, [](float a, float b) { return max2(a, b); });
    f32x2 pp[NU][4], pm[NU][4]; float sp[NU], sm[NU];
#pragma unroll
    for (int u = 0; u < NU; ++u) {
        const float cp = -mx[u] * L2E, cm = -nm[u] * L2E;
#pragma unroll
        for (int i = 0; i < 4; ++i) {
            const f32x2 a = pv[u][i] * L2E + cp, b = pv[u][i] * (-L2E) + cm;
            pp[u][i] = (f32x2){__builtin_amdgcn_exp2f(a[0]), __builtin_amdgcn_exp2f(a[1])};
            pm[u][i] = (f32x2){__builtin_amdgcn_exp2f(b[0]), __builtin_amdgcn_exp2f(b[1])};
        }
        const f32x2 s2 = (pp[u][0] + pp[u][1]) + (pp[u][2] + pp[u][3]), m2 = (pm[u][0] + pm[u][1]) + (pm[u][2] + pm[u][3]);
        sp[u] = s2[0] + s2[1]; sm[u] = m2[0] + m2[1];
    }
    red2_g<NU>(sp, sm, [](float a, float b) { return a + b; });
#pragma unroll
    for (int u = 0; u < NU; ++u) {
        const float rp = __builtin_amdgcn_rcpf(sp[u]), rm = __builtin_amdgcn_rcpf(sm[u]);
#pragma unroll
        for (int i = 0; i < 4; ++i) { const f32x2 a = pp[u][i] * rp, b = pm[u][i] * rm; P[u][i] = pk_bf16(a[0], a[1]); M[u][i] = pk_bf16(b[0], b[1]); }
    }
}

__device__ __forceinline__ GAS u64* flagp(unsigned char* ws, unsigned i) { return (GAS u64*)(ws + WS_FLG + (size_t)i * 64); }
__device__ __forceinline__ void flag_set(unsigned char* ws, unsigned i, u64 tag) { __hip_atomic_store(flagp(ws, i), tag, __ATOMIC_RELAXED, __HIP_MEMORY_SCOPE_AGENT); }
__device__ __forceinline__ void flag_wait(unsigned char* ws, unsigned i0, unsigned st, int n, u64 tag, int lane) {
    GAS u64* p = flagp(ws, i0 + st * (unsigned)(lane < n ? lane : 0));
#pragma unroll 1
    for (unsigned it = 0; it < (1u << 21); ++it) {
        const u64 v = __hip_atomic_load(p, __ATOMIC_RELAXED, __HIP_MEMORY_SCOPE_AGENT);
        if (__all(v == tag)) break;
        __builtin_amdgcn_s_sleep(1);
    }
    asm volatile("" ::: "memory");
}
__device__ __forceinline__ __amdgpu_buffer_rsrc_t ws_rsrc(unsigned char* ws) { return __builtin_amdgcn_make_buffer_rsrc((void*)ws, 0, WS_END, 0x00020000); }
#define ST_SC1(v, rs, off) __builtin_amdgcn_raw_buffer_store_b128(v, rs, off, 0, 16)
#define LD_SC1(rs, off) __builtin_amdgcn_raw_buffer_load_b128(rs, off, 0, 16)

template <int GRP>
__device__ __forceinline__ void g1_mainloop(const unsigned ldsb, lds_t lds, f32x4 (&acc)[8][5], cptr (&bp)[5], const size_t ld4, cptr xn, const int wl, const int lane, const int (&ba)[5],
                                            cptr wd, cptr wo) {
    const size_t ldq = (size_t)32 * 3072 * 2, ldg = (size_t)32 * F_ * 2;
    const int aoff = GRP * 8192 + lane * 16;
    const int p0 = wl == 0 ? 0 : 5 * wl + 1;
    const unsigned av0 = p0 * 1024 + lane * 16, av1 = av0 + 4096;
    int so = 0, sl = 3 * G1_STAGE;
    cptr an = xn + 3 * 16384;
    unsigned touch = 0;
    cptr tp = wd + (size_t)(blockIdx.x * 224 + (lane & 15)) * 128;
    bf16x8 A[8], B[5];
#pragma unroll 1
    for (int t = 0; t < 32; ++t) {
        const bool more = t < 29;
        const unsigned lb = ldsb + sl + G1_SA + wl * 1024, la = ldsb + sl + p0 * 1024, rb = ldsb + so;
        if (more) {
            if (GRP == 0) {
                B[0] = lds_tr2_asm<0>(rb + ba[0]); B[1] = lds_tr2_asm<0>(rb + ba[1]);
                glds_v(bp[0], lb);
                B[2] = lds_tr2_asm<0>(rb + ba[2]); B[3] = lds_tr2_asm<0>(rb + ba[3]);
                glds_v(bp[1], lb + 4096);
                B[4] = lds_tr2_asm<0>(rb + ba[4]);
                A[0] = lds_b128(lds + so + aoff); A[1] = lds_b128(lds + so + aoff + 1024);
                glds_v(bp[2], lb + 8192);
                A[2] = lds_b128(lds + so + aoff + 2048); A[3] = lds_b128(lds + so + aoff + 3072); A[4] = lds_b128(lds + so + aoff + 4096);
                glds_v(bp[3], lb + 12288);
                A[5] = lds_b128(lds + so + aoff + 5120); A[6] = lds_b128(lds + so + aoff + 6144); A[7] = lds_b128(lds + so + aoff + 7168);
                glds_v(bp[4], lb + 16384);
                bp[0] += ldq; bp[1] += ldg; bp[2] += ldg; bp[3] += ldg; bp[4] += ld4;
                WAIT_L0(); WAIT_V(10);
            } else if (wl < 3) {
                B[0] = lds_tr2_asm<0>(rb + ba[0]); B[1] = lds_tr2_asm<0>(rb + ba[1]);
                glds_s_sc1_first<0>(av0, an, la);
                B[2] = lds_tr2_asm<0>(rb + ba[2]); B[3] = lds_tr2_asm<0>(rb + ba[3]);
                glds_s_sc1_next<1024>(av0, an);
                B[4] = lds_tr2_asm<0>(rb + ba[4]);
                A[0] = lds_b128(lds + so + aoff); A[1] = lds_b128(lds + so + aoff + 1024);
                glds_s_sc1_next<2048>(av0, an);
                A[2] = lds_b128(lds + so + aoff + 2048); A[3] = lds_b128(lds + so + aoff + 3072);
                glds_s_sc1_next<3072>(av0, an);
                A[4] = lds_b128(lds + so + aoff + 4096); A[5] = lds_b128(lds + so + aoff + 5120);
                glds_s_sc1_first<0>(av1, an, la + 4096);
                A[6] = lds_b128(lds + so + aoff + 6144); A[7] = lds_b128(lds + so + aoff + 7168);
                if (wl == 0) glds_s_sc1_next<1024>(av1, an);
                an += 16384;
                WAIT_L0();
                if (wl == 0) WAIT_V(12); else WAIT_V(10);
            } else {
                if (t < 18) {
                    asm volatile("global_load_dword %0, %1, off" : "+v"(touch) : "v"(tp) : "memory");
                    if (t == 13) tp = wo + (size_t)(blockIdx.x * 64 + (lane & 15)) * 128; else tp += 2048;
                }
#pragma unroll
                for (int n = 0; n < 5; ++n) B[n] = lds_tr2_asm<0>(rb + ba[n]);
#pragma unroll
                for (int m = 0; m < 8; ++m) A[m] = lds_b128(lds + so + aoff + m * 1024);
                WAIT_L0();
            }
        } else {
#pragma unroll
            for (int n = 0; n < 5; ++n) B[n] = lds_tr2_asm<0>(rb + ba[n]);
#pragma unroll
            for (int m = 0; m < 8; ++m) A[m] = lds_b128(lds + so + aoff + m * 1024);
            WAIT_L0(); WAIT_V(0);
        }
        so = (so == 3 * G1_STAGE) ? 0 : so + G1_STAGE;
        sl = (sl == 3 * G1_STAGE) ? 0 : sl + G1_STAGE;
        asm volatile("" : "+s"(so), "+s"(sl));
        SCHED(); BAR(); SCHED();
        __builtin_amdgcn_s_setprio(1);
#pragma unroll
        for (int m = 0; m < 8; ++m)
#pragma unroll
            for (int n = 0; n < 5; ++n) { const int nn = (m & 1) ? 4 - n : n; MFMA_ACC(acc[m][nn], B[nn], A[m]); }
        __builtin_amdgcn_s_setprio(0);
        SCHED(); BAR(); SCHED();
    }
    asm volatile("" ::"v"(touch));
}

__device__ __forceinline__ void g2_issueB(unsigned bvoff, cptr sb, unsigned l) {
    glds_s(bvoff, sb, l);
    glds_s_o128(bvoff, sb, l + 4096);
}

__global__ __launch_bounds__(512, 2) void k_fused(const u16* __restrict__ x, const u16* __restrict__ wqkv, const u16* __restrict__ wphi, const u16* __restrict__ wo,
                                                  const u16* __restrict__ wg, const u16* __restrict__ wu, const u16* __restrict__ wd,
                                                  unsigned char* __restrict__ ws, u16* __restrict__ out) {
    extern __shared__ __attribute__((aligned(16))) unsigned char shm[];
    lds_t lds = (lds_t)shm;
    const unsigned ldsb = (unsigned)(size_t)lds;
    const int wid = __builtin_amdgcn_readfirstlane(threadIdx.x >> 6);
    const int grp = wid >> 2, wc = wid & 3;
    const int x8 = blockIdx.x & 7, r = blockIdx.x >> 3, ks = x8 >> 1, panel = 4 * (x8 & 1) + (r >> 3), c8 = r & 7, j = 4 * ks + (c8 >> 1), kvt = c8 & 1;
    const __amdgpu_buffer_rsrc_t rs = ws_rsrc(ws);
    STAMP_DECL; STAMP(0);
    const u64 t_start = __builtin_amdgcn_s_memrealtime();
#define PAD_FROM(t0_, T) do { while (__builtin_amdgcn_s_memrealtime() - (t0_) < (u64)(T)) __builtin_amdgcn_s_sleep(2); } while (0)
    if (threadIdx.x == 0) __hip_atomic_store((GAS u64*)(ws + ((size_t)100 << 20) + blockIdx.x * 64), t_start, __ATOMIC_RELAXED, __HIP_MEMORY_SCOPE_AGENT);
#define PAD(T) do { while (__builtin_amdgcn_s_memrealtime() - t_start < (u64)(T)) __builtin_amdgcn_s_sleep(2); } while (0)
    u64 tag;
    f32x4 acc[8][5];

    {
        const int lane = threadIdx.x & 63, g = lane >> 4;
        const int row = panel * 256 + (ks * 8 + c8) * 8 + (lane & 7);
        u32x4 xv[4]; u32x2 ev = {0, 0};
#pragma unroll
        for (int i = 0; i < 4; ++i) xv[i] = (u32x4){0, 0, 0, 0};
        {
            const unsigned zero = 0;
            asm volatile("global_load_dwordx2 %0, %1, %2 sc1" : "+v"(ev) : "v"(zero), "s"(ws + WS_FLG + FL_EPOCH * 64) : "memory");
        }
        unsigned tlbt = 0, codet = 0;
        cptr bp[5];
        const size_t ldq = (size_t)32 * 3072 * 2, ldg = (size_t)32 * F_ * 2, ld4 = kvt ? ldq : ldg;
        if (grp == 0) {
            const int gq = (j & 1) ? 7 * j + 3 : 7 * j, gp = (j & 1) ? 7 * j + 1 : 7 * j + 4, gh = (j & 1) ? 7 * j : 7 * j + 6;
            const int krow = 8 * wc + (lane >> 3), chunk = (lane & 7) ^ swzB(krow);
            const size_t rq = (size_t)krow * 3072 * 2 + chunk * 16, rg = (size_t)krow * F_ * 2 + chunk * 16;
            if (!kvt) {
                bp[0] = (cptr)(wqkv + 64 * j) + rq;
                bp[1] = (cptr)(wg + 32 * gq) + rg;      bp[2] = (cptr)(wg + 32 * gq + 64) + rg;
                bp[3] = (cptr)(wu + 32 * gq) + rg;      bp[4] = (cptr)(wu + 32 * gq + 64) + rg;
            } else {
                bp[0] = (cptr)(wqkv + 1024 + 64 * j) + rq;
                bp[1] = (cptr)(wg + 32 * gp) + rg;
                bp[2] = (chunk < 4 ? (cptr)(wg + 32 * gh) : (cptr)(wu + 32 * gh) - 64) + rg;
                bp[3] = (cptr)(wu + 32 * gp) + rg;
                bp[4] = (cptr)(wqkv + 2048 + 64 * j) + rq;
            }
#pragma unroll
            for (int s = 0; s < 3; ++s) {
#pragma unroll
                for (int c = 0; c < 5; ++c) glds_v(bp[c], ldsb + s * G1_STAGE + G1_SA + c * 4096 + wc * 1024);
                bp[0] += ldq; bp[1] += ldg; bp[2] += ldg; bp[3] += ldg; bp[4] += ld4;
            }
            glds_v((cptr)(wphi + (size_t)(2 * j) * 1024 + wc * 512 + lane * 8), ldsb + L_WPHI + wc * 1024);
            {
                const int e = r * 40 + wc * 10 + (lane < 10 ? lane : 9);
                cptr p;
                if (e < 96) p = (cptr)wqkv + (size_t)e * 65536;
                else if (e < 208) p = (cptr)wg + (size_t)(e - 96) * 65536;
                else if (e < 320) p = (cptr)wu + (size_t)(e - 208) * 65536;
                else if (e < 432) p = (cptr)wd + (size_t)(e - 320) * 65536;
                else if (e < 464) p = (cptr)wo + (size_t)(e - 432) * 65536;
                else if (e < 528) p = (cptr)out + (size_t)(e - 464) * 65536;
                else if (e < 592) p = (cptr)x + (size_t)(e - 528) * 65536;
                else p = (cptr)ws + (size_t)min(e - 592, (int)(WS_END / 65536)) * 65536;
                asm volatile("global_load_dword %0, %1, off sc1" : "+v"(tlbt) : "v"(p) : "memory");
            }
            {
                u64 pc; asm volatile("s_getpc_b64 %0" : "=s"(pc));
                const int ln_ = wc * 64 + lane;
                cptr p = (cptr)(pc & ~(u64)0x7f) + (ln_ < CODE_LINES ? ln_ : CODE_LINES - 1) * 128;
                asm volatile("global_load_dword %0, %1, off" : "+v"(codet) : "v"(p) : "memory");
            }
        } else {
#pragma unroll
            for (int c = 0; c < 5; ++c) bp[c] = nullptr;
            const unsigned xo = (unsigned)(row * D_ + (wc * 8 + (lane >> 3)) * 8) * 2u;
            asm volatile("global_load_dwordx4 %0, %4, %5\n\tglobal_load_dwordx4 %1, %4, %5 offset:512\n\tglobal_load_dwordx4 %2, %4, %5 offset:1024\n\tglobal_load_dwordx4 %3, %4, %5 offset:1536\n\t"
                         "s_waitcnt vmcnt(0)" : "+v"(xv[0]), "+v"(xv[1]), "+v"(xv[2]), "+v"(xv[3]) : "v"(xo), "s"(x) : "memory");
        }
        asm volatile("s_waitcnt vmcnt(" XW_CNT ")" : "+v"(ev)::"memory");
        {
            u64 z = (((u64)(unsigned)__builtin_amdgcn_readfirstlane(ev[1]) << 32) | (unsigned)__builtin_amdgcn_readfirstlane(ev[0])) + 0x9E3779B97F4A7C15ull;
            z = (z ^ (z >> 30)) * 0xBF58476D1CE4E5B9ull; z = (z ^ (z >> 27)) * 0x94D049BB133111EBull; tag = z ^ (z >> 31);
        }
        STAMP(1);
        JIT(1);
        LAS f32x2* red = (LAS f32x2*)(lds + L_MISC);
        float f[32];
        if (grp == 1) {
            float s = 0.f;
#pragma unroll
            for (int i = 0; i < 4; ++i)
#pragma unroll
                for (int k = 0; k < 4; ++k) { f[8 * i + 2 * k] = lo_f(xv[i][k]); f[8 * i + 2 * k + 1] = hi_f(xv[i][k]); }
            { f32x2 s4[4];
#pragma unroll
              for (int i = 0; i < 4; ++i) s4[i] = (f32x2){f[2 * i], f[2 * i + 1]};
#pragma unroll
              for (int i = 4; i < 16; ++i) s4[i & 3] += (f32x2){f[2 * i], f[2 * i + 1]};
              const f32x2 s2 = (s4[0] + s4[1]) + (s4[2] + s4[3]);
              s = s2[0] + s2[1]; }
            s += ror8(s); s = sum_g(s);
            const float mw = s * (1.0f / 256.0f);
            f32x2 dv[16], q4[4];
#pragma unroll
            for (int i = 0; i < 16; ++i) dv[i] = (f32x2){f[2 * i], f[2 * i + 1]} - mw;
            SCHED();
#pragma unroll
            for (int i = 0; i < 4; ++i) q4[i] = dv[i] * dv[i];
#pragma unroll
            for (int i = 4; i < 16; ++i) q4[i & 3] = __builtin_elementwise_fma(dv[i], dv[i], q4[i & 3]);
            const f32x2 q2 = (q4[0] + q4[1]) + (q4[2] + q4[3]);
            float q = q2[0] + q2[1];
            q += ror8(q); q = sum_g(q);
            if (lane < 8) red[wc * 8 + lane] = (f32x2){s, q};
        }
        LBAR();
        if (grp == 1) {
            f32x2 p[4];
#pragma unroll
            for (int w = 0; w < 4; ++w) p[w] = red[w * 8 + (lane & 7)];
            const float mean = ((p[0][0] + p[1][0]) + (p[2][0] + p[3][0])) * (1.0f / D_);
            float m2 = (p[0][1] + p[1][1]) + (p[2][1] + p[3][1]);
#pragma unroll
            for (int w = 0; w < 4; ++w) { const float dm = p[w][0] * (1.0f / 256.0f) - mean; m2 += 256.0f * dm * dm; }
            const float var = m2 * (1.0f / D_);
            const float m = rbf(mean), sd = rbf(sqrtf(rbf(rbf(var) + 1e-5f))), rsd = 1.0f / sd;
            unsigned w[16];
#pragma unroll
            for (int i = 0; i < 16; ++i) { const f32x2 d = (f32x2){f[2 * i], f[2 * i + 1]} - m; w[i] = pk_bf16(d[0], d[1]); }
            asm volatile("" : "+v"(w[0]), "+v"(w[1]), "+v"(w[2]), "+v"(w[3]), "+v"(w[4]), "+v"(w[5]), "+v"(w[6]), "+v"(w[7]));
            asm volatile("" : "+v"(w[8]), "+v"(w[9]), "+v"(w[10]), "+v"(w[11]), "+v"(w[12]), "+v"(w[13]), "+v"(w[14]), "+v"(w[15]));
            const unsigned xo = WS_XN + (unsigned)((panel * 32 * 16 + ((row >> 4) & 15)) * 1024 + (row & 15) * 16);
#pragma unroll
            for (int it = 0; it < 4; ++it) {
                const int pc = it * 32 + wc * 8 + (lane >> 3), kc = pc >> 2, kq = pc & 3;
                f32x2 a[4], q0[4];
#pragma unroll
                for (int i = 0; i < 4; ++i) { a[i] = (f32x2){lo_f(w[4 * it + i]), hi_f(w[4 * it + i])}; q0[i] = a[i] * rsd; }
#pragma unroll
                for (int i = 0; i < 4; ++i) a[i] = __builtin_elementwise_fma(-q0[i], (f32x2){sd, sd}, a[i]);
#pragma unroll
                for (int i = 0; i < 4; ++i) q0[i] = __builtin_elementwise_fma(a[i], (f32x2){rsd, rsd}, q0[i]);
                const u32x4 o = {pk_bf16(q0[0][0], q0[0][1]), pk_bf16(q0[1][0], q0[1][1]), pk_bf16(q0[2][0], q0[2][1]), pk_bf16(q0[3][0], q0[3][1])};
                ST_SC1(o, rs, xo + (unsigned)(kc * 16384 + kq * 256));
                SCHED();
            }
            WAIT_V(0);
        }
        LBAR();
        STAMP(2);
        if (threadIdx.x == 256) flag_set(ws, FL_LN + panel * 32 + ks * 8 + c8, tag);
        if (wid == 0) flag_wait(ws, FL_LN + panel * 32, 1, 32, tag, lane);
        LBAR();
        STAMP(3);
        JIT(3);

        cptr xn = uni((cptr)ws + WS_XN + (size_t)panel * 32 * 16384);
        if (grp == 1 && wc < 3) {
            const int p0 = wc == 0 ? 0 : 5 * wc + 1;
            const unsigned av0 = p0 * 1024 + lane * 16, av1 = av0 + 4096;
#pragma unroll
            for (int s3 = 0; s3 < 3; ++s3) {
                const unsigned la = ldsb + s3 * G1_STAGE + p0 * 1024;
                glds_s_sc1_first<0>(av0, xn + s3 * 16384, la); glds_s_sc1_next<1024>(av0, xn + s3 * 16384);
                glds_s_sc1_next<2048>(av0, xn + s3 * 16384);   glds_s_sc1_next<3072>(av0, xn + s3 * 16384);
                glds_s_sc1_first<0>(av1, xn + s3 * 16384, la + 4096);
                if (wc == 0) glds_s_sc1_next<1024>(av1, xn + s3 * 16384);
            }
            if (wc == 0) WAIT_V(12); else WAIT_V(10);
        }
        int ba[5];
        {
            int cgA, tlA, cgB, tlB;
            if (!kvt) { cgA = 1 + (wc >> 1); tlA = 2 * (wc & 1); cgB = 3 + (wc >> 1); tlB = tlA; }
            else if (wc < 2) { cgA = 1; tlA = 2 * wc; cgB = 3; tlB = 2 * wc; }
            else if (wc == 2) { cgA = 2; tlA = 0; cgB = 2; tlB = 2; }
            else { cgA = 4; tlA = 0; cgB = 4; tlB = 2; }
            const int q4 = (lane >> 2) & 3, p = lane & 3, krow = 8 * g + q4, sw = swzB(krow);
            auto adr = [&](int cg, int tl) { return G1_SA + cg * 4096 + krow * 128 + (((2 * tl + (p >> 1)) ^ sw) << 4) + (p & 1) * 8; };
            ba[0] = adr(cgA, tlA); ba[1] = adr(cgA, tlA + 1); ba[2] = adr(cgB, tlB); ba[3] = adr(cgB, tlB + 1); ba[4] = adr(0, wc);
        }
#pragma unroll
        for (int m = 0; m < 8; ++m)
#pragma unroll
            for (int n = 0; n < 5; ++n) acc[m][n] = (f32x4){0.f, 0.f, 0.f, 0.f};
        if (grp == 0) asm volatile("s_waitcnt vmcnt(0)" : "+v"(tlbt), "+v"(codet)::"memory");
        BAR();
        STAMP(4);
        if (grp == 0) { g1_mainloop<0>(ldsb, lds, acc, bp, ld4, xn, wc, lane, ba, (cptr)wd, (cptr)wo); BAR(); }
        else          { BAR(); g1_mainloop<1>(ldsb, lds, acc, bp, ld4, xn, wc, lane, ba, (cptr)wd, (cptr)wo); }
        STAMP(5);
    }

    JIT(5);
    int tz = threadIdx.x; asm volatile("" : "+v"(tz));
    const int lane = tz & 63, g = lane >> 4, l15 = lane & 15;
    const unsigned bvoff = (unsigned)((8 * wc + (lane >> 3)) * 2048 + c8 * 256 + (((lane & 7) ^ swzB(8 * wc + (lane >> 3))) << 4));
    const unsigned bl = ldsb + G2_SB + grp * 24576 + wc * 1024;
    cptr sbp = uni((cptr)wd + ((size_t)(28 * ks + grp) << 16));
    cptr sa = uni((cptr)ws + WS_H + ((size_t)(panel * NKC2 + 28 * ks + grp) << 14));
    const unsigned al = ldsb + grp * 49152 + wc * 4096;
    auto startA = [&](int s2) {
        int tz4 = threadIdx.x; asm volatile("" : "+v"(tz4));
        const unsigned avoff = (unsigned)(wc * 4096 + (tz4 & 63) * 16);
        glds_s_sc1_first<0>(avoff, sa, al + s2 * 16384); glds_s_sc1_next<1024>(avoff, sa); glds_s_sc1_next<2048>(avoff, sa); glds_s_sc1_next<3072>(avoff, sa);
        sa += 32768;
    };
    auto prefetchB = [&]() { g2_issueB(bvoff, sbp, bl); g2_issueB(bvoff, sbp + 131072, bl + 8192); sbp += 262144; };
    {
        const int gq = (j & 1) ? 7 * j + 3 : 7 * j, gp = (j & 1) ? 7 * j + 1 : 7 * j + 4, gh = (j & 1) ? 7 * j : 7 * j + 6;
        {
            lds_t kp = lds + E_KP + (128 * grp + l15) * 256 + ((((wc >> 1) * 4 + (wc & 1) * 2 + (g >> 1)) ^ l15) << 4) + (g & 1) * 8;
#pragma unroll
            for (int m = 0; m < 8; ++m) *(LAS u32x2*)(kp + m * 4096) = (u32x2){pk_bf16(acc[m][4][0], acc[m][4][1]), pk_bf16(acc[m][4][2], acc[m][4][3])};
        }
        const bool has_h = !(kvt && wc == 3);
        const int hkc = !kvt ? gq + wc : (wc < 2 ? gp + wc : gh);
        const unsigned hoff = WS_H + (unsigned)(((panel * NKC2 + hkc) * 16 + 8 * grp) * 1024 + frag_off(lane));
        auto geglu_c = [&](int m) {
            const f32x2 a0 = gelu_mul2(acc[m][0].lo, acc[m][2].lo), a1 = gelu_mul2(acc[m][0].hi, acc[m][2].hi);
            const f32x2 b0 = gelu_mul2(acc[m][1].lo, acc[m][3].lo), b1 = gelu_mul2(acc[m][1].hi, acc[m][3].hi);
            return pack_frag(a0, a1, b0, b1);
        };
        auto geglu = [&](int m) { ST_SC1(geglu_c(m), rs, hoff + m * 1024); SCHED(); };
        const int q4 = (lane >> 2) & 3, p4 = lane & 3;
        auto logits = [&](u32x4 (&lg)[4]) {
            lds_t wp = lds + L_WPHI + (8 * g + q4) * 64 + p4 * 8;
            lds_t qp = lds + E_KP + (32 * wid + l15) * 256;
            bf16x8 wf[2][2], qf[4];
#pragma unroll
            for (int hh = 0; hh < 2; ++hh) {
                wf[hh][0] = lds_tr2(wp + hh * 2048, wp + hh * 2048 + 256); wf[hh][1] = lds_tr2(wp + hh * 2048 + 32, wp + hh * 2048 + 288);
                qf[hh * 2] = lds_b128(qp + (((hh * 4 + g) ^ l15) << 4)); qf[hh * 2 + 1] = lds_b128(qp + 4096 + (((hh * 4 + g) ^ l15) << 4));
            }
            SCHED();
            const f32x4 z4 = {0.f, 0.f, 0.f, 0.f};
            f32x4 p0[4], p1[4];
#pragma unroll
            for (int u = 0; u < 4; ++u) { p0[u] = MFMA(wf[u >> 1][0], qf[u], z4); p1[u] = MFMA(wf[u >> 1][1], qf[u], z4); }
            SCHED();
#pragma unroll
            for (int u = 0; u < 4; ++u) lg[u] = (u32x4){pk_bf16(p0[u][0], p0[u][1]), pk_bf16(p0[u][2], p0[u][3]), pk_bf16(p1[u][0], p1[u][1]), pk_bf16(p1[u][2], p1[u][3])};
            asm volatile("" : "+v"(lg[0]), "+v"(lg[1]), "+v"(lg[2]), "+v"(lg[3]));
        };
        if (kvt) {
            LBAR();
            if (wc == 3) {
                const int sw = swzP(l15);
                lds_t vp = lds + E_V + (128 * grp + l15) * 128 + (g & 1) * 8;
#pragma unroll
                for (int n = 0; n < 4; ++n) {
                    lds_t vn = vp + (((2 * n + (g >> 1)) ^ sw) << 4);
#pragma unroll
                    for (int m = 0; m < 8; ++m) *(LAS u32x2*)(vn + m * 2048) = (u32x2){pk_bf16(acc[m][n][0], acc[m][n][1]), pk_bf16(acc[m][n][2], acc[m][n][3])};
                }
            }
            STAMP(6);
            u32x4 lg[4], P[4], M[4];
            logits(lg);
            WAIT_L0();
            soft_pm<4>(lg, P, M);
            {
                const int sw = swzF(l15);
                lds_t pb = lds + E_KP + (32 * wid + l15) * 256 + (g & 1) * 8;
#pragma unroll
                for (int u = 0; u < 4; ++u) {
                    const int c0 = (u >> 1) * 8 + (g >> 1), tb = u & 1;
                    *(LAS u32x2*)(pb + tb * 4096 + (((c0 + 0) ^ sw) << 4)) = (u32x2){P[u][0], P[u][1]};
                    *(LAS u32x2*)(pb + tb * 4096 + (((c0 + 2) ^ sw) << 4)) = (u32x2){P[u][2], P[u][3]};
                    *(LAS u32x2*)(pb + tb * 4096 + (((c0 + 4) ^ sw) << 4)) = (u32x2){M[u][0], M[u][1]};
                    *(LAS u32x2*)(pb + tb * 4096 + (((c0 + 6) ^ sw) << 4)) = (u32x2){M[u][2], M[u][3]};
                }
            }
            LBAR();
            STAMP(7);
            JIT(7);
            {
                const int hh = wid >> 2, ft = wid & 3;
                f32x4 kv0 = {0.f, 0.f, 0.f, 0.f}, kv1 = kv0, zz = kv0;
                const bf16x8 ones = {0x3F80, 0x3F80, 0x3F80, 0x3F80, 0x3F80, 0x3F80, 0x3F80, 0x3F80};
                const int t0 = 8 * g + q4, t1 = t0 + 4, cp = hh * 8 + 2 * ft + (p4 >> 1), cv = hh * 4 + (p4 >> 1), hb8 = (p4 & 1) * 8;
                lds_t pf0 = lds + E_KP + t0 * 256 + ((cp ^ swzF(t0 & 15)) << 4) + hb8, pf1 = lds + E_KP + t1 * 256 + ((cp ^ swzF(t1 & 15)) << 4) + hb8;
                lds_t va0 = lds + E_V + t0 * 128 + ((cv ^ swzP(t0)) << 4) + hb8, va1 = lds + E_V + t1 * 128 + ((cv ^ swzP(t1)) << 4) + hb8;
                lds_t vb0 = lds + E_V + t0 * 128 + (((cv + 2) ^ swzP(t0)) << 4) + hb8, vb1 = lds + E_V + t1 * 128 + (((cv + 2) ^ swzP(t1)) << 4) + hb8;
#pragma unroll
                for (int k8 = 0; k8 < 8; ++k8) {
                    const bf16x8 pf = lds_tr2(pf0 + k8 * 8192, pf1 + k8 * 8192), v0 = lds_tr2(va0 + k8 * 4096, va1 + k8 * 4096), v1 = lds_tr2(vb0 + k8 * 4096, vb1 + k8 * 4096);
                    kv0 = MFMA(pf, v0, kv0); kv1 = MFMA(pf, v1, kv1); zz = MFMA(pf, ones, zz);
                }
                ST_SC1(((u32x4){pk_bf16(kv0[0], kv0[1]), pk_bf16(kv0[2], kv0[3]), pk_bf16(kv1[0], kv1[1]), pk_bf16(kv1[2], kv1[3])}), rs,
                       WS_PART + (unsigned)(((panel * 32 + 2 * j + hh) * 4 + ft) * 1024 + lane * 16));
                if (l15 == 0) ST_SC1(__builtin_bit_cast(u32x4, zz), rs, WS_PZ + (unsigned)(((panel * 32 + 2 * j + hh) * 64 + 16 * ft + 4 * g) * 4));
            }
            SCHED();
            u32x4 h0 = {0, 0, 0, 0};
            if (has_h) h0 = geglu_c(0);
            WAIT_V(0);
            LBAR();
            if (threadIdx.x == 0) flag_set(ws, FL_KV + j * 8 + panel, tag);
            STAMP(8);
            JIT(9);
            prefetchB();
            if (has_h) {
                ST_SC1(h0, rs, hoff); SCHED();
                geglu(1); geglu(2); geglu(3); geglu(4); geglu(5); geglu(6); geglu(7);
            }
            WAIT_V(0);
            LBAR();
            if (threadIdx.x == 0) flag_set(ws, FL_H + (panel * 4 + ks) * 8 + c8, tag);
            STAMP(9);
        } else {
            u32x4 lg[4], P[4], M[4];
            LBAR();
            logits(lg);
            soft_pm<4>(lg, P, M);
#pragma unroll
            for (int u = 0; u < 4; ++u) asm volatile("" : "+v"(P[u]), "+v"(M[u]));
            SCHED();
            STAMP(6);
            JIT(11);
#pragma unroll
            for (int m = 0; m < 8; ++m) geglu(m);
            prefetchB();
            WAIT_V(4);
            LBAR();
            if (threadIdx.x == 0) flag_set(ws, FL_H + (panel * 4 + ks) * 8 + c8, tag);
            STAMP(7);
            if (wid == 0) flag_wait(ws, FL_KV + j * 8, 1, 8, tag, lane);
            if (blockIdx.x == 0 && wid == 1) {
                GAS u64* p = flagp(ws, FL_LN + lane);
#pragma unroll 1
                for (unsigned it = 0; it < (1u << 21); ++it) {
                    const u64 v0 = __hip_atomic_load(p, __ATOMIC_RELAXED, __HIP_MEMORY_SCOPE_AGENT), v1 = __hip_atomic_load(p + 64 * 8, __ATOMIC_RELAXED, __HIP_MEMORY_SCOPE_AGENT);
                    const u64 v2 = __hip_atomic_load(p + 128 * 8, __ATOMIC_RELAXED, __HIP_MEMORY_SCOPE_AGENT), v3 = __hip_atomic_load(p + 192 * 8, __ATOMIC_RELAXED, __HIP_MEMORY_SCOPE_AGENT);
                    if (__all(v0 == tag && v1 == tag && v2 == tag && v3 == tag)) { if (lane == 0) flag_set(ws, FL_EPOCH, tag); break; }
                    __builtin_amdgcn_s_sleep(1);
                }
            }
            LBAR();
            STAMP(8);
            u32x2 hfl = {0, 0};
            {
                const int hh = wid >> 2, ft = wid & 3;
                asm volatile("global_load_dwordx2 %0, %1, %2 sc1" : "+v"(hfl) : "v"((FL_H + (panel * 4 + ks) * 8 + (lane & 7)) * 64), "s"(ws + WS_FLG) : "memory");
                f32x4 a0, a1, az;
                u32x4 pw[8]; f32x4 pz[8];
#pragma unroll
                for (int p = 0; p < 8; ++p) {
                    pw[p] = LD_SC1(rs, WS_PART + (unsigned)(((p * 32 + 2 * j + hh) * 4 + ft) * 1024 + lane * 16));
                    pz[p] = __builtin_bit_cast(f32x4, LD_SC1(rs, WS_PZ + (unsigned)(((p * 32 + 2 * j + hh) * 64 + 16 * ft + 4 * g) * 4)));
                }
#pragma unroll
                for (int p = 0; p < 8; ++p) {
                    const f32x4 b0 = {lo_f(pw[p][0]), hi_f(pw[p][0]), lo_f(pw[p][1]), hi_f(pw[p][1])}, b1 = {lo_f(pw[p][2]), hi_f(pw[p][2]), lo_f(pw[p][3]), hi_f(pw[p][3])};
                    if (p == 0) { a0 = b0; a1 = b1; az = pz[0]; } else { a0 += b0; a1 += b1; az += pz[p]; }
                }
                lds_t kb = lds + L_KVL + ((hh * 2 + (ft >> 1)) * 3) * 1024 + lane * 16 + (ft & 1) * 8;
                *(LAS u32x2*)(kb) = (u32x2){pk_bf16(a0[0], a0[1]), pk_bf16(a0[2], a0[3])};
                *(LAS u32x2*)(kb + 1024) = (u32x2){pk_bf16(a1[0], a1[1]), pk_bf16(a1[2], a1[3])};
                *(LAS u32x2*)(kb + 2048) = (u32x2){pk_bf16(az[0], az[1]), pk_bf16(az[2], az[3])};
            }
            LBAR();
            STAMP(9);
            JIT(13);
            {
                asm volatile("" : "+v"(hfl));
                const u64 v = ((u64)hfl[1] << 32) | hfl[0];
                if (!__all(v == tag)) flag_wait(ws, FL_H + (panel * 4 + ks) * 8, 1, 8, tag, lane);
                startA(0); startA(1);
            }
#pragma unroll
            for (int hh = 0; hh < 2; ++hh) {
                bf16x8 kf[2][3];
#pragma unroll
                for (int s = 0; s < 2; ++s)
#pragma unroll
                    for (int d = 0; d < 3; ++d) kf[s][d] = lds_b128(lds + L_KVL + ((hh * 2 + s) * 3 + d) * 1024 + lane * 16);
#pragma unroll
                for (int tb = 0; tb < 2; ++tb) {
                    const bf16x8 Pf = __builtin_bit_cast(bf16x8, P[hh * 2 + tb]), Mf = __builtin_bit_cast(bf16x8, M[hh * 2 + tb]);
                    const f32x4 z4 = {0.f, 0.f, 0.f, 0.f};
                    const f32x4 n0 = MFMA(kf[1][0], Mf, MFMA(kf[0][0], Pf, z4));
                    const f32x4 n1 = MFMA(kf[1][1], Mf, MFMA(kf[0][1], Pf, z4));
                    const f32x4 dn = MFMA(kf[1][2], Mf, MFMA(kf[0][2], Pf, z4));
                    const float rden = __builtin_amdgcn_rcpf(rbf(rbf(dn[0]) + 1e-6f));
                    ST_SC1(pack_frag(n0.lo * rden, n0.hi * rden, n1.lo * rden, n1.hi * rden), rs, WS_H + (unsigned)(((panel * NKC2 + 112 + 2 * j + hh) * 16 + 2 * wid + tb) * 1024 + frag_off(lane)));
                }
            }
        }
    }
    STAMP(10);
    STAMP_OUT(0, 0, 11);

    STAMP(0);
    if (kvt) {
        if (wid == 0) flag_wait(ws, FL_H + (panel * 4 + ks) * 8, 1, 8, tag, lane);
        LBAR();
        startA(0); startA(1);
    }
    STAMP(1);
    f32x4 c2[8][4];
    {
        int tz3 = threadIdx.x; asm volatile("" : "+v"(tz3));
        const int lane = tz3 & 63, g = lane >> 4;
        const unsigned avoff = (unsigned)(wc * 4096 + lane * 16);
        const unsigned bvoff = (unsigned)((8 * wc + (lane >> 3)) * 2048 + c8 * 256 + (((lane & 7) ^ swzB(8 * wc + (lane >> 3))) << 4));
        const int wm = wc >> 1, wn = wc & 1;
        unsigned bb[4];
        {
            const int q4 = (lane >> 2) & 3, p = lane & 3, kr = 8 * g + q4, sw = swzB(kr);
#pragma unroll
            for (int tl = 0; tl < 4; ++tl) bb[tl] = ldsb + G2_SB + grp * 24576 + wn * 4096 + kr * 128 + (((2 * tl + (p >> 1)) ^ sw) << 4) + (p & 1) * 8;
        }
        lds_t ap = lds + grp * 49152 + wm * 8192 + lane * 16;
        u32x2 ofl = {0, 0};
        const unsigned fo = (FL_O + (panel * 4 + ks) * 4 + (lane & 3)) * 64;
        if (kvt) WAIT_V(4); else WAIT_V(8);
        LBAR();
        if (grp == 1) BAR();
        STAMP(2);
        auto step = [&](auto uc, auto fc, const int t) {
            constexpr int U = decltype(uc)::value, W = (U + 2) % 3;
            constexpr bool FIRST = decltype(fc)::value;
            bf16x8 A[8], B[4];
#define FST(x)
            FST(f0);
            if (U == 0) {
                if (t == 12) {
                    asm volatile("" : "+v"(ofl));
                    const u64 v = ((u64)ofl[1] << 32) | ofl[0];
                    if (!__all(v == tag)) flag_wait(ws, FL_O + (panel * 4 + ks) * 4, 1, 4, tag, lane);
                    sa = uni((cptr)ws + WS_H + ((size_t)(panel * NKC2 + 112 + 8 * ks + grp) << 14));
                    sbp = uni((cptr)wo + ((size_t)(8 * ks + grp) << 16));
                }
                asm volatile("s_cmp_lg_u32 %3, 9\n\ts_cbranch_scc1 .Lofl_%=\n\tglobal_load_dwordx2 %0, %1, %2 sc1\n.Lofl_%=:" : "+v"(ofl) : "v"(fo), "s"(ws + WS_FLG), "s"(t) : "scc", "memory");
            }
            if (U == 2 && t == 2 && !kvt && threadIdx.x == 0) flag_set(ws, FL_O + (panel * 4 + ks) * 4 + (c8 >> 1), tag);
            const bool more = U == 0 || t < 16;
            B[0] = lds_tr2_asm<U * 8192>(bb[0]); B[1] = lds_tr2_asm<U * 8192>(bb[1]);
            if (more) glds_s(bvoff, sbp, bl + W * 8192);
            B[2] = lds_tr2_asm<U * 8192>(bb[2]); B[3] = lds_tr2_asm<U * 8192>(bb[3]);
            if (more) { glds_s_o128(bvoff, sbp, bl + W * 8192 + 4096); sbp += 131072; }
            A[0] = lds_b128(ap + U * 16384); A[1] = lds_b128(ap + U * 16384 + 1024);
            if (more) { glds_s_sc1_first<0>(avoff, sa, al + W * 16384); glds_s_sc1_next<1024>(avoff, sa); }
            A[2] = lds_b128(ap + U * 16384 + 2048); A[3] = lds_b128(ap + U * 16384 + 3072); A[4] = lds_b128(ap + U * 16384 + 4096);
            if (more) { glds_s_sc1_next<2048>(avoff, sa); glds_s_sc1_next<3072>(avoff, sa); sa += 32768; }
            A[5] = lds_b128(ap + U * 16384 + 5120); A[6] = lds_b128(ap + U * 16384 + 6144); A[7] = lds_b128(ap + U * 16384 + 7168);
            FST(f1);
            WAIT_L0();
            FST(f2);
            if (!more) WAIT_V(0);
            SCHED(); BAR(); SCHED();
            FST(f3);
            __builtin_amdgcn_s_setprio(1);
    #pragma unroll
            for (int m = 0; m < 8; ++m)
#pragma unroll
                for (int n = 0; n < 4; ++n) { const int nn = (m & 1) ? 3 - n : n; if (FIRST) MFMA_SET(c2[m][nn], B[nn], A[m]); else MFMA_ACC(c2[m][nn], B[nn], A[m]); }
            __builtin_amdgcn_s_setprio(0);
            FST(f4);
            WAIT_V(6);
            FST(f5);
            SCHED(); BAR(); SCHED();
        };
        step(std::integral_constant<int, 0>{}, std::true_type{}, 0);
#pragma unroll 1
        for (int t = 1; t < 18; t += 3) {
            step(std::integral_constant<int, 1>{}, std::false_type{}, t); step(std::integral_constant<int, 2>{}, std::false_type{}, t + 1);
            if (t < 16) step(std::integral_constant<int, 0>{}, std::false_type{}, t + 2);
        }
        if (grp == 0) BAR();
    }
    STAMP(3);
    {
        int tz2 = threadIdx.x; asm volatile("" : "+v"(tz2));
        const int ln = tz2 & 63, tile = panel * 8 + c8, wm = wc >> 1, wn = wc & 1;
        const int qk = 2 * wm + grp, qs = qk ^ 1;
        const bool publisher = qk != ks;
        const int mh = wid >> 2, nq = wid & 3;
        const int row0 = panel * 256 + ks * 64 + mh * 32 + (ln & 15), col0 = c8 * 128 + nq * 32 + 8 * frag_kq(ln);
        constexpr int L_OWN = 0, L_SEND = 65536;
        u32x4 xr0 = {0, 0, 0, 0}, xr1 = xr0;
        {
            const unsigned xo = (unsigned)(row0 * D_ + col0) * 2u;
            asm volatile("global_load_dwordx4 %0, %1, %2" : "+v"(xr0) : "v"(xo), "s"(x) : "memory");
            asm volatile("global_load_dwordx4 %0, %1, %2" : "+v"(xr1) : "v"(xo + 16 * D_ * 2), "s"(x) : "memory");
        }
        f32x4 hk[4][4], hs[4][4];
#pragma unroll
        for (int m = 0; m < 4; ++m)
#pragma unroll
            for (int n = 0; n < 4; ++n) { hk[m][n] = grp ? c2[4 + m][n] : c2[m][n]; hs[m][n] = grp ? c2[m][n] : c2[4 + m][n]; }
        auto dump_own = [&](const f32x4 (&v)[4][4]) {
#pragma unroll
            for (int m = 0; m < 4; ++m)
#pragma unroll
                for (int n = 0; n < 4; ++n) *(LAS f32x4*)(lds + L_OWN + ((grp * 4 + m) * 8 + 4 * wn + n) * 1024 + ln * 16) = v[m][n];
        };
        if (qs == ks) dump_own(hs);
        else {
#pragma unroll
            for (int m = 0; m < 4; ++m)
#pragma unroll
                for (int n2 = 0; n2 < 2; ++n2) {
                    const f32x4 a = hs[m][2 * n2], b = hs[m][2 * n2 + 1];
                    *(LAS u32x4*)(lds + L_SEND + (wid ^ 4) * 8192 + (m * 2 + n2) * 1024 + ln * 16) = (u32x4){pk_bf16(a[0], a[1]), pk_bf16(a[2], a[3]), pk_bf16(b[0], b[1]), pk_bf16(b[2], b[3])};
                }
        }
        if (qk == ks) dump_own(hk);
        LBAR();
        if (publisher) {
            const unsigned xbase = WS_XCH + (unsigned)((((tile * 4 + ks) * 8 + wid) * 8) * 1024 + ln * 16);
#pragma unroll
            for (int m = 0; m < 4; ++m)
#pragma unroll
                for (int n2 = 0; n2 < 2; ++n2) {
                    const u32x4 w = *(const LAS u32x4*)(lds + L_SEND + wid * 8192 + (m * 2 + n2) * 1024 + ln * 16);
                    const f32x4 a = hk[m][2 * n2] + (f32x4){lo_f(w[0]), hi_f(w[0]), lo_f(w[1]), hi_f(w[1])}, b = hk[m][2 * n2 + 1] + (f32x4){lo_f(w[2]), hi_f(w[2]), lo_f(w[3]), hi_f(w[3])};
                    ST_SC1(((u32x4){pk_bf16(a[0], a[1]), pk_bf16(a[2], a[3]), pk_bf16(b[0], b[1]), pk_bf16(b[2], b[3])}), rs, xbase + (m * 2 + n2) * 1024);
                }
        }
        f32x4 e[2][2];
#pragma unroll
        for (int a = 0; a < 2; ++a)
#pragma unroll
            for (int b = 0; b < 2; ++b)
                e[a][b] = *(const LAS f32x4*)(lds + L_OWN + ((2 * mh + a) * 8 + 2 * nq + b) * 1024 + ln * 16) + *(const LAS f32x4*)(lds + L_OWN + ((4 + 2 * mh + a) * 8 + 2 * nq + b) * 1024 + ln * 16);
        STAMP(4);
        asm volatile("s_waitcnt vmcnt(0)" : "+v"(xr0), "+v"(xr1)::"memory");
        if (publisher && ln == 0) flag_set(ws, FL_X + (tile * 4 + ks) * 8 + wid, tag);
        STAMP(5);
        JIT(19);
        const int wsrc = (ks & 1) * 4 + (ks >> 1) * 2 + (nq >> 1);
        {
            GAS u64* p = flagp(ws, FL_X + (tile * 4 + ((ks + 1 + (ln < 3 ? ln : 0)) & 3)) * 8 + wsrc);
#pragma unroll 1
            for (unsigned it = 0; it < (1u << 21); ++it) {
                const u64 v = __hip_atomic_load(p, __ATOMIC_RELAXED, __HIP_MEMORY_SCOPE_AGENT);
                if (__all(v == tag)) break;
                __builtin_amdgcn_s_sleep(1);
            }
            asm volatile("" ::: "memory");
        }
        u32x4 pw[3][2];
#pragma unroll
        for (int i = 0; i < 3; ++i) {
            const unsigned base = WS_XCH + (unsigned)((((tile * 4 + i + (i >= ks ? 1 : 0)) * 8 + wsrc) * 8 + (nq & 1) + 4 * mh) * 1024 + ln * 16);
            pw[i][0] = LD_SC1(rs, base); pw[i][1] = LD_SC1(rs, base + 2048);
        }
#pragma unroll
        for (int i = 0; i < 3; ++i)
#pragma unroll
            for (int a = 0; a < 2; ++a) {
                const u32x4 w = pw[i][a];
                e[a][0] += (f32x4){lo_f(w[0]), hi_f(w[0]), lo_f(w[1]), hi_f(w[1])};
                e[a][1] += (f32x4){lo_f(w[2]), hi_f(w[2]), lo_f(w[3]), hi_f(w[3])};
            }
        const __amdgpu_buffer_rsrc_t ors = __builtin_amdgcn_make_buffer_rsrc((void*)out, 0, S_ * D_ * 2, 0x00020000);
#pragma unroll
        for (int a = 0; a < 2; ++a) {
            float u[4], v[4];
#pragma unroll
            for (int q = 0; q < 4; ++q) {
                auto rr = __builtin_amdgcn_permlane16_swap(__float_as_uint(e[a][0][q]), __float_as_uint(e[a][1][q]), false, false);
                u[q] = __uint_as_float(rr[0]); v[q] = __uint_as_float(rr[1]);
            }
            const u32x4 xv = a ? xr1 : xr0;
            u32x4 o;
            o[0] = pk_bf16(u[0] + lo_f(xv[0]), u[1] + hi_f(xv[0])); o[1] = pk_bf16(u[2] + lo_f(xv[1]), u[3] + hi_f(xv[1]));
            o[2] = pk_bf16(v[0] + lo_f(xv[2]), v[1] + hi_f(xv[2])); o[3] = pk_bf16(v[2] + lo_f(xv[3]), v[3] + hi_f(xv[3]));
            __builtin_amdgcn_raw_buffer_store_b128(o, ors, (unsigned)(((row0 + 16 * a) * D_ + col0) * 2), 0, 16);
        }
    }
    STAMP(6);
    {
        const int ln_ = threadIdx.x & 63;
        u64 m_ = PAD_GLOBAL == 1 ? ~0ull : 0ull;
        for (int i = 0; i < 4; ++i) { const u64 v_ = __hip_atomic_load((GAS u64*)(ws + ((size_t)100 << 20) + (i * 64 + ln_) * 64), __ATOMIC_RELAXED, __HIP_MEMORY_SCOPE_AGENT);
            m_ = PAD_GLOBAL == 1 ? (v_ < m_ ? v_ : m_) : (v_ > m_ ? v_ : m_); }
        for (int o = 32; o >= 1; o >>= 1) { const u64 v_ = __shfl_xor(m_, o); m_ = PAD_GLOBAL == 1 ? (v_ < m_ ? v_ : m_) : (v_ > m_ ? v_ : m_); }
        PAD_FROM(m_, 10000);
    }
    STAMP_OUT(1, 0, 7);
}

extern "C" void kernel_launch(void* const* d_in, const int* in_sizes, int n_in, void* d_out, int out_size, void* d_ws, size_t ws_size, hipStream_t stream) {
    const u16* x = (const u16*)d_in[0]; const u16* wqkv = (const u16*)d_in[1]; const u16* wphi = (const u16*)d_in[2]; const u16* wo = (const u16*)d_in[3];
    const u16* wg = (const u16*)d_in[5]; const u16* wu = (const u16*)d_in[6]; const u16* wd = (const u16*)d_in[7];
    (void)hipFuncSetAttribute((const void*)k_fused, hipFuncAttributeMaxDynamicSharedMemorySize, L_TOTAL);
    k_fused<<<256, 512, L_TOTAL, stream>>>(x, wqkv, wphi, wo, wg, wu, wd, (unsigned char*)d_ws, (u16*)d_out);
}
```

```cpp
#define PAD_DIAG
#define PAD_END 10000
#include <hip/hip_runtime.h>
#include <type_traits>

typedef unsigned short u16;
typedef unsigned long long u64;
typedef short bf16x8 __attribute__((ext_vector_type(8)));
typedef short s16x4 __attribute__((ext_vector_type(4)));
typedef float f32x4 __attribute__((ext_vector_type(4)));
typedef float f32x2 __attribute__((ext_vector_type(2)));
typedef unsigned u32x4 __attribute__((ext_vector_type(4)));
typedef unsigned u32x2 __attribute__((ext_vector_type(2)));
#define LAS __attribute__((address_space(3)))
#define GAS __attribute__((address_space(1)))
typedef LAS unsigned char* lds_t;
typedef const unsigned char* cptr;

constexpr int S_ = 2048, D_ = 1024, F_ = 3584;
constexpr int CODE_LINES = 256;
constexpr int NKC2 = 144;

constexpr unsigned WS_XN   = 0;
constexpr unsigned WS_H    = WS_XN + S_ * D_ * 2;
constexpr unsigned WS_PART = WS_H + 8 * NKC2 * 16 * 1024;
constexpr unsigned WS_PZ   = WS_PART + 8 * 32 * 4 * 1024;
constexpr unsigned WS_XCH  = WS_PZ + 8 * 32 * 64 * 4;
constexpr unsigned WS_FLG  = WS_XCH + 64 * 4 * 8 * 8 * 1024;
constexpr unsigned FL_LN = 0, FL_KV = FL_LN + 256, FL_H = FL_KV + 128, FL_O = FL_H + 256, FL_X = FL_O + 128, FL_EPOCH = FL_X + 2048, FL_N = FL_EPOCH + 1;
constexpr unsigned WS_END  = WS_FLG + FL_N * 64;

constexpr int G1_SA = 16384, G1_STAGE = 36864;
constexpr int L_MISC = 158720, L_WPHI = 159744, L_TOTAL = 163840;
constexpr int L_KVL = 147456;
constexpr int E_KP = 0, E_V = 65536;
constexpr int G2_SB = 98304;

__device__ __forceinline__ unsigned pk_bf16(float lo, float hi) {
    typedef __bf16 bf2 __attribute__((ext_vector_type(2)));
    bf2 r = {(__bf16)lo, (__bf16)hi};
    return __builtin_bit_cast(unsigned, r);
}
__device__ __forceinline__ float rbf(float x) { return __uint_as_float(pk_bf16(x, x) & 0xffff0000u); }
__device__ __forceinline__ float lo_f(unsigned w) { return __uint_as_float(w << 16); }
__device__ __forceinline__ float hi_f(unsigned w) { return __uint_as_float(w & 0xffff0000u); }

#define STAMP_DECL
#define STAMP(i)
#define STAMP_ARG
#define STAMP_PASS
#define STAMP_OUT(kern, lo, n)

#define XW_CNT "18"
#define JIT(k)
#define WAIT_V(n) asm volatile("s_waitcnt vmcnt(" #n ")" ::: "memory")
#define WAIT_L0() do { asm volatile("" ::: "memory"); __builtin_amdgcn_s_waitcnt(0xC07F); asm volatile("" ::: "memory"); } while (0)
#define BAR() __builtin_amdgcn_s_barrier()
#define LBAR() asm volatile("s_waitcnt lgkmcnt(0)\n\ts_barrier" ::: "memory")
#define SCHED() __builtin_amdgcn_sched_barrier(0)
#define MFMA(a, b, c) __builtin_amdgcn_mfma_f32_16x16x32_bf16(a, b, c, 0, 0, 0)
#define MFMA_SET(c, a, b) asm volatile("v_mfma_f32_16x16x32_bf16 %0, %1, %2, 0" : "=&v"(c) : "v"(a), "v"(b))
#define MFMA_ACC(c, a, b) asm volatile("v_mfma_f32_16x16x32_bf16 %0, %1, %2, %0" : "+v"(c) : "v"(a), "v"(b))

__device__ __forceinline__ cptr uni(cptr p) {
    const u64 v = (u64)p;
    return (cptr)(((u64)(unsigned)__builtin_amdgcn_readfirstlane((unsigned)(v >> 32)) << 32) | (unsigned)__builtin_amdgcn_readfirstlane((unsigned)v));
}
__device__ __forceinline__ void glds_v(cptr g, unsigned l) {
    asm volatile("s_mov_b32 m0, %1\n\ts_nop 0\n\tglobal_load_lds_dwordx4 %0, off" ::"v"(g), "s"(l) : "memory");
}
__device__ __forceinline__ void glds_s(unsigned voff, cptr sb, unsigned l) {
    asm volatile("s_nop 2\n\ts_mov_b32 m0, %2\n\ts_nop 0\n\tglobal_load_lds_dwordx4 %0, %1" ::"v"(voff), "s"(sb), "s"(l) : "memory");
}
__device__ __forceinline__ void glds_s_o128(unsigned voff, cptr sb, unsigned l) {
    asm volatile("s_mov_b32 m0, %2\n\ts_nop 0\n\tglobal_load_lds_dwordx4 %0, %1 offset:128" ::"v"(voff), "s"(sb), "s"(l - 128u) : "memory");
}
template <int OFF> __device__ __forceinline__ void glds_s_sc1_first(unsigned voff, cptr sb, unsigned l) {
    asm volatile("s_nop 2\n\ts_mov_b32 m0, %2\n\ts_nop 0\n\tglobal_load_lds_dwordx4 %0, %1 offset:%3 sc1" ::"v"(voff), "s"(sb), "s"(l), "i"(OFF) : "memory");
}
template <int OFF> __device__ __forceinline__ void glds_s_sc1_next(unsigned voff, cptr sb) {
    asm volatile("global_load_lds_dwordx4 %0, %1 offset:%2 sc1" ::"v"(voff), "s"(sb), "i"(OFF) : "memory");
}
template <int OFF> __device__ __forceinline__ void gld_sc1(u32x4& d, unsigned voff, cptr sb) {
    asm volatile("global_load_dwordx4 %0, %1, %2 offset:%3 sc1" : "+v"(d) : "v"(voff), "s"(sb), "i"(OFF) : "memory");
}
__device__ __forceinline__ bf16x8 lds_b128(lds_t p) { return *(const LAS bf16x8*)p; }
__device__ __forceinline__ bf16x8 lds_tr2(lds_t p0, lds_t p1) {
    s16x4 a = __builtin_amdgcn_ds_read_tr16_b64_v4i16((LAS s16x4*)p0);
    s16x4 b = __builtin_amdgcn_ds_read_tr16_b64_v4i16((LAS s16x4*)p1);
    return __builtin_shufflevector(a, b, 0, 1, 2, 3, 4, 5, 6, 7);
}
template <int OFF> __device__ __forceinline__ bf16x8 lds_tr2_asm(unsigned addr) {
    s16x4 a, b;
    asm volatile("ds_read_b64_tr_b16 %0, %2 offset:%3\n\tds_read_b64_tr_b16 %1, %2 offset:%4" : "=&v"(a), "=&v"(b) : "v"(addr), "i"(OFF), "i"(OFF + 512) : "memory");
    return __builtin_shufflevector(a, b, 0, 1, 2, 3, 4, 5, 6, 7);
}

__device__ __forceinline__ float x16(float v, float& o) { auto r = __builtin_amdgcn_permlane16_swap(__float_as_uint(v), __float_as_uint(v), false, false); o = __uint_as_float(r[1]); return __uint_as_float(r[0]); }
__device__ __forceinline__ float x32(float v, float& o) { auto r = __builtin_amdgcn_permlane32_swap(__float_as_uint(v), __float_as_uint(v), false, false); o = __uint_as_float(r[1]); return __uint_as_float(r[0]); }
__device__ __forceinline__ float sum_g(float v) { float o, a = x16(v, o); v = a + o; a = x32(v, o); return a + o; }
__device__ __forceinline__ float max_g(float v) { float o, a = x16(v, o); v = fmaxf(a, o); a = x32(v, o); return fmaxf(a, o); }
__device__ __forceinline__ float min_g(float v) { float o, a = x16(v, o); v = fminf(a, o); a = x32(v, o); return fminf(a, o); }
__device__ __forceinline__ float ror8(float v) { return __int_as_float(__builtin_amdgcn_update_dpp(0, __float_as_int(v), 0x128, 0xf, 0xf, false)); }

__device__ __forceinline__ int swzB(int k) { return (((k >> 1) & 1) << 1) | (((k >> 3) & 1) << 2); }
__device__ __forceinline__ int swzP(int k) { return ((k >> 2) & 1) | (((k >> 1) & 1) << 1) | (((k >> 3) & 1) << 2); }
__device__ __forceinline__ int swzF(int k) { return ((k >> 2) & 1) | ((k & 3) << 1) | (((k >> 3) & 1) << 3); }

__device__ __forceinline__ f32x2 gelu_mul2(f32x2 g, f32x2 u) {
    constexpr float C0 = -2.0f * 0.7978845608028654f * 1.4426950408889634f, C1 = C0 * 0.044715f;
    const f32x2 a = g * (g * g * C1 + C0);
    const f32x2 d = (f32x2){__builtin_amdgcn_exp2f(a[0]), __builtin_amdgcn_exp2f(a[1])} + 1.0f;
    return (g * u) * (f32x2){__builtin_amdgcn_rcpf(d[0]), __builtin_amdgcn_rcpf(d[1])};
}
__device__ __forceinline__ u32x4 pack_frag(const f32x2 a0, const f32x2 a1, const f32x2 b0, const f32x2 b1) {
    unsigned x0 = pk_bf16(a0[0], a0[1]), x1 = pk_bf16(a1[0], a1[1]), y0 = pk_bf16(b0[0], b0[1]), y1 = pk_bf16(b1[0], b1[1]);
    auto r0 = __builtin_amdgcn_permlane16_swap(x0, y0, false, false);
    auto r1 = __builtin_amdgcn_permlane16_swap(x1, y1, false, false);
    u32x4 o; o[0] = r0[0]; o[1] = r1[0]; o[2] = r0[1]; o[3] = r1[1];
    return o;
}
__device__ __forceinline__ int frag_kq(int lane) { const int g = lane >> 4; return ((g & 1) << 1) | (g >> 1); }
__device__ __forceinline__ int frag_off(int lane) { return (frag_kq(lane) * 16 + (lane & 15)) * 16; }

__device__ __forceinline__ float max3(float a, float b, float c) { float r; asm("v_max3_f32 %0, %1, %2, %3" : "=v"(r) : "v"(a), "v"(b), "v"(c)); return r; }
__device__ __forceinline__ float min3(float a, float b, float c) { float r; asm("v_min3_f32 %0, %1, %2, %3" : "=v"(r) : "v"(a), "v"(b), "v"(c)); return r; }
__device__ __forceinline__ float max2(float a, float b) { float r; asm("v_max_f32 %0, %1, %2" : "=v"(r) : "v"(a), "v"(b)); return r; }
template <int NU, class Op> __device__ __forceinline__ void red2_g(float (&a)[NU], float (&b)[NU], Op op) {
    float z[NU], y[NU];
#pragma unroll
    for (int u = 0; u < NU; ++u) {
        auto r = __builtin_amdgcn_permlane16_swap(__float_as_uint(a[u]), __float_as_uint(b[u]), false, false);
        z[u] = op(__uint_as_float(r[0]), __uint_as_float(r[1]));
    }
#pragma unroll
    for (int u = 0; u < NU; ++u) {
        auto q = __builtin_amdgcn_permlane32_swap(__float_as_uint(z[u]), __float_as_uint(z[u]), false, false);
        y[u] = op(__uint_as_float(q[0]), __uint_as_float(q[1]));
    }
#pragma unroll
    for (int u = 0; u < NU; ++u) {
        auto w = __builtin_amdgcn_permlane16_swap(__float_as_uint(y[u]), __float_as_uint(y[u]), false, false);
        a[u] = __uint_as_float(w[0]); b[u] = __uint_as_float(w[1]);
    }
}
template <int NU> __device__ __forceinline__ void soft_pm(const u32x4 (&w)[NU], u32x4 (&P)[NU], u32x4 (&M)[NU]) {
    constexpr float L2E = 1.4426950408889634f;
    f32x2 pv[NU][4]; float mx[NU], nm[NU];
#pragma unroll
    for (int u = 0; u < NU; ++u) {
#pragma unroll
        for (int i = 0; i < 4; ++i) pv[u][i] = (f32x2){lo_f(w[u][i]), hi_f(w[u][i])};
        mx[u] = max3(max3(max3(pv[u][0][0], pv[u][0][1], pv[u][1][0]), pv[u][1][1], pv[u][2][0]), max2(pv[u][2][1], pv[u][3][0]), pv[u][3][1]);
        nm[u] = -min3(min3(min3(pv[u][0][0], pv[u][0][1], pv[u][1][0]), pv[u][1][1], pv[u][2][0]), min3(pv[u][2][1], pv[u][3][0], pv[u][3][1]), pv[u][3][1]);
    }
    red2_g<NU>(mx, nm, [](float a, float b) { return max2(a, b); });
    f32x2 pp[NU][4], pm[NU][4]; float sp[NU], sm[NU];
#pragma unroll
    for (int u = 0; u < NU; ++u) {
        const float cp = -mx[u] * L2E, cm = -nm[u] * L2E;
#pragma unroll
        for (int i = 0; i < 4; ++i) {
            const f32x2 a = pv[u][i] * L2E + cp, b = pv[u][i] * (-L2E) + cm;
            pp[u][i] = (f32x2){__builtin_amdgcn_exp2f(a[0]), __builtin_amdgcn_exp2f(a[1])};
            pm[u][i] = (f32x2){__builtin_amdgcn_exp2f(b[0]), __builtin_amdgcn_exp2f(b[1])};
        }
        const f32x2 s2 = (pp[u][0] + pp[u][1]) + (pp[u][2] + pp[u][3]), m2 = (pm[u][0] + pm[u][1]) + (pm[u][2] + pm[u][3]);
        sp[u] = s2[0] + s2[1]; sm[u] = m2[0] + m2[1];
    }
    red2_g<NU>(sp, sm, [](float a, float b) { return a + b; });
#pragma unroll
    for (int u = 0; u < NU; ++u) {
        const float rp = __builtin_amdgcn_rcpf(sp[u]), rm = __builtin_amdgcn_rcpf(sm[u]);
#pragma unroll
        for (int i = 0; i < 4; ++i) { const f32x2 a = pp[u][i] * rp, b = pm[u][i] * rm; P[u][i] = pk_bf16(a[0], a[1]); M[u][i] = pk_bf16(b[0], b[1]); }
    }
}

__device__ __forceinline__ GAS u64* flagp(unsigned char* ws, unsigned i) { return (GAS u64*)(ws + WS_FLG + (size_t)i * 64); }
__device__ __forceinline__ void flag_set(unsigned char* ws, unsigned i, u64 tag) { __hip_atomic_store(flagp(ws, i), tag, __ATOMIC_RELAXED, __HIP_MEMORY_SCOPE_AGENT); }
__device__ __forceinline__ void flag_wait(unsigned char* ws, unsigned i0, unsigned st, int n, u64 tag, int lane) {
    GAS u64* p = flagp(ws, i0 + st * (unsigned)(lane < n ? lane : 0));
#pragma unroll 1
    for (unsigned it = 0; it < (1u << 21); ++it) {
        const u64 v = __hip_atomic_load(p, __ATOMIC_RELAXED, __HIP_MEMORY_SCOPE_AGENT);
        if (__all(v == tag)) break;
        __builtin_amdgcn_s_sleep(1);
    }
    asm volatile("" ::: "memory");
}
__device__ __forceinline__ __amdgpu_buffer_rsrc_t ws_rsrc(unsigned char* ws) { return __builtin_amdgcn_make_buffer_rsrc((void*)ws, 0, WS_END, 0x00020000); }
#define ST_SC1(v, rs, off) __builtin_amdgcn_raw_buffer_store_b128(v, rs, off, 0, 16)
#define LD_SC1(rs, off) __builtin_amdgcn_raw_buffer_load_b128(rs, off, 0, 16)

template <int GRP>
__device__ __forceinline__ void g1_mainloop(const unsigned ldsb, lds_t lds, f32x4 (&acc)[8][5], cptr (&bp)[5], const size_t ld4, cptr xn, const int wl, const int lane, const int (&ba)[5],
                                            cptr wd, cptr wo) {
    const size_t ldq = (size_t)32 * 3072 * 2, ldg = (size_t)32 * F_ * 2;
    const int aoff = GRP * 8192 + lane * 16;
    const int p0 = wl == 0 ? 0 : 5 * wl + 1;
    const unsigned av0 = p0 * 1024 + lane * 16, av1 = av0 + 4096;
    int so = 0, sl = 3 * G1_STAGE;
    cptr an = xn + 3 * 16384;
    unsigned touch = 0;
    cptr tp = wd + (size_t)(blockIdx.x * 224 + (lane & 15)) * 128;
    bf16x8 A[8], B[5];
#pragma unroll 1
    for (int t = 0; t < 32; ++t) {
        const bool more = t < 29;
        const unsigned lb = ldsb + sl + G1_SA + wl * 1024, la = ldsb + sl + p0 * 1024, rb = ldsb + so;
        if (more) {
            if (GRP == 0) {
                B[0] = lds_tr2_asm<0>(rb + ba[0]); B[1] = lds_tr2_asm<0>(rb + ba[1]);
                glds_v(bp[0], lb);
                B[2] = lds_tr2_asm<0>(rb + ba[2]); B[3] = lds_tr2_asm<0>(rb + ba[3]);
                glds_v(bp[1], lb + 4096);
                B[4] = lds_tr2_asm<0>(rb + ba[4]);
                A[0] = lds_b128(lds + so + aoff); A[1] = lds_b128(lds + so + aoff + 1024);
                glds_v(bp[2], lb + 8192);
                A[2] = lds_b128(lds + so + aoff + 2048); A[3] = lds_b128(lds + so + aoff + 3072); A[4] = lds_b128(lds + so + aoff + 4096);
                glds_v(bp[3], lb + 12288);
                A[5] = lds_b128(lds + so + aoff + 5120); A[6] = lds_b128(lds + so + aoff + 6144); A[7] = lds_b128(lds + so + aoff + 7168);
                glds_v(bp[4], lb + 16384);
                bp[0] += ldq; bp[1] += ldg; bp[2] += ldg; bp[3] += ldg; bp[4] += ld4;
                WAIT_L0(); WAIT_V(10);
            } else if (wl < 3) {
                B[0] = lds_tr2_asm<0>(rb + ba[0]); B[1] = lds_tr2_asm<0>(rb + ba[1]);
                glds_s_sc1_first<0>(av0, an, la);
                B[2] = lds_tr2_asm<0>(rb + ba[2]); B[3] = lds_tr2_asm<0>(rb + ba[3]);
                glds_s_sc1_next<1024>(av0, an);
                B[4] = lds_tr2_asm<0>(rb + ba[4]);
                A[0] = lds_b128(lds + so + aoff); A[1] = lds_b128(lds + so + aoff + 1024);
                glds_s_sc1_next<2048>(av0, an);
                A[2] = lds_b128(lds + so + aoff + 2048); A[3] = lds_b128(lds + so + aoff + 3072);
                glds_s_sc1_next<3072>(av0, an);
                A[4] = lds_b128(lds + so + aoff + 4096); A[5] = lds_b128(lds + so + aoff + 5120);
                glds_s_sc1_first<0>(av1, an, la + 4096);
                A[6] = lds_b128(lds + so + aoff + 6144); A[7] = lds_b128(lds + so + aoff + 7168);
                if (wl == 0) glds_s_sc1_next<1024>(av1, an);
                an += 16384;
                WAIT_L0();
                if (wl == 0) WAIT_V(12); else WAIT_V(10);
            } else {
                if (t < 18) {
                    asm volatile("global_load_dword %0, %1, off" : "+v"(touch) : "v"(tp) : "memory");
                    if (t == 13) tp = wo + (size_t)(blockIdx.x * 64 + (lane & 15)) * 128; else tp += 2048;
                }
#pragma unroll
                for (int n = 0; n < 5; ++n) B[n] = lds_tr2_asm<0>(rb + ba[n]);
#pragma unroll
                for (int m = 0; m < 8; ++m) A[m] = lds_b128(lds + so + aoff + m * 1024);
                WAIT_L0();
            }
        } else {
#pragma unroll
            for (int n = 0; n < 5; ++n) B[n] = lds_tr2_asm<0>(rb + ba[n]);
#pragma unroll
            for (int m = 0; m < 8; ++m) A[m] = lds_b128(lds + so + aoff + m * 1024);
            WAIT_L0(); WAIT_V(0);
        }
        so = (so == 3 * G1_STAGE) ? 0 : so + G1_STAGE;
        sl = (sl == 3 * G1_STAGE) ? 0 : sl + G1_STAGE;
        asm volatile("" : "+s"(so), "+s"(sl));
        SCHED(); BAR(); SCHED();
        __builtin_amdgcn_s_setprio(1);
#pragma unroll
        for (int m = 0; m < 8; ++m)
#pragma unroll
            for (int n = 0; n < 5; ++n) { const int nn = (m & 1) ? 4 - n : n; MFMA_ACC(acc[m][nn], B[nn], A[m]); }
        __builtin_amdgcn_s_setprio(0);
        SCHED(); BAR(); SCHED();
    }
    asm volatile("" ::"v"(touch));
}

__device__ __forceinline__ void g2_issueB(unsigned bvoff, cptr sb, unsigned l) {
    glds_s(bvoff, sb, l);
    glds_s_o128(bvoff, sb, l + 4096);
}

__global__ __launch_bounds__(512, 2) void k_fused(const u16* __restrict__ x, const u16* __restrict__ wqkv, const u16* __restrict__ wphi, const u16* __restrict__ wo,
                                                  const u16* __restrict__ wg, const u16* __restrict__ wu, const u16* __restrict__ wd,
                                                  unsigned char* __restrict__ ws, u16* __restrict__ out) {
    extern __shared__ __attribute__((aligned(16))) unsigned char shm[];
    lds_t lds = (lds_t)shm;
    const unsigned ldsb = (unsigned)(size_t)lds;
    const int wid = __builtin_amdgcn_readfirstlane(threadIdx.x >> 6);
    const int grp = wid >> 2, wc = wid & 3;
    const int x8 = blockIdx.x & 7, r = blockIdx.x >> 3, ks = x8 >> 1, panel = 4 * (x8 & 1) + (r >> 3), c8 = r & 7, j = 4 * ks + (c8 >> 1), kvt = c8 & 1;
    const __amdgpu_buffer_rsrc_t rs = ws_rsrc(ws);
    STAMP_DECL; STAMP(0);
    const u64 t_start = __builtin_amdgcn_s_memrealtime();
#define PAD(T) do { while (__builtin_amdgcn_s_memrealtime() - t_start < (u64)(T)) __builtin_amdgcn_s_sleep(2); } while (0)
    u64 tag;
    f32x4 acc[8][5];

    {
        const int lane = threadIdx.x & 63, g = lane >> 4;
        const int row = panel * 256 + (ks * 8 + c8) * 8 + (lane & 7);
        u32x4 xv[4]; u32x2 ev = {0, 0};
#pragma unroll
        for (int i = 0; i < 4; ++i) xv[i] = (u32x4){0, 0, 0, 0};
        {
            const unsigned zero = 0;
            asm volatile("global_load_dwordx2 %0, %1, %2 sc1" : "+v"(ev) : "v"(zero), "s"(ws + WS_FLG + FL_EPOCH * 64) : "memory");
        }
        unsigned tlbt = 0, codet = 0;
        cptr bp[5];
        const size_t ldq = (size_t)32 * 3072 * 2, ldg = (size_t)32 * F_ * 2, ld4 = kvt ? ldq : ldg;
        if (grp == 0) {
            const int gq = (j & 1) ? 7 * j + 3 : 7 * j, gp = (j & 1) ? 7 * j + 1 : 7 * j + 4, gh = (j & 1) ? 7 * j : 7 * j + 6;
            const int krow = 8 * wc + (lane >> 3), chunk = (lane & 7) ^ swzB(krow);
            const size_t rq = (size_t)krow * 3072 * 2 + chunk * 16, rg = (size_t)krow * F_ * 2 + chunk * 16;
            if (!kvt) {
                bp[0] = (cptr)(wqkv + 64 * j) + rq;
                bp[1] = (cptr)(wg + 32 * gq) + rg;      bp[2] = (cptr)(wg + 32 * gq + 64) + rg;
                bp[3] = (cptr)(wu + 32 * gq) + rg;      bp[4] = (cptr)(wu + 32 * gq + 64) + rg;
            } else {
                bp[0] = (cptr)(wqkv + 1024 + 64 * j) + rq;
                bp[1] = (cptr)(wg + 32 * gp) + rg;
                bp[2] = (chunk < 4 ? (cptr)(wg + 32 * gh) : (cptr)(wu + 32 * gh) - 64) + rg;
                bp[3] = (cptr)(wu + 32 * gp) + rg;
                bp[4] = (cptr)(wqkv + 2048 + 64 * j) + rq;
            }
#pragma unroll
            for (int s = 0; s < 3; ++s) {
#pragma unroll
                for (int c = 0; c < 5; ++c) glds_v(bp[c], ldsb + s * G1_STAGE + G1_SA + c * 4096 + wc * 1024);
                bp[0] += ldq; bp[1] += ldg; bp[2] += ldg; bp[3] += ldg; bp[4] += ld4;
            }
            glds_v((cptr)(wphi + (size_t)(2 * j) * 1024 + wc * 512 + lane * 8), ldsb + L_WPHI + wc * 1024);
            {
                const int e = r * 40 + wc * 10 + (lane < 10 ? lane : 9);
                cptr p;
                if (e < 96) p = (cptr)wqkv + (size_t)e * 65536;
                else if (e < 208) p = (cptr)wg + (size_t)(e - 96) * 65536;
                else if (e < 320) p = (cptr)wu + (size_t)(e - 208) * 65536;
                else if (e < 432) p = (cptr)wd + (size_t)(e - 320) * 65536;
                else if (e < 464) p = (cptr)wo + (size_t)(e - 432) * 65536;
                else if (e < 528) p = (cptr)out + (size_t)(e - 464) * 65536;
                else if (e < 592) p = (cptr)x + (size_t)(e - 528) * 65536;
                else p = (cptr)ws + (size_t)min(e - 592, (int)(WS_END / 65536)) * 65536;
                asm volatile("global_load_dword %0, %1, off sc1" : "+v"(tlbt) : "v"(p) : "memory");
            }
            {
                u64 pc; asm volatile("s_getpc_b64 %0" : "=s"(pc));
                const int ln_ = wc * 64 + lane;
                cptr p = (cptr)(pc & ~(u64)0x7f) + (ln_ < CODE_LINES ? ln_ : CODE_LINES - 1) * 128;
                asm volatile("global_load_dword %0, %1, off" : "+v"(codet) : "v"(p) : "memory");
            }
        } else {
#pragma unroll
            for (int c = 0; c < 5; ++c) bp[c] = nullptr;
            const unsigned xo = (unsigned)(row * D_ + (wc * 8 + (lane >> 3)) * 8) * 2u;
            asm volatile("global_load_dwordx4 %0, %4, %5\n\tglobal_load_dwordx4 %1, %4, %5 offset:512\n\tglobal_load_dwordx4 %2, %4, %5 offset:1024\n\tglobal_load_dwordx4 %3, %4, %5 offset:1536\n\t"
                         "s_waitcnt vmcnt(0)" : "+v"(xv[0]), "+v"(xv[1]), "+v"(xv[2]), "+v"(xv[3]) : "v"(xo), "s"(x) : "memory");
        }
        asm volatile("s_waitcnt vmcnt(" XW_CNT ")" : "+v"(ev)::"memory");
        {
            u64 z = (((u64)(unsigned)__builtin_amdgcn_readfirstlane(ev[1]) << 32) | (unsigned)__builtin_amdgcn_readfirstlane(ev[0])) + 0x9E3779B97F4A7C15ull;
            z = (z ^ (z >> 30)) * 0xBF58476D1CE4E5B9ull; z = (z ^ (z >> 27)) * 0x94D049BB133111EBull; tag = z ^ (z >> 31);
        }
        STAMP(1);
        JIT(1);
        LAS f32x2* red = (LAS f32x2*)(lds + L_MISC);
        float f[32];
        if (grp == 1) {
            float s = 0.f;
#pragma unroll
            for (int i = 0; i < 4; ++i)
#pragma unroll
                for (int k = 0; k < 4; ++k) { f[8 * i + 2 * k] = lo_f(xv[i][k]); f[8 * i + 2 * k + 1] = hi_f(xv[i][k]); }
            { f32x2 s4[4];
#pragma unroll
              for (int i = 0; i < 4; ++i) s4[i] = (f32x2){f[2 * i], f[2 * i + 1]};
#pragma unroll
              for (int i = 4; i < 16; ++i) s4[i & 3] += (f32x2){f[2 * i], f[2 * i + 1]};
              const f32x2 s2 = (s4[0] + s4[1]) + (s4[2] + s4[3]);
              s = s2[0] + s2[1]; }
            s += ror8(s); s = sum_g(s);
            const float mw = s * (1.0f / 256.0f);
            f32x2 dv[16], q4[4];
#pragma unroll
            for (int i = 0; i < 16; ++i) dv[i] = (f32x2){f[2 * i], f[2 * i + 1]} - mw;
            SCHED();
#pragma unroll
            for (int i = 0; i < 4; ++i) q4[i] = dv[i] * dv[i];
#pragma unroll
            for (int i = 4; i < 16; ++i) q4[i & 3] = __builtin_elementwise_fma(dv[i], dv[i], q4[i & 3]);
            const f32x2 q2 = (q4[0] + q4[1]) + (q4[2] + q4[3]);
            float q = q2[0] + q2[1];
            q += ror8(q); q = sum_g(q);
            if (lane < 8) red[wc * 8 + lane] = (f32x2){s, q};
        }
        LBAR();
        if (grp == 1) {
            f32x2 p[4];
#pragma unroll
            for (int w = 0; w < 4; ++w) p[w] = red[w * 8 + (lane & 7)];
            const float mean = ((p[0][0] + p[1][0]) + (p[2][0] + p[3][0])) * (1.0f / D_);
            float m2 = (p[0][1] + p[1][1]) + (p[2][1] + p[3][1]);
#pragma unroll
            for (int w = 0; w < 4; ++w) { const float dm = p[w][0] * (1.0f / 256.0f) - mean; m2 += 256.0f * dm * dm; }
            const float var = m2 * (1.0f / D_);
            const float m = rbf(mean), sd = rbf(sqrtf(rbf(rbf(var) + 1e-5f))), rsd = 1.0f / sd;
            unsigned w[16];
#pragma unroll
            for (int i = 0; i < 16; ++i) { const f32x2 d = (f32x2){f[2 * i], f[2 * i + 1]} - m; w[i] = pk_bf16(d[0], d[1]); }
            asm volatile("" : "+v"(w[0]), "+v"(w[1]), "+v"(w[2]), "+v"(w[3]), "+v"(w[4]), "+v"(w[5]), "+v"(w[6]), "+v"(w[7]));
            asm volatile("" : "+v"(w[8]), "+v"(w[9]), "+v"(w[10]), "+v"(w[11]), "+v"(w[12]), "+v"(w[13]), "+v"(w[14]), "+v"(w[15]));
            const unsigned xo = WS_XN + (unsigned)((panel * 32 * 16 + ((row >> 4) & 15)) * 1024 + (row & 15) * 16);
#pragma unroll
            for (int it = 0; it < 4; ++it) {
                const int pc = it * 32 + wc * 8 + (lane >> 3), kc = pc >> 2, kq = pc & 3;
                f32x2 a[4], q0[4];
#pragma unroll
                for (int i = 0; i < 4; ++i) { a[i] = (f32x2){lo_f(w[4 * it + i]), hi_f(w[4 * it + i])}; q0[i] = a[i] * rsd; }
#pragma unroll
                for (int i = 0; i < 4; ++i) a[i] = __builtin_elementwise_fma(-q0[i], (f32x2){sd, sd}, a[i]);
#pragma unroll
                for (int i = 0; i < 4; ++i) q0[i] = __builtin_elementwise_fma(a[i], (f32x2){rsd, rsd}, q0[i]);
                const u32x4 o = {pk_bf16(q0[0][0], q0[0][1]), pk_bf16(q0[1][0], q0[1][1]), pk_bf16(q0[2][0], q0[2][1]), pk_bf16(q0[3][0], q0[3][1])};
                ST_SC1(o, rs, xo + (unsigned)(kc * 16384 + kq * 256));
                SCHED();
            }
            WAIT_V(0);
        }
        LBAR();
        STAMP(2);
        if (threadIdx.x == 256) flag_set(ws, FL_LN + panel * 32 + ks * 8 + c8, tag);
        if (wid == 0) flag_wait(ws, FL_LN + panel * 32, 1, 32, tag, lane);
        LBAR();
        STAMP(3);
        JIT(3);

        cptr xn = uni((cptr)ws + WS_XN + (size_t)panel * 32 * 16384);
        if (grp == 1 && wc < 3) {
            const int p0 = wc == 0 ? 0 : 5 * wc + 1;
            const unsigned av0 = p0 * 1024 + lane * 16, av1 = av0 + 4096;
#pragma unroll
            for (int s3 = 0; s3 < 3; ++s3) {
                const unsigned la = ldsb + s3 * G1_STAGE + p0 * 1024;
                glds_s_sc1_first<0>(av0, xn + s3 * 16384, la); glds_s_sc1_next<1024>(av0, xn + s3 * 16384);
                glds_s_sc1_next<2048>(av0, xn + s3 * 16384);   glds_s_sc1_next<3072>(av0, xn + s3 * 16384);
                glds_s_sc1_first<0>(av1, xn + s3 * 16384, la + 4096);
                if (wc == 0) glds_s_sc1_next<1024>(av1, xn + s3 * 16384);
            }
            if (wc == 0) WAIT_V(12); else WAIT_V(10);
        }
        int ba[5];
        {
            int cgA, tlA, cgB, tlB;
            if (!kvt) { cgA = 1 + (wc >> 1); tlA = 2 * (wc & 1); cgB = 3 + (wc >> 1); tlB = tlA; }
            else if (wc < 2) { cgA = 1; tlA = 2 * wc; cgB = 3; tlB = 2 * wc; }
            else if (wc == 2) { cgA = 2; tlA = 0; cgB = 2; tlB = 2; }
            else { cgA = 4; tlA = 0; cgB = 4; tlB = 2; }
            const int q4 = (lane >> 2) & 3, p = lane & 3, krow = 8 * g + q4, sw = swzB(krow);
            auto adr = [&](int cg, int tl) { return G1_SA + cg * 4096 + krow * 128 + (((2 * tl + (p >> 1)) ^ sw) << 4) + (p & 1) * 8; };
            ba[0] = adr(cgA, tlA); ba[1] = adr(cgA, tlA + 1); ba[2] = adr(cgB, tlB); ba[3] = adr(cgB, tlB + 1); ba[4] = adr(0, wc);
        }
#pragma unroll
        for (int m = 0; m < 8; ++m)
#pragma unroll
            for (int n = 0; n < 5; ++n) acc[m][n] = (f32x4){0.f, 0.f, 0.f, 0.f};
        if (grp == 0) asm volatile("s_waitcnt vmcnt(0)" : "+v"(tlbt), "+v"(codet)::"memory");
        BAR();
        STAMP(4);
        if (grp == 0) { g1_mainloop<0>(ldsb, lds, acc, bp, ld4, xn, wc, lane, ba, (cptr)wd, (cptr)wo); BAR(); }
        else          { BAR(); g1_mainloop<1>(ldsb, lds, acc, bp, ld4, xn, wc, lane, ba, (cptr)wd, (cptr)wo); }
        STAMP(5);
    }

    JIT(5);
    int tz = threadIdx.x; asm volatile("" : "+v"(tz));
    const int lane = tz & 63, g = lane >> 4, l15 = lane & 15;
    const unsigned bvoff = (unsigned)((8 * wc + (lane >> 3)) * 2048 + c8 * 256 + (((lane & 7) ^ swzB(8 * wc + (lane >> 3))) << 4));
    const unsigned bl = ldsb + G2_SB + grp * 24576 + wc * 1024;
    cptr sbp = uni((cptr)wd + ((size_t)(28 * ks + grp) << 16));
    cptr sa = uni((cptr)ws + WS_H + ((size_t)(panel * NKC2 + 28 * ks + grp) << 14));
    const unsigned al = ldsb + grp * 49152 + wc * 4096;
    auto startA = [&](int s2) {
        int tz4 = threadIdx.x; asm volatile("" : "+v"(tz4));
        const unsigned avoff = (unsigned)(wc * 4096 + (tz4 & 63) * 16);
        glds_s_sc1_first<0>(avoff, sa, al + s2 * 16384); glds_s_sc1_next<1024>(avoff, sa); glds_s_sc1_next<2048>(avoff, sa); glds_s_sc1_next<3072>(avoff, sa);
        sa += 32768;
    };
    auto prefetchB = [&]() { g2_issueB(bvoff, sbp, bl); g2_issueB(bvoff, sbp + 131072, bl + 8192); sbp += 262144; };
    {
        const int gq = (j & 1) ? 7 * j + 3 : 7 * j, gp = (j & 1) ? 7 * j + 1 : 7 * j + 4, gh = (j & 1) ? 7 * j : 7 * j + 6;
        {
            lds_t kp = lds + E_KP + (128 * grp + l15) * 256 + ((((wc >> 1) * 4 + (wc & 1) * 2 + (g >> 1)) ^ l15) << 4) + (g & 1) * 8;
#pragma unroll
            for (int m = 0; m < 8; ++m) *(LAS u32x2*)(kp + m * 4096) = (u32x2){pk_bf16(acc[m][4][0], acc[m][4][1]), pk_bf16(acc[m][4][2], acc[m][4][3])};
        }
        const bool has_h = !(kvt && wc == 3);
        const int hkc = !kvt ? gq + wc : (wc < 2 ? gp + wc : gh);
        const unsigned hoff = WS_H + (unsigned)(((panel * NKC2 + hkc) * 16 + 8 * grp) * 1024 + frag_off(lane));
        auto geglu_c = [&](int m) {
            const f32x2 a0 = gelu_mul2(acc[m][0].lo, acc[m][2].lo), a1 = gelu_mul2(acc[m][0].hi, acc[m][2].hi);
            const f32x2 b0 = gelu_mul2(acc[m][1].lo, acc[m][3].lo), b1 = gelu_mul2(acc[m][1].hi, acc[m][3].hi);
            return pack_frag(a0, a1, b0, b1);
        };
        auto geglu = [&](int m) { ST_SC1(geglu_c(m), rs, hoff + m * 1024); SCHED(); };
        const int q4 = (lane >> 2) & 3, p4 = lane & 3;
        auto logits = [&](u32x4 (&lg)[4]) {
            lds_t wp = lds + L_WPHI + (8 * g + q4) * 64 + p4 * 8;
            lds_t qp = lds + E_KP + (32 * wid + l15) * 256;
            bf16x8 wf[2][2], qf[4];
#pragma unroll
            for (int hh = 0; hh < 2; ++hh) {
                wf[hh][0] = lds_tr2(wp + hh * 2048, wp + hh * 2048 + 256); wf[hh][1] = lds_tr2(wp + hh * 2048 + 32, wp + hh * 2048 + 288);
                qf[hh * 2] = lds_b128(qp + (((hh * 4 + g) ^ l15) << 4)); qf[hh * 2 + 1] = lds_b128(qp + 4096 + (((hh * 4 + g) ^ l15) << 4));
            }
            SCHED();
            const f32x4 z4 = {0.f, 0.f, 0.f, 0.f};
            f32x4 p0[4], p1[4];
#pragma unroll
            for (int u = 0; u < 4; ++u) { p0[u] = MFMA(wf[u >> 1][0], qf[u], z4); p1[u] = MFMA(wf[u >> 1][1], qf[u], z4); }
            SCHED();
#pragma unroll
            for (int u = 0; u < 4; ++u) lg[u] = (u32x4){pk_bf16(p0[u][0], p0[u][1]), pk_bf16(p0[u][2], p0[u][3]), pk_bf16(p1[u][0], p1[u][1]), pk_bf16(p1[u][2], p1[u][3])};
            asm volatile("" : "+v"(lg[0]), "+v"(lg[1]), "+v"(lg[2]), "+v"(lg[3]));
        };
        if (kvt) {
            LBAR();
            if (wc == 3) {
                const int sw = swzP(l15);
                lds_t vp = lds + E_V + (128 * grp + l15) * 128 + (g & 1) * 8;
#pragma unroll
                for (int n = 0; n < 4; ++n) {
                    lds_t vn = vp + (((2 * n + (g >> 1)) ^ sw) << 4);
#pragma unroll
                    for (int m = 0; m < 8; ++m) *(LAS u32x2*)(vn + m * 2048) = (u32x2){pk_bf16(acc[m][n][0], acc[m][n][1]), pk_bf16(acc[m][n][2], acc[m][n][3])};
                }
            }
            STAMP(6);
            u32x4 lg[4], P[4], M[4];
            logits(lg);
            WAIT_L0();
            soft_pm<4>(lg, P, M);
            {
                const int sw = swzF(l15);
                lds_t pb = lds + E_KP + (32 * wid + l15) * 256 + (g & 1) * 8;
#pragma unroll
                for (int u = 0; u < 4; ++u) {
                    const int c0 = (u >> 1) * 8 + (g >> 1), tb = u & 1;
                    *(LAS u32x2*)(pb + tb * 4096 + (((c0 + 0) ^ sw) << 4)) = (u32x2){P[u][0], P[u][1]};
                    *(LAS u32x2*)(pb + tb * 4096 + (((c0 + 2) ^ sw) << 4)) = (u32x2){P[u][2], P[u][3]};
                    *(LAS u32x2*)(pb + tb * 4096 + (((c0 + 4) ^ sw) << 4)) = (u32x2){M[u][0], M[u][1]};
                    *(LAS u32x2*)(pb + tb * 4096 + (((c0 + 6) ^ sw) << 4)) = (u32x2){M[u][2], M[u][3]};
                }
            }
            LBAR();
            STAMP(7);
            JIT(7);
            {
                const int hh = wid >> 2, ft = wid & 3;
                f32x4 kv0 = {0.f, 0.f, 0.f, 0.f}, kv1 = kv0, zz = kv0;
                const bf16x8 ones = {0x3F80, 0x3F80, 0x3F80, 0x3F80, 0x3F80, 0x3F80, 0x3F80, 0x3F80};
                const int t0 = 8 * g + q4, t1 = t0 + 4, cp = hh * 8 + 2 * ft + (p4 >> 1), cv = hh * 4 + (p4 >> 1), hb8 = (p4 & 1) * 8;
                lds_t pf0 = lds + E_KP + t0 * 256 + ((cp ^ swzF(t0 & 15)) << 4) + hb8, pf1 = lds + E_KP + t1 * 256 + ((cp ^ swzF(t1 & 15)) << 4) + hb8;
                lds_t va0 = lds + E_V + t0 * 128 + ((cv ^ swzP(t0)) << 4) + hb8, va1 = lds + E_V + t1 * 128 + ((cv ^ swzP(t1)) << 4) + hb8;
                lds_t vb0 = lds + E_V + t0 * 128 + (((cv + 2) ^ swzP(t0)) << 4) + hb8, vb1 = lds + E_V + t1 * 128 + (((cv + 2) ^ swzP(t1)) << 4) + hb8;
#pragma unroll
                for (int k8 = 0; k8 < 8; ++k8) {
                    const bf16x8 pf = lds_tr2(pf0 + k8 * 8192, pf1 + k8 * 8192), v0 = lds_tr2(va0 + k8 * 4096, va1 + k8 * 4096), v1 = lds_tr2(vb0 + k8 * 4096, vb1 + k8 * 4096);
                    kv0 = MFMA(pf, v0, kv0); kv1 = MFMA(pf, v1, kv1); zz = MFMA(pf, ones, zz);
                }
                ST_SC1(((u32x4){pk_bf16(kv0[0], kv0[1]), pk_bf16(kv0[2], kv0[3]), pk_bf16(kv1[0], kv1[1]), pk_bf16(kv1[2], kv1[3])}), rs,
                       WS_PART + (unsigned)(((panel * 32 + 2 * j + hh) * 4 + ft) * 1024 + lane * 16));
                if (l15 == 0) ST_SC1(__builtin_bit_cast(u32x4, zz), rs, WS_PZ + (unsigned)(((panel * 32 + 2 * j + hh) * 64 + 16 * ft + 4 * g) * 4));
            }
            SCHED();
            u32x4 h0 = {0, 0, 0, 0};
            if (has_h) h0 = geglu_c(0);
            WAIT_V(0);
            LBAR();
            if (threadIdx.x == 0) flag_set(ws, FL_KV + j * 8 + panel, tag);
            STAMP(8);
            JIT(9);
            prefetchB();
            if (has_h) {
                ST_SC1(h0, rs, hoff); SCHED();
                geglu(1); geglu(2); geglu(3); geglu(4); geglu(5); geglu(6); geglu(7);
            }
            WAIT_V(0);
            LBAR();
            if (threadIdx.x == 0) flag_set(ws, FL_H + (panel * 4 + ks) * 8 + c8, tag);
            STAMP(9);
        } else {
            u32x4 lg[4], P[4], M[4];
            LBAR();
            logits(lg);
            soft_pm<4>(lg, P, M);
#pragma unroll
            for (int u = 0; u < 4; ++u) asm volatile("" : "+v"(P[u]), "+v"(M[u]));
            SCHED();
            STAMP(6);
            JIT(11);
#pragma unroll
            for (int m = 0; m < 8; ++m) geglu(m);
            prefetchB();
            WAIT_V(4);
            LBAR();
            if (threadIdx.x == 0) flag_set(ws, FL_H + (panel * 4 + ks) * 8 + c8, tag);
            STAMP(7);
            if (wid == 0) flag_wait(ws, FL_KV + j * 8, 1, 8, tag, lane);
            if (blockIdx.x == 0 && wid == 1) {
                GAS u64* p = flagp(ws, FL_LN + lane);
#pragma unroll 1
                for (unsigned it = 0; it < (1u << 21); ++it) {
                    const u64 v0 = __hip_atomic_load(p, __ATOMIC_RELAXED, __HIP_MEMORY_SCOPE_AGENT), v1 = __hip_atomic_load(p + 64 * 8, __ATOMIC_RELAXED, __HIP_MEMORY_SCOPE_AGENT);
                    const u64 v2 = __hip_atomic_load(p + 128 * 8, __ATOMIC_RELAXED, __HIP_MEMORY_SCOPE_AGENT), v3 = __hip_atomic_load(p + 192 * 8, __ATOMIC_RELAXED, __HIP_MEMORY_SCOPE_AGENT);
                    if (__all(v0 == tag && v1 == tag && v2 == tag && v3 == tag)) { if (lane == 0) flag_set(ws, FL_EPOCH, tag); break; }
                    __builtin_amdgcn_s_sleep(1);
                }
            }
            LBAR();
            STAMP(8);
            u32x2 hfl = {0, 0};
            {
                const int hh = wid >> 2, ft = wid & 3;
                asm volatile("global_load_dwordx2 %0, %1, %2 sc1" : "+v"(hfl) : "v"((FL_H + (panel * 4 + ks) * 8 + (lane & 7)) * 64), "s"(ws + WS_FLG) : "memory");
                f32x4 a0, a1, az;
                u32x4 pw[8]; f32x4 pz[8];
#pragma unroll
                for (int p = 0; p < 8; ++p) {
                    pw[p] = LD_SC1(rs, WS_PART + (unsigned)(((p * 32 + 2 * j + hh) * 4 + ft) * 1024 + lane * 16));
                    pz[p] = __builtin_bit_cast(f32x4, LD_SC1(rs, WS_PZ + (unsigned)(((p * 32 + 2 * j + hh) * 64 + 16 * ft + 4 * g) * 4)));
                }
#pragma unroll
                for (int p = 0; p < 8; ++p) {
                    const f32x4 b0 = {lo_f(pw[p][0]), hi_f(pw[p][0]), lo_f(pw[p][1]), hi_f(pw[p][1])}, b1 = {lo_f(pw[p][2]), hi_f(pw[p][2]), lo_f(pw[p][3]), hi_f(pw[p][3])};
                    if (p == 0) { a0 = b0; a1 = b1; az = pz[0]; } else { a0 += b0; a1 += b1; az += pz[p]; }
                }
                lds_t kb = lds + L_KVL + ((hh * 2 + (ft >> 1)) * 3) * 1024 + lane * 16 + (ft & 1) * 8;
                *(LAS u32x2*)(kb) = (u32x2){pk_bf16(a0[0], a0[1]), pk_bf16(a0[2], a0[3])};
                *(LAS u32x2*)(kb + 1024) = (u32x2){pk_bf16(a1[0], a1[1]), pk_bf16(a1[2], a1[3])};
                *(LAS u32x2*)(kb + 2048) = (u32x2){pk_bf16(az[0], az[1]), pk_bf16(az[2], az[3])};
            }
            LBAR();
            STAMP(9);
            JIT(13);
            {
                asm volatile("" : "+v"(hfl));
                const u64 v = ((u64)hfl[1] << 32) | hfl[0];
                if (!__all(v == tag)) flag_wait(ws, FL_H + (panel * 4 + ks) * 8, 1, 8, tag, lane);
                startA(0); startA(1);
            }
#pragma unroll
            for (int hh = 0; hh < 2; ++hh) {
                bf16x8 kf[2][3];
#pragma unroll
                for (int s = 0; s < 2; ++s)
#pragma unroll
                    for (int d = 0; d < 3; ++d) kf[s][d] = lds_b128(lds + L_KVL + ((hh * 2 + s) * 3 + d) * 1024 + lane * 16);
#pragma unroll
                for (int tb = 0; tb < 2; ++tb) {
                    const bf16x8 Pf = __builtin_bit_cast(bf16x8, P[hh * 2 + tb]), Mf = __builtin_bit_cast(bf16x8, M[hh * 2 + tb]);
                    const f32x4 z4 = {0.f, 0.f, 0.f, 0.f};
                    const f32x4 n0 = MFMA(kf[1][0], Mf, MFMA(kf[0][0], Pf, z4));
                    const f32x4 n1 = MFMA(kf[1][1], Mf, MFMA(kf[0][1], Pf, z4));
                    const f32x4 dn = MFMA(kf[1][2], Mf, MFMA(kf[0][2], Pf, z4));
                    const float rden = __builtin_amdgcn_rcpf(rbf(rbf(dn[0]) + 1e-6f));
                    ST_SC1(pack_frag(n0.lo * rden, n0.hi * rden, n1.lo * rden, n1.hi * rden), rs, WS_H + (unsigned)(((panel * NKC2 + 112 + 2 * j + hh) * 16 + 2 * wid + tb) * 1024 + frag_off(lane)));
                }
            }
        }
    }
    STAMP(10);
    STAMP_OUT(0, 0, 11);

    STAMP(0);
    if (kvt) {
        if (wid == 0) flag_wait(ws, FL_H + (panel * 4 + ks) * 8, 1, 8, tag, lane);
        LBAR();
        startA(0); startA(1);
    }
    STAMP(1);
    f32x4 c2[8][4];
    {
        int tz3 = threadIdx.x; asm volatile("" : "+v"(tz3));
        const int lane = tz3 & 63, g = lane >> 4;
        const unsigned avoff = (unsigned)(wc * 4096 + lane * 16);
        const unsigned bvoff = (unsigned)((8 * wc + (lane >> 3)) * 2048 + c8 * 256 + (((lane & 7) ^ swzB(8 * wc + (lane >> 3))) << 4));
        const int wm = wc >> 1, wn = wc & 1;
        unsigned bb[4];
        {
            const int q4 = (lane >> 2) & 3, p = lane & 3, kr = 8 * g + q4, sw = swzB(kr);
#pragma unroll
            for (int tl = 0; tl < 4; ++tl) bb[tl] = ldsb + G2_SB + grp * 24576 + wn * 4096 + kr * 128 + (((2 * tl + (p >> 1)) ^ sw) << 4) + (p & 1) * 8;
        }
        lds_t ap = lds + grp * 49152 + wm * 8192 + lane * 16;
        u32x2 ofl = {0, 0};
        const unsigned fo = (FL_O + (panel * 4 + ks) * 4 + (lane & 3)) * 64;
        if (kvt) WAIT_V(4); else WAIT_V(8);
        LBAR();
        if (grp == 1) BAR();
        STAMP(2);
        auto step = [&](auto uc, auto fc, const int t) {
            constexpr int U = decltype(uc)::value, W = (U + 2) % 3;
            constexpr bool FIRST = decltype(fc)::value;
            bf16x8 A[8], B[4];
#define FST(x)
            FST(f0);
            if (U == 0) {
                if (t == 12) {
                    asm volatile("" : "+v"(ofl));
                    const u64 v = ((u64)ofl[1] << 32) | ofl[0];
                    if (!__all(v == tag)) flag_wait(ws, FL_O + (panel * 4 + ks) * 4, 1, 4, tag, lane);
                    sa = uni((cptr)ws + WS_H + ((size_t)(panel * NKC2 + 112 + 8 * ks + grp) << 14));
                    sbp = uni((cptr)wo + ((size_t)(8 * ks + grp) << 16));
                }
                asm volatile("s_cmp_lg_u32 %3, 9\n\ts_cbranch_scc1 .Lofl_%=\n\tglobal_load_dwordx2 %0, %1, %2 sc1\n.Lofl_%=:" : "+v"(ofl) : "v"(fo), "s"(ws + WS_FLG), "s"(t) : "scc", "memory");
            }
            if (U == 2 && t == 2 && !kvt && threadIdx.x == 0) flag_set(ws, FL_O + (panel * 4 + ks) * 4 + (c8 >> 1), tag);
            const bool more = U == 0 || t < 16;
            B[0] = lds_tr2_asm<U * 8192>(bb[0]); B[1] = lds_tr2_asm<U * 8192>(bb[1]);
            if (more) glds_s(bvoff, sbp, bl + W * 8192);
            B[2] = lds_tr2_asm<U * 8192>(bb[2]); B[3] = lds_tr2_asm<U * 8192>(bb[3]);
            if (more) { glds_s_o128(bvoff, sbp, bl + W * 8192 + 4096); sbp += 131072; }
            A[0] = lds_b128(ap + U * 16384); A[1] = lds_b128(ap + U * 16384 + 1024);
            if (more) { glds_s_sc1_first<0>(avoff, sa, al + W * 16384); glds_s_sc1_next<1024>(avoff, sa); }
            A[2] = lds_b128(ap + U * 16384 + 2048); A[3] = lds_b128(ap + U * 16384 + 3072); A[4] = lds_b128(ap + U * 16384 + 4096);
            if (more) { glds_s_sc1_next<2048>(avoff, sa); glds_s_sc1_next<3072>(avoff, sa); sa += 32768; }
            A[5] = lds_b128(ap + U * 16384 + 5120); A[6] = lds_b128(ap + U * 16384 + 6144); A[7] = lds_b128(ap + U * 16384 + 7168);
            FST(f1);
            WAIT_L0();
            FST(f2);
            if (!more) WAIT_V(0);
            SCHED(); BAR(); SCHED();
            FST(f3);
            __builtin_amdgcn_s_setprio(1);
    #pragma unroll
            for (int m = 0; m < 8; ++m)
#pragma unroll
                for (int n = 0; n < 4; ++n) { const int nn = (m & 1) ? 3 - n : n; if (FIRST) MFMA_SET(c2[m][nn], B[nn], A[m]); else MFMA_ACC(c2[m][nn], B[nn], A[m]); }
            __builtin_amdgcn_s_setprio(0);
            FST(f4);
            WAIT_V(6);
            FST(f5);
            SCHED(); BAR(); SCHED();
        };
        step(std::integral_constant<int, 0>{}, std::true_type{}, 0);
#pragma unroll 1
        for (int t = 1; t < 18; t += 3) {
            step(std::integral_constant<int, 1>{}, std::false_type{}, t); step(std::integral_constant<int, 2>{}, std::false_type{}, t + 1);
            if (t < 16) step(std::integral_constant<int, 0>{}, std::false_type{}, t + 2);
        }
        if (grp == 0) BAR();
    }
    STAMP(3);
    {
        int tz2 = threadIdx.x; asm volatile("" : "+v"(tz2));
        const int ln = tz2 & 63, tile = panel * 8 + c8, wm = wc >> 1, wn = wc & 1;
        const int qk = 2 * wm + grp, qs = qk ^ 1;
        const bool publisher = qk != ks;
        const int mh = wid >> 2, nq = wid & 3;
        const int row0 = panel * 256 + ks * 64 + mh * 32 + (ln & 15), col0 = c8 * 128 + nq * 32 + 8 * frag_kq(ln);
        constexpr int L_OWN = 0, L_SEND = 65536;
        u32x4 xr0 = {0, 0, 0, 0}, xr1 = xr0;
        {
            const unsigned xo = (unsigned)(row0 * D_ + col0) * 2u;
            asm volatile("global_load_dwordx4 %0, %1, %2" : "+v"(xr0) : "v"(xo), "s"(x) : "memory");
            asm volatile("global_load_dwordx4 %0, %1, %2" : "+v"(xr1) : "v"(xo + 16 * D_ * 2), "s"(x) : "memory");
        }
        f32x4 hk[4][4], hs[4][4];
#pragma unroll
        for (int m = 0; m < 4; ++m)
#pragma unroll
            for (int n = 0; n < 4; ++n) { hk[m][n] = grp ? c2[4 + m][n] : c2[m][n]; hs[m][n] = grp ? c2[m][n] : c2[4 + m][n]; }
        auto dump_own = [&](const f32x4 (&v)[4][4]) {
#pragma unroll
            for (int m = 0; m < 4; ++m)
#pragma unroll
                for (int n = 0; n < 4; ++n) *(LAS f32x4*)(lds + L_OWN + ((grp * 4 + m) * 8 + 4 * wn + n) * 1024 + ln * 16) = v[m][n];
        };
        if (qs == ks) dump_own(hs);
        else {
#pragma unroll
            for (int m = 0; m < 4; ++m)
#pragma unroll
                for (int n2 = 0; n2 < 2; ++n2) {
                    const f32x4 a = hs[m][2 * n2], b = hs[m][2 * n2 + 1];
                    *(LAS u32x4*)(lds + L_SEND + (wid ^ 4) * 8192 + (m * 2 + n2) * 1024 + ln * 16) = (u32x4){pk_bf16(a[0], a[1]), pk_bf16(a[2], a[3]), pk_bf16(b[0], b[1]), pk_bf16(b[2], b[3])};
                }
        }
        if (qk == ks) dump_own(hk);
        LBAR();
        if (publisher) {
            const unsigned xbase = WS_XCH + (unsigned)((((tile * 4 + ks) * 8 + wid) * 8) * 1024 + ln * 16);
#pragma unroll
            for (int m = 0; m < 4; ++m)
#pragma unroll
                for (int n2 = 0; n2 < 2; ++n2) {
                    const u32x4 w = *(const LAS u32x4*)(lds + L_SEND + wid * 8192 + (m * 2 + n2) * 1024 + ln * 16);
                    const f32x4 a = hk[m][2 * n2] + (f32x4){lo_f(w[0]), hi_f(w[0]), lo_f(w[1]), hi_f(w[1])}, b = hk[m][2 * n2 + 1] + (f32x4){lo_f(w[2]), hi_f(w[2]), lo_f(w[3]), hi_f(w[3])};
                    ST_SC1(((u32x4){pk_bf16(a[0], a[1]), pk_bf16(a[2], a[3]), pk_bf16(b[0], b[1]), pk_bf16(b[2], b[3])}), rs, xbase + (m * 2 + n2) * 1024);
                }
        }
        f32x4 e[2][2];
#pragma unroll
        for (int a = 0; a < 2; ++a)
#pragma unroll
            for (int b = 0; b < 2; ++b)
                e[a][b] = *(const LAS f32x4*)(lds + L_OWN + ((2 * mh + a) * 8 + 2 * nq + b) * 1024 + ln * 16) + *(const LAS f32x4*)(lds + L_OWN + ((4 + 2 * mh + a) * 8 + 2 * nq + b) * 1024 + ln * 16);
        STAMP(4);
        asm volatile("s_waitcnt vmcnt(0)" : "+v"(xr0), "+v"(xr1)::"memory");
        if (publisher && ln == 0) flag_set(ws, FL_X + (tile * 4 + ks) * 8 + wid, tag);
        STAMP(5);
        JIT(19);
        const int wsrc = (ks & 1) * 4 + (ks >> 1) * 2 + (nq >> 1);
        {
            GAS u64* p = flagp(ws, FL_X + (tile * 4 + ((ks + 1 + (ln < 3 ? ln : 0)) & 3)) * 8 + wsrc);
#pragma unroll 1
            for (unsigned it = 0; it < (1u << 21); ++it) {
                const u64 v = __hip_atomic_load(p, __ATOMIC_RELAXED, __HIP_MEMORY_SCOPE_AGENT);
                if (__all(v == tag)) break;
                __builtin_amdgcn_s_sleep(1);
            }
            asm volatile("" ::: "memory");
        }
        u32x4 pw[3][2];
#pragma unroll
        for (int i = 0; i < 3; ++i) {
            const unsigned base = WS_XCH + (unsigned)((((tile * 4 + i + (i >= ks ? 1 : 0)) * 8 + wsrc) * 8 + (nq & 1) + 4 * mh) * 1024 + ln * 16);
            pw[i][0] = LD_SC1(rs, base); pw[i][1] = LD_SC1(rs, base + 2048);
        }
#pragma unroll
        for (int i = 0; i < 3; ++i)
#pragma unroll
            for (int a = 0; a < 2; ++a) {
                const u32x4 w = pw[i][a];
                e[a][0] += (f32x4){lo_f(w[0]), hi_f(w[0]), lo_f(w[1]), hi_f(w[1])};
                e[a][1] += (f32x4){lo_f(w[2]), hi_f(w[2]), lo_f(w[3]), hi_f(w[3])};
            }
        const __amdgpu_buffer_rsrc_t ors = __builtin_amdgcn_make_buffer_rsrc((void*)out, 0, S_ * D_ * 2, 0x00020000);
#pragma unroll
        for (int a = 0; a < 2; ++a) {
            float u[4], v[4];
#pragma unroll
            for (int q = 0; q < 4; ++q) {
                auto rr = __builtin_amdgcn_permlane16_swap(__float_as_uint(e[a][0][q]), __float_as_uint(e[a][1][q]), false, false);
                u[q] = __uint_as_float(rr[0]); v[q] = __uint_as_float(rr[1]);
            }
            const u32x4 xv = a ? xr1 : xr0;
            u32x4 o;
            o[0] = pk_bf16(u[0] + lo_f(xv[0]), u[1] + hi_f(xv[0])); o[1] = pk_bf16(u[2] + lo_f(xv[1]), u[3] + hi_f(xv[1]));
            o[2] = pk_bf16(v[0] + lo_f(xv[2]), v[1] + hi_f(xv[2])); o[3] = pk_bf16(v[2] + lo_f(xv[3]), v[3] + hi_f(xv[3]));
            __builtin_amdgcn_raw_buffer_store_b128(o, ors, (unsigned)(((row0 + 16 * a) * D_ + col0) * 2), 0, 16);
        }
    }
    STAMP(6);
    PAD(PAD_END);
    STAMP_OUT(1, 0, 7);
}

extern "C" void kernel_launch(void* const* d_in, const int* in_sizes, int n_in, void* d_out, int out_size, void* d_ws, size_t ws_size, hipStream_t stream) {
    const u16* x = (const u16*)d_in[0]; const u16* wqkv = (const u16*)d_in[1]; const u16* wphi = (const u16*)d_in[2]; const u16* wo = (const u16*)d_in[3];
    const u16* wg = (const u16*)d_in[5]; const u16* wu = (const u16*)d_in[6]; const u16* wd = (const u16*)d_in[7];
    (void)hipFuncSetAttribute((const void*)k_fused, hipFuncAttributeMaxDynamicSharedMemorySize, L_TOTAL);
    k_fused<<<256, 512, L_TOTAL, stream>>>(x, wqkv, wphi, wo, wg, wu, wd, (unsigned char*)d_ws, (u16*)d_out);
}
```

```cpp
#include <hip/hip_runtime.h>
#include <type_traits>

typedef unsigned short u16;
typedef unsigned long long u64;
typedef short bf16x8 __attribute__((ext_vector_type(8)));
typedef short s16x4 __attribute__((ext_vector_type(4)));
typedef float f32x4 __attribute__((ext_vector_type(4)));
typedef float f32x2 __attribute__((ext_vector_type(2)));
typedef unsigned u32x4 __attribute__((ext_vector_type(4)));
typedef unsigned u32x2 __attribute__((ext_vector_type(2)));
#define LAS __attribute__((address_space(3)))
#define GAS __attribute__((address_space(1)))
typedef LAS unsigned char* lds_t;
typedef const unsigned char* cptr;

constexpr int S_ = 2048, D_ = 1024, F_ = 3584;
constexpr int CODE_LINES = 256;
constexpr int NKC2 = 144;

constexpr unsigned WS_XN   = 0;
constexpr unsigned WS_H    = WS_XN + S_ * D_ * 2;
constexpr unsigned WS_PART = WS_H + 8 * NKC2 * 16 * 1024;
constexpr unsigned WS_PZ   = WS_PART + 8 * 32 * 4 * 1024;
constexpr unsigned WS_XCH  = WS_PZ + 8 * 32 * 64 * 4;
constexpr unsigned WS_FLG  = WS_XCH + 64 * 4 * 8 * 8 * 1024;
constexpr unsigned FL_LN = 0, FL_KV = FL_LN + 256, FL_H = FL_KV + 128, FL_O = FL_H + 256, FL_X = FL_O + 128, FL_EPOCH = FL_X + 2048, FL_N = FL_EPOCH + 1;
constexpr unsigned WS_END  = WS_FLG + FL_N * 64;

constexpr int G1_SA = 16384, G1_STAGE = 36864;
constexpr int L_MISC = 158720, L_WPHI = 159744, L_TOTAL = 163840;
constexpr int L_KVL = 147456;
constexpr int E_KP = 0, E_V = 65536;
constexpr int G2_SB = 98304;

__device__ __forceinline__ unsigned pk_bf16(float lo, float hi) {
    typedef __bf16 bf2 __attribute__((ext_vector_type(2)));
    bf2 r = {(__bf16)lo, (__bf16)hi};
    return __builtin_bit_cast(unsigned, r);
}
__device__ __forceinline__ float rbf(float x) { return __uint_as_float(pk_bf16(x, x) & 0xffff0000u); }
__device__ __forceinline__ float lo_f(unsigned w) { return __uint_as_float(w << 16); }
__device__ __forceinline__ float hi_f(unsigned w) { return __uint_as_float(w & 0xffff0000u); }

#define XW_CNT "18"
#define WAIT_V(n) asm volatile("s_waitcnt vmcnt(" #n ")" ::: "memory")
#define WAIT_L0() do { asm volatile("" ::: "memory"); __builtin_amdgcn_s_waitcnt(0xC07F); asm volatile("" ::: "memory"); } while (0)
#define BAR() __builtin_amdgcn_s_barrier()
#define LBAR() asm volatile("s_waitcnt lgkmcnt(0)\n\ts_barrier" ::: "memory")
#define SCHED() __builtin_amdgcn_sched_barrier(0)
#define MFMA(a, b, c) __builtin_amdgcn_mfma_f32_16x16x32_bf16(a, b, c, 0, 0, 0)
#define MFMA_SET(c, a, b) asm volatile("v_mfma_f32_16x16x32_bf16 %0, %1, %2, 0" : "=&v"(c) : "v"(a), "v"(b))
#define MFMA_ACC(c, a, b) asm volatile("v_mfma_f32_16x16x32_bf16 %0, %1, %2, %0" : "+v"(c) : "v"(a), "v"(b))

__device__ __forceinline__ cptr uni(cptr p) {
    const u64 v = (u64)p;
    return (cptr)(((u64)(unsigned)__builtin_amdgcn_readfirstlane((unsigned)(v >> 32)) << 32) | (unsigned)__builtin_amdgcn_readfirstlane((unsigned)v));
}
__device__ __forceinline__ void glds_v(cptr g, unsigned l) {
    asm volatile("s_mov_b32 m0, %1\n\ts_nop 0\n\tglobal_load_lds_dwordx4 %0, off" ::"v"(g), "s"(l) : "memory");
}
__device__ __forceinline__ void glds_s(unsigned voff, cptr sb, unsigned l) {
    asm volatile("s_nop 2\n\ts_mov_b32 m0, %2\n\ts_nop 0\n\tglobal_load_lds_dwordx4 %0, %1" ::"v"(voff), "s"(sb), "s"(l) : "memory");
}
__device__ __forceinline__ void glds_s_o128(unsigned voff, cptr sb, unsigned l) {
    asm volatile("s_mov_b32 m0, %2\n\ts_nop 0\n\tglobal_load_lds_dwordx4 %0, %1 offset:128" ::"v"(voff), "s"(sb), "s"(l - 128u) : "memory");
}
template <int OFF> __device__ __forceinline__ void glds_s_sc1_first(unsigned voff, cptr sb, unsigned l) {
    asm volatile("s_nop 2\n\ts_mov_b32 m0, %2\n\ts_nop 0\n\tglobal_load_lds_dwordx4 %0, %1 offset:%3 sc1" ::"v"(voff), "s"(sb), "s"(l), "i"(OFF) : "memory");
}
template <int OFF> __device__ __forceinline__ void glds_s_sc1_next(unsigned voff, cptr sb) {
    asm volatile("global_load_lds_dwordx4 %0, %1 offset:%2 sc1" ::"v"(voff), "s"(sb), "i"(OFF) : "memory");
}
__device__ __forceinline__ bf16x8 lds_b128(lds_t p) { return *(const LAS bf16x8*)p; }
__device__ __forceinline__ bf16x8 lds_tr2(lds_t p0, lds_t p1) {
    s16x4 a = __builtin_amdgcn_ds_read_tr16_b64_v4i16((LAS s16x4*)p0);
    s16x4 b = __builtin_amdgcn_ds_read_tr16_b64_v4i16((LAS s16x4*)p1);
    return __builtin_shufflevector(a, b, 0, 1, 2, 3, 4, 5, 6, 7);
}
template <int OFF> __device__ __forceinline__ bf16x8 lds_tr2_asm(unsigned addr) {
    s16x4 a, b;
    asm volatile("ds_read_b64_tr_b16 %0, %2 offset:%3\n\tds_read_b64_tr_b16 %1, %2 offset:%4" : "=&v"(a), "=&v"(b) : "v"(addr), "i"(OFF), "i"(OFF + 512) : "memory");
    return __builtin_shufflevector(a, b, 0, 1, 2, 3, 4, 5, 6, 7);
}

__device__ __forceinline__ float x16(float v, float& o) { auto r = __builtin_amdgcn_permlane16_swap(__float_as_uint(v), __float_as_uint(v), false, false); o = __uint_as_float(r[1]); return __uint_as_float(r[0]); }
__device__ __forceinline__ float x32(float v, float& o) { auto r = __builtin_amdgcn_permlane32_swap(__float_as_uint(v), __float_as_uint(v), false, false); o = __uint_as_float(r[1]); return __uint_as_float(r[0]); }
__device__ __forceinline__ float sum_g(float v) { float o, a = x16(v, o); v = a + o; a = x32(v, o); return a + o; }
__device__ __forceinline__ float ror8(float v) { return __int_as_float(__builtin_amdgcn_update_dpp(0, __float_as_int(v), 0x128, 0xf, 0xf, false)); }

__device__ __forceinline__ int swzB(int k) { return (((k >> 1) & 1) << 1) | (((k >> 3) & 1) << 2); }
__device__ __forceinline__ int swzP(int k) { return ((k >> 2) & 1) | (((k >> 1) & 1) << 1) | (((k >> 3) & 1) << 2); }
__device__ __forceinline__ int swzF(int k) { return ((k >> 2) & 1) | ((k & 3) << 1) | (((k >> 3) & 1) << 3); }

__device__ __forceinline__ f32x2 gelu_mul2(f32x2 g, f32x2 u) {
    constexpr float C0 = -2.0f * 0.7978845608028654f * 1.4426950408889634f, C1 = C0 * 0.044715f;
    const f32x2 a = g * (g * g * C1 + C0);
    const f32x2 d = (f32x2){__builtin_amdgcn_exp2f(a[0]), __builtin_amdgcn_exp2f(a[1])} + 1.0f;
    return (g * u) * (f32x2){__builtin_amdgcn_rcpf(d[0]), __builtin_amdgcn_rcpf(d[1])};
}
__device__ __forceinline__ u32x4 pack_frag(const f32x2 a0, const f32x2 a1, const f32x2 b0, const f32x2 b1) {
    unsigned x0 = pk_bf16(a0[0], a0[1]), x1 = pk_bf16(a1[0], a1[1]), y0 = pk_bf16(b0[0], b0[1]), y1 = pk_bf16(b1[0], b1[1]);
    auto r0 = __builtin_amdgcn_permlane16_swap(x0, y0, false, false);
    auto r1 = __builtin_amdgcn_permlane16_swap(x1, y1, false, false);
    u32x4 o; o[0] = r0[0]; o[1] = r1[0]; o[2] = r0[1]; o[3] = r1[1];
    return o;
}
__device__ __forceinline__ int frag_kq(int lane) { const int g = lane >> 4; return ((g & 1) << 1) | (g >> 1); }
__device__ __forceinline__ int frag_off(int lane) { return (frag_kq(lane) * 16 + (lane & 15)) * 16; }

__device__ __forceinline__ float max3(float a, float b, float c) { float r; asm("v_max3_f32 %0, %1, %2, %3" : "=v"(r) : "v"(a), "v"(b), "v"(c)); return r; }
__device__ __forceinline__ float min3(float a, float b, float c) { float r; asm("v_min3_f32 %0, %1, %2, %3" : "=v"(r) : "v"(a), "v"(b), "v"(c)); return r; }
__device__ __forceinline__ float max2(float a, float b) { float r; asm("v_max_f32 %0, %1, %2" : "=v"(r) : "v"(a), "v"(b)); return r; }
template <int NU, class Op> __device__ __forceinline__ void red2_g(float (&a)[NU], float (&b)[NU], Op op) {
    float z[NU], y[NU];
#pragma unroll
    for (int u = 0; u < NU; ++u) {
        auto r = __builtin_amdgcn_permlane16_swap(__float_as_uint(a[u]), __float_as_uint(b[u]), false, false);
        z[u] = op(__uint_as_float(r[0]), __uint_as_float(r[1]));
    }
#pragma unroll
    for (int u = 0; u < NU; ++u) {
        auto q = __builtin_amdgcn_permlane32_swap(__float_as_uint(z[u]), __float_as_uint(z[u]), false, false);
        y[u] = op(__uint_as_float(q[0]), __uint_as_float(q[1]));
    }
#pragma unroll
    for (int u = 0; u < NU; ++u) {
        auto w = __builtin_amdgcn_permlane16_swap(__float_as_uint(y[u]), __float_as_uint(y[u]), false, false);
        a[u] = __uint_as_float(w[0]); b[u] = __uint_as_float(w[1]);
    }
}
template <int NU> __device__ __forceinline__ void soft_pm(const u32x4 (&w)[NU], u32x4 (&P)[NU], u32x4 (&M)[NU]) {
    constexpr float L2E = 1.4426950408889634f;
    f32x2 pv[NU][4]; float mx[NU], nm[NU];
#pragma unroll
    for (int u = 0; u < NU; ++u) {
#pragma unroll
        for (int i = 0; i < 4; ++i) pv[u][i] = (f32x2){lo_f(w[u][i]), hi_f(w[u][i])};
        mx[u] = max3(max3(max3(pv[u][0][0], pv[u][0][1], pv[u][1][0]), pv[u][1][1], pv[u][2][0]), max2(pv[u][2][1], pv[u][3][0]), pv[u][3][1]);
        nm[u] = -min3(min3(min3(pv[u][0][0], pv[u][0][1], pv[u][1][0]), pv[u][1][1], pv[u][2][0]), min3(pv[u][2][1], pv[u][3][0], pv[u][3][1]), pv[u][3][1]);
    }
    red2_g<NU>(mx, nm, [](float a, float b) { return max2(a, b); });
    f32x2 pp[NU][4], pm[NU][4]; float sp[NU], sm[NU];
#pragma unroll
    for (int u = 0; u < NU; ++u) {
        const float cp = -mx[u] * L2E, cm = -nm[u] * L2E;
#pragma unroll
        for (int i = 0; i < 4; ++i) {
            const f32x2 a = pv[u][i] * L2E + cp, b = pv[u][i] * (-L2E) + cm;
            pp[u][i] = (f32x2){__builtin_amdgcn_exp2f(a[0]), __builtin_amdgcn_exp2f(a[1])};
            pm[u][i] = (f32x2){__builtin_amdgcn_exp2f(b[0]), __builtin_amdgcn_exp2f(b[1])};
        }
        const f32x2 s2 = (pp[u][0] + pp[u][1]) + (pp[u][2] + pp[u][3]), m2 = (pm[u][0] + pm[u][1]) + (pm[u][2] + pm[u][3]);
        sp[u] = s2[0] + s2[1]; sm[u] = m2[0] + m2[1];
    }
    red2_g<NU>(sp, sm, [](float a, float b) { return a + b; });
#pragma unroll
    for (int u = 0; u < NU; ++u) {
        const float rp = __builtin_amdgcn_rcpf(sp[u]), rm = __builtin_amdgcn_rcpf(sm[u]);
#pragma unroll
        for (int i = 0; i < 4; ++i) { const f32x2 a = pp[u][i] * rp, b = pm[u][i] * rm; P[u][i] = pk_bf16(a[0], a[1]); M[u][i] = pk_bf16(b[0], b[1]); }
    }
}

__device__ __forceinline__ GAS u64* flagp(unsigned char* ws, unsigned i) { return (GAS u64*)(ws + WS_FLG + (size_t)i * 64); }
__device__ __forceinline__ void flag_set(unsigned char* ws, unsigned i, u64 tag) { __hip_atomic_store(flagp(ws, i), tag, __ATOMIC_RELAXED, __HIP_MEMORY_SCOPE_AGENT); }
__device__ __forceinline__ void flag_wait(unsigned char* ws, unsigned i0, unsigned st, int n, u64 tag, int lane) {
    GAS u64* p = flagp(ws, i0 + st * (unsigned)(lane < n ? lane : 0));
#pragma unroll 1
    for (unsigned it = 0; it < (1u << 21); ++it) {
        const u64 v = __hip_atomic_load(p, __ATOMIC_RELAXED, __HIP_MEMORY_SCOPE_AGENT);
        if (__all(v == tag)) break;
        __builtin_amdgcn_s_sleep(1);
    }
    asm volatile("" ::: "memory");
}
__device__ __forceinline__ __amdgpu_buffer_rsrc_t ws_rsrc(unsigned char* ws) { return __builtin_amdgcn_make_buffer_rsrc((void*)ws, 0, WS_END, 0x00020000); }
#define ST_SC1(v, rs, off) __builtin_amdgcn_raw_buffer_store_b128(v, rs, off, 0, 16)
#define LD_SC1(rs, off) __builtin_amdgcn_raw_buffer_load_b128(rs, off, 0, 16)

template <int KIND>
__device__ __forceinline__ void g1_mainloop(const unsigned ldsb, lds_t lds, f32x4 (&acc)[8][5], cptr (&bp)[5], const size_t ld4, cptr xn, const int wl, const int lane, const int (&ba)[5],
                                            cptr wd, cptr wo) {
    const size_t ldq = (size_t)32 * 3072 * 2, ldg = (size_t)32 * F_ * 2;
    constexpr int GRP = KIND ? 1 : 0;
    const int aoff = GRP * 8192 + lane * 16;
    const int p0 = wl == 0 ? 0 : 5 * wl + 1;
    const unsigned av0 = p0 * 1024 + lane * 16, av1 = av0 + 4096;
    int so = 0, sl = 3 * G1_STAGE;
    cptr an = xn + 3 * 16384;
    unsigned touch = 0;
    cptr tp = wd + (size_t)(blockIdx.x * 224 + (lane & 15)) * 128;
    bf16x8 A[8], B[5];
#pragma unroll 1
    for (int t = 0; t < 32; ++t) {
        const bool more = t < 29;
        const unsigned lb = ldsb + sl + G1_SA + wl * 1024, la = ldsb + sl + p0 * 1024, rb = ldsb + so;
        if (KIND == 2) {
            asm volatile("s_cmp_gt_i32 %2, 17\n\ts_cbranch_scc1 .Ltch_%=\n\tglobal_load_dword %0, %1, off\n.Ltch_%=:" : "+v"(touch) : "v"(tp), "s"(t) : "scc", "memory");
            if (t == 13) tp = wo + (size_t)(blockIdx.x * 64 + (lane & 15)) * 128; else tp += t < 17 ? 2048 : 0;
        }
        if (more) {
            if (KIND == 0) {
                B[0] = lds_tr2_asm<0>(rb + ba[0]); B[1] = lds_tr2_asm<0>(rb + ba[1]);
                glds_v(bp[0], lb);
                B[2] = lds_tr2_asm<0>(rb + ba[2]); B[3] = lds_tr2_asm<0>(rb + ba[3]);
                glds_v(bp[1], lb + 4096);
                B[4] = lds_tr2_asm<0>(rb + ba[4]);
                A[0] = lds_b128(lds + so + aoff); A[1] = lds_b128(lds + so + aoff + 1024);
                glds_v(bp[2], lb + 8192);
                A[2] = lds_b128(lds + so + aoff + 2048); A[3] = lds_b128(lds + so + aoff + 3072); A[4] = lds_b128(lds + so + aoff + 4096);
                glds_v(bp[3], lb + 12288);
                A[5] = lds_b128(lds + so + aoff + 5120); A[6] = lds_b128(lds + so + aoff + 6144); A[7] = lds_b128(lds + so + aoff + 7168);
                glds_v(bp[4], lb + 16384);
                bp[0] += ldq; bp[1] += ldg; bp[2] += ldg; bp[3] += ldg; bp[4] += ld4;
                WAIT_L0(); WAIT_V(10);
            } else if (KIND == 1) {
                B[0] = lds_tr2_asm<0>(rb + ba[0]); B[1] = lds_tr2_asm<0>(rb + ba[1]);
                glds_s_sc1_first<0>(av0, an, la);
                B[2] = lds_tr2_asm<0>(rb + ba[2]); B[3] = lds_tr2_asm<0>(rb + ba[3]);
                glds_s_sc1_next<1024>(av0, an);
                B[4] = lds_tr2_asm<0>(rb + ba[4]);
                A[0] = lds_b128(lds + so + aoff); A[1] = lds_b128(lds + so + aoff + 1024);
                glds_s_sc1_next<2048>(av0, an);
                A[2] = lds_b128(lds + so + aoff + 2048); A[3] = lds_b128(lds + so + aoff + 3072);
                glds_s_sc1_next<3072>(av0, an);
                A[4] = lds_b128(lds + so + aoff + 4096); A[5] = lds_b128(lds + so + aoff + 5120);
                glds_s_sc1_first<0>(av1, an, la + 4096);
                A[6] = lds_b128(lds + so + aoff + 6144); A[7] = lds_b128(lds + so + aoff + 7168);
                if (wl == 0) glds_s_sc1_next<1024>(av1, an);
                an += 16384;
                WAIT_L0();
                if (wl == 0) WAIT_V(12); else WAIT_V(10);
            } else {
#pragma unroll
                for (int n = 0; n < 5; ++n) B[n] = lds_tr2_asm<0>(rb + ba[n]);
#pragma unroll
                for (int m = 0; m < 8; ++m) A[m] = lds_b128(lds + so + aoff + m * 1024);
                WAIT_L0();
            }
        } else {
#pragma unroll
            for (int n = 0; n < 5; ++n) B[n] = lds_tr2_asm<0>(rb + ba[n]);
#pragma unroll
            for (int m = 0; m < 8; ++m) A[m] = lds_b128(lds + so + aoff + m * 1024);
            WAIT_L0(); WAIT_V(0);
        }
        so = (so == 3 * G1_STAGE) ? 0 : so + G1_STAGE;
        sl = (sl == 3 * G1_STAGE) ? 0 : sl + G1_STAGE;
        asm volatile("" : "+s"(so), "+s"(sl));
        SCHED(); BAR(); SCHED();
#pragma unroll
        for (int m = 0; m < 8; ++m)
#pragma unroll
            for (int n = 0; n < 5; ++n) { const int nn = (m & 1) ? 4 - n : n; MFMA_ACC(acc[m][nn], B[nn], A[m]); }
        SCHED(); BAR(); SCHED();
    }
    if (KIND == 2) asm volatile("" ::"v"(touch));
}

__device__ __forceinline__ void g2_issueB(unsigned bvoff, cptr sb, unsigned l) {
    glds_s(bvoff, sb, l);
    glds_s_o128(bvoff, sb, l + 4096);
}

__global__ __launch_bounds__(512, 2) void k_fused(const u16* __restrict__ x, const u16* __restrict__ wqkv, const u16* __restrict__ wphi, const u16* __restrict__ wo,
                                                  const u16* __restrict__ wg, const u16* __restrict__ wu, const u16* __restrict__ wd,
                                                  unsigned char* __restrict__ ws, u16* __restrict__ out) {
    extern __shared__ __attribute__((aligned(16))) unsigned char shm[];
    lds_t lds = (lds_t)shm;
    const unsigned ldsb = (unsigned)(size_t)lds;
    const int wid = __builtin_amdgcn_readfirstlane(threadIdx.x >> 6);
    const int grp = wid >> 2, wc = wid & 3;
    const int x8 = blockIdx.x & 7, r = blockIdx.x >> 3, ks = x8 >> 1, panel = 4 * (x8 & 1) + (r >> 3), c8 = r & 7, j = 4 * ks + (c8 >> 1), kvt = c8 & 1;
    const __amdgpu_buffer_rsrc_t rs = ws_rsrc(ws);
    u64 tag;
    f32x4 acc[8][5];

    {
        const int lane = threadIdx.x & 63, g = lane >> 4;
        const int row = panel * 256 + (ks * 8 + c8) * 8 + (lane & 7);
        u32x4 xv[4]; u32x2 ev = {0, 0};
#pragma unroll
        for (int i = 0; i < 4; ++i) xv[i] = (u32x4){0, 0, 0, 0};
        {
            const unsigned zero = 0;
            asm volatile("global_load_dwordx2 %0, %1, %2 sc1" : "+v"(ev) : "v"(zero), "s"(ws + WS_FLG + FL_EPOCH * 64) : "memory");
        }
        unsigned tlbt = 0, codet = 0;
        cptr bp[5];
        const size_t ldq = (size_t)32 * 3072 * 2, ldg = (size_t)32 * F_ * 2, ld4 = kvt ? ldq : ldg;
        if (grp == 0) {
            const int gq = (j & 1) ? 7 * j + 3 : 7 * j, gp = (j & 1) ? 7 * j + 1 : 7 * j + 4, gh = (j & 1) ? 7 * j : 7 * j + 6;
            const int krow = 8 * wc + (lane >> 3), chunk = (lane & 7) ^ swzB(krow);
            const size_t rq = (size_t)krow * 3072 * 2 + chunk * 16, rg = (size_t)krow * F_ * 2 + chunk * 16;
            if (!kvt) {
                bp[0] = (cptr)(wqkv + 64 * j) + rq;
                bp[1] = (cptr)(wg + 32 * gq) + rg;      bp[2] = (cptr)(wg + 32 * gq + 64) + rg;
                bp[3] = (cptr)(wu + 32 * gq) + rg;      bp[4] = (cptr)(wu + 32 * gq + 64) + rg;
            } else {
                bp[0] = (cptr)(wqkv + 1024 + 64 * j) + rq;
                bp[1] = (cptr)(wg + 32 * gp) + rg;
                bp[2] = (chunk < 4 ? (cptr)(wg + 32 * gh) : (cptr)(wu + 32 * gh) - 64) + rg;
                bp[3] = (cptr)(wu + 32 * gp) + rg;
                bp[4] = (cptr)(wqkv + 2048 + 64 * j) + rq;
            }
#pragma unroll
            for (int s = 0; s < 3; ++s) {
#pragma unroll
                for (int c = 0; c < 5; ++c) glds_v(bp[c], ldsb + s * G1_STAGE + G1_SA + c * 4096 + wc * 1024);
                bp[0] += ldq; bp[1] += ldg; bp[2] += ldg; bp[3] += ldg; bp[4] += ld4;
            }
            glds_v((cptr)(wphi + (size_t)(2 * j) * 1024 + wc * 512 + lane * 8), ldsb + L_WPHI + wc * 1024);
            {
                const int e = r * 40 + wc * 10 + (lane < 10 ? lane : 9);
                cptr p;
                if (e < 96) p = (cptr)wqkv + (size_t)e * 65536;
                else if (e < 208) p = (cptr)wg + (size_t)(e - 96) * 65536;
                else if (e < 320) p = (cptr)wu + (size_t)(e - 208) * 65536;
                else if (e < 432) p = (cptr)wd + (size_t)(e - 320) * 65536;
                else if (e < 464) p = (cptr)wo + (size_t)(e - 432) * 65536;
                else if (e < 528) p = (cptr)out + (size_t)(e - 464) * 65536;
                else if (e < 592) p = (cptr)x + (size_t)(e - 528) * 65536;
                else p = (cptr)ws + (size_t)min(e - 592, (int)(WS_END / 65536)) * 65536;
                asm volatile("global_load_dword %0, %1, off sc1" : "+v"(tlbt) : "v"(p) : "memory");
            }
            {
                u64 pc; asm volatile("s_getpc_b64 %0" : "=s"(pc));
                const int ln_ = wc * 64 + lane;
                cptr p = (cptr)(pc & ~(u64)0x7f) + (ln_ < CODE_LINES ? ln_ : CODE_LINES - 1) * 128;
                asm volatile("global_load_dword %0, %1, off" : "+v"(codet) : "v"(p) : "memory");
            }
        } else {
#pragma unroll
            for (int c = 0; c < 5; ++c) bp[c] = nullptr;
            const unsigned xo = (unsigned)(row * D_ + (wc * 8 + (lane >> 3)) * 8) * 2u;
            asm volatile("global_load_dwordx4 %0, %4, %5\n\tglobal_load_dwordx4 %1, %4, %5 offset:512\n\tglobal_load_dwordx4 %2, %4, %5 offset:1024\n\tglobal_load_dwordx4 %3, %4, %5 offset:1536\n\t"
                         "s_waitcnt vmcnt(0)" : "+v"(xv[0]), "+v"(xv[1]), "+v"(xv[2]), "+v"(xv[3]) : "v"(xo), "s"(x) : "memory");
        }
        asm volatile("s_waitcnt vmcnt(" XW_CNT ")" : "+v"(ev)::"memory");
        {
            u64 z = (((u64)(unsigned)__builtin_amdgcn_readfirstlane(ev[1]) << 32) | (unsigned)__builtin_amdgcn_readfirstlane(ev[0])) + 0x9E3779B97F4A7C15ull;
            z = (z ^ (z >> 30)) * 0xBF58476D1CE4E5B9ull; z = (z ^ (z >> 27)) * 0x94D049BB133111EBull; tag = z ^ (z >> 31);
        }
        LAS f32x2* red = (LAS f32x2*)(lds + L_MISC);
        float f[32];
        if (grp == 1) {
            float s = 0.f;
#pragma unroll
            for (int i = 0; i < 4; ++i)
#pragma unroll
                for (int k = 0; k < 4; ++k) { f[8 * i + 2 * k] = lo_f(xv[i][k]); f[8 * i + 2 * k + 1] = hi_f(xv[i][k]); }
            { f32x2 s4[4];
#pragma unroll
              for (int i = 0; i < 4; ++i) s4[i] = (f32x2){f[2 * i], f[2 * i + 1]};
#pragma unroll
              for (int i = 4; i < 16; ++i) s4[i & 3] += (f32x2){f[2 * i], f[2 * i + 1]};
              const f32x2 s2 = (s4[0] + s4[1]) + (s4[2] + s4[3]);
              s = s2[0] + s2[1]; }
            s += ror8(s); s = sum_g(s);
            const float mw = s * (1.0f / 256.0f);
            f32x2 dv[16], q4[4];
#pragma unroll
            for (int i = 0; i < 16; ++i) dv[i] = (f32x2){f[2 * i], f[2 * i + 1]} - mw;
            SCHED();
#pragma unroll
            for (int i = 0; i < 4; ++i) q4[i] = dv[i] * dv[i];
#pragma unroll
            for (int i = 4; i < 16; ++i) q4[i & 3] = __builtin_elementwise_fma(dv[i], dv[i], q4[i & 3]);
            const f32x2 q2 = (q4[0] + q4[1]) + (q4[2] + q4[3]);
            float q = q2[0] + q2[1];
            q += ror8(q); q = sum_g(q);
            if (lane < 8) red[wc * 8 + lane] = (f32x2){s, q};
        }
        LBAR();
        if (grp == 1) {
            f32x2 p[4];
#pragma unroll
            for (int w = 0; w < 4; ++w) p[w] = red[w * 8 + (lane & 7)];
            const float mean = ((p[0][0] + p[1][0]) + (p[2][0] + p[3][0])) * (1.0f / D_);
            float m2 = (p[0][1] + p[1][1]) + (p[2][1] + p[3][1]);
#pragma unroll
            for (int w = 0; w < 4; ++w) { const float dm = p[w][0] * (1.0f / 256.0f) - mean; m2 += 256.0f * dm * dm; }
            const float var = m2 * (1.0f / D_);
            const float m = rbf(mean), sd = rbf(sqrtf(rbf(rbf(var) + 1e-5f))), rsd = 1.0f / sd;
            unsigned w[16];
#pragma unroll
            for (int i = 0; i < 16; ++i) { const f32x2 d = (f32x2){f[2 * i], f[2 * i + 1]} - m; w[i] = pk_bf16(d[0], d[1]); }
            asm volatile("" : "+v"(w[0]), "+v"(w[1]), "+v"(w[2]), "+v"(w[3]), "+v"(w[4]), "+v"(w[5]), "+v"(w[6]), "+v"(w[7]));
            asm volatile("" : "+v"(w[8]), "+v"(w[9]), "+v"(w[10]), "+v"(w[11]), "+v"(w[12]), "+v"(w[13]), "+v"(w[14]), "+v"(w[15]));
            const unsigned xo = WS_XN + (unsigned)((panel * 32 * 16 + ((row >> 4) & 15)) * 1024 + (row & 15) * 16);
#pragma unroll
            for (int it = 0; it < 4; ++it) {
                const int pc = it * 32 + wc * 8 + (lane >> 3), kc = pc >> 2, kq = pc & 3;
                f32x2 a[4], q0[4];
#pragma unroll
                for (int i = 0; i < 4; ++i) { a[i] = (f32x2){lo_f(w[4 * it + i]), hi_f(w[4 * it + i])}; q0[i] = a[i] * rsd; }
#pragma unroll
                for (int i = 0; i < 4; ++i) a[i] = __builtin_elementwise_fma(-q0[i], (f32x2){sd, sd}, a[i]);
#pragma unroll
                for (int i = 0; i < 4; ++i) q0[i] = __builtin_elementwise_fma(a[i], (f32x2){rsd, rsd}, q0[i]);
                const u32x4 o = {pk_bf16(q0[0][0], q0[0][1]), pk_bf16(q0[1][0], q0[1][1]), pk_bf16(q0[2][0], q0[2][1]), pk_bf16(q0[3][0], q0[3][1])};
                ST_SC1(o, rs, xo + (unsigned)(kc * 16384 + kq * 256));
                SCHED();
            }
            WAIT_V(0);
        }
        LBAR();
        if (threadIdx.x == 256) flag_set(ws, FL_LN + panel * 32 + ks * 8 + c8, tag);
        if (wid == 0) flag_wait(ws, FL_LN + panel * 32, 1, 32, tag, lane);
        LBAR();

        cptr xn = uni((cptr)ws + WS_XN + (size_t)panel * 32 * 16384);
        if (grp == 1 && wc < 3) {
            const int p0 = wc == 0 ? 0 : 5 * wc + 1;
            const unsigned av0 = p0 * 1024 + lane * 16, av1 = av0 + 4096;
#pragma unroll
            for (int s3 = 0; s3 < 3; ++s3) {
                const unsigned la = ldsb + s3 * G1_STAGE + p0 * 1024;
                glds_s_sc1_first<0>(av0, xn + s3 * 16384, la); glds_s_sc1_next<1024>(av0, xn + s3 * 16384);
                glds_s_sc1_next<2048>(av0, xn + s3 * 16384);   glds_s_sc1_next<3072>(av0, xn + s3 * 16384);
                glds_s_sc1_first<0>(av1, xn + s3 * 16384, la + 4096);
                if (wc == 0) glds_s_sc1_next<1024>(av1, xn + s3 * 16384);
            }
            if (wc == 0) WAIT_V(12); else WAIT_V(10);
        }
        int ba[5];
        {
            int cgA, tlA, cgB, tlB;
            if (!kvt) { cgA = 1 + (wc >> 1); tlA = 2 * (wc & 1); cgB = 3 + (wc >> 1); tlB = tlA; }
            else if (wc < 2) { cgA = 1; tlA = 2 * wc; cgB = 3; tlB = 2 * wc; }
            else if (wc == 2) { cgA = 2; tlA = 0; cgB = 2; tlB = 2; }
            else { cgA = 4; tlA = 0; cgB = 4; tlB = 2; }
            const int q4 = (lane >> 2) & 3, p = lane & 3, krow = 8 * g + q4, sw = swzB(krow);
            auto adr = [&](int cg, int tl) { return G1_SA + cg * 4096 + krow * 128 + (((2 * tl + (p >> 1)) ^ sw) << 4) + (p & 1) * 8; };
            ba[0] = adr(cgA, tlA); ba[1] = adr(cgA, tlA + 1); ba[2] = adr(cgB, tlB); ba[3] = adr(cgB, tlB + 1); ba[4] = adr(0, wc);
        }
#pragma unroll
        for (int m = 0; m < 8; ++m)
#pragma unroll
            for (int n = 0; n < 5; ++n) acc[m][n] = (f32x4){0.f, 0.f, 0.f, 0.f};
        if (grp == 0) asm volatile("s_waitcnt vmcnt(0)" : "+v"(tlbt), "+v"(codet)::"memory");
        BAR();
        if (grp == 0) { g1_mainloop<0>(ldsb, lds, acc, bp, ld4, xn, wc, lane, ba, (cptr)wd, (cptr)wo); BAR(); }
        else if (wc < 3) { BAR(); g1_mainloop<1>(ldsb, lds, acc, bp, ld4, xn, wc, lane, ba, (cptr)wd, (cptr)wo); }
        else             { BAR(); g1_mainloop<2>(ldsb, lds, acc, bp, ld4, xn, wc, lane, ba, (cptr)wd, (cptr)wo); }
    }

    int tz = threadIdx.x; asm volatile("" : "+v"(tz));
    const int lane = tz & 63, g = lane >> 4, l15 = lane & 15;
    const unsigned bvoff = (unsigned)((8 * wc + (lane >> 3)) * 2048 + c8 * 256 + (((lane & 7) ^ swzB(8 * wc + (lane >> 3))) << 4));
    const unsigned bl = ldsb + G2_SB + grp * 24576 + wc * 1024;
    cptr sbp = uni((cptr)wd + ((size_t)(28 * ks + grp) << 16));
    cptr sa = uni((cptr)ws + WS_H + ((size_t)(panel * NKC2 + 28 * ks + grp) << 14));
    const unsigned al = ldsb + grp * 49152 + wc * 4096;
    auto startA = [&](int s2) {
        int tz4 = threadIdx.x; asm volatile("" : "+v"(tz4));
        const unsigned avoff = (unsigned)(wc * 4096 + (tz4 & 63) * 16);
        glds_s_sc1_first<0>(avoff, sa, al + s2 * 16384); glds_s_sc1_next<1024>(avoff, sa); glds_s_sc1_next<2048>(avoff, sa); glds_s_sc1_next<3072>(avoff, sa);
        sa += 32768;
    };
    auto prefetchB = [&]() { g2_issueB(bvoff, sbp, bl); g2_issueB(bvoff, sbp + 131072, bl + 8192); sbp += 262144; };
    {
        const int gq = (j & 1) ? 7 * j + 3 : 7 * j, gp = (j & 1) ? 7 * j + 1 : 7 * j + 4, gh = (j & 1) ? 7 * j : 7 * j + 6;
        {
            lds_t kp = lds + E_KP + (128 * grp + l15) * 256 + ((((wc >> 1) * 4 + (wc & 1) * 2 + (g >> 1)) ^ l15) << 4) + (g & 1) * 8;
#pragma unroll
            for (int m = 0; m < 8; ++m) *(LAS u32x2*)(kp + m * 4096) = (u32x2){pk_bf16(acc[m][4][0], acc[m][4][1]), pk_bf16(acc[m][4][2], acc[m][4][3])};
        }
        const bool has_h = !(kvt && wc == 3);
        const int hkc = !kvt ? gq + wc : (wc < 2 ? gp + wc : gh);
        const unsigned hoff = WS_H + (unsigned)(((panel * NKC2 + hkc) * 16 + 8 * grp) * 1024 + frag_off(lane));
        auto geglu_c = [&](int m) {
            const f32x2 a0 = gelu_mul2(acc[m][0].lo, acc[m][2].lo), a1 = gelu_mul2(acc[m][0].hi, acc[m][2].hi);
            const f32x2 b0 = gelu_mul2(acc[m][1].lo, acc[m][3].lo), b1 = gelu_mul2(acc[m][1].hi, acc[m][3].hi);
            return pack_frag(a0, a1, b0, b1);
        };
        auto geglu = [&](int m) { ST_SC1(geglu_c(m), rs, hoff + m * 1024); SCHED(); };
        const int q4 = (lane >> 2) & 3, p4 = lane & 3;
        auto logits = [&](u32x4 (&lg)[4]) {
            lds_t wp = lds + L_WPHI + (8 * g + q4) * 64 + p4 * 8;
            lds_t qp = lds + E_KP + (32 * wid + l15) * 256;
            bf16x8 wf[2][2], qf[4];
#pragma unroll
            for (int hh = 0; hh < 2; ++hh) {
                wf[hh][0] = lds_tr2(wp + hh * 2048, wp + hh * 2048 + 256); wf[hh][1] = lds_tr2(wp + hh * 2048 + 32, wp + hh * 2048 + 288);
                qf[hh * 2] = lds_b128(qp + (((hh * 4 + g) ^ l15) << 4)); qf[hh * 2 + 1] = lds_b128(qp + 4096 + (((hh * 4 + g) ^ l15) << 4));
            }
            SCHED();
            const f32x4 z4 = {0.f, 0.f, 0.f, 0.f};
            f32x4 p0[4], p1[4];
#pragma unroll
            for (int u = 0; u < 4; ++u) { p0[u] = MFMA(wf[u >> 1][0], qf[u], z4); p1[u] = MFMA(wf[u >> 1][1], qf[u], z4); }
            SCHED();
#pragma unroll
            for (int u = 0; u < 4; ++u) lg[u] = (u32x4){pk_bf16(p0[u][0], p0[u][1]), pk_bf16(p0[u][2], p0[u][3]), pk_bf16(p1[u][0], p1[u][1]), pk_bf16(p1[u][2], p1[u][3])};
            asm volatile("" : "+v"(lg[0]), "+v"(lg[1]), "+v"(lg[2]), "+v"(lg[3]));
        };
        if (kvt) {
            LBAR();
            if (wc == 3) {
                const int sw = swzP(l15);
                lds_t vp = lds + E_V + (128 * grp + l15) * 128 + (g & 1) * 8;
#pragma unroll
                for (int n = 0; n < 4; ++n) {
                    lds_t vn = vp + (((2 * n + (g >> 1)) ^ sw) << 4);
#pragma unroll
                    for (int m = 0; m < 8; ++m) *(LAS u32x2*)(vn + m * 2048) = (u32x2){pk_bf16(acc[m][n][0], acc[m][n][1]), pk_bf16(acc[m][n][2], acc[m][n][3])};
                }
            }
            u32x4 lg[4], P[4], M[4];
            logits(lg);
            WAIT_L0();
            soft_pm<4>(lg, P, M);
            {
                const int sw = swzF(l15);
                lds_t pb = lds + E_KP + (32 * wid + l15) * 256 + (g & 1) * 8;
#pragma unroll
                for (int u = 0; u < 4; ++u) {
                    const int c0 = (u >> 1) * 8 + (g >> 1), tb = u & 1;
                    *(LAS u32x2*)(pb + tb * 4096 + (((c0 + 0) ^ sw) << 4)) = (u32x2){P[u][0], P[u][1]};
                    *(LAS u32x2*)(pb + tb * 4096 + (((c0 + 2) ^ sw) << 4)) = (u32x2){P[u][2], P[u][3]};
                    *(LAS u32x2*)(pb + tb * 4096 + (((c0 + 4) ^ sw) << 4)) = (u32x2){M[u][0], M[u][1]};
                    *(LAS u32x2*)(pb + tb * 4096 + (((c0 + 6) ^ sw) << 4)) = (u32x2){M[u][2], M[u][3]};
                }
            }
            LBAR();
            {
                const int hh = wid >> 2, ft = wid & 3;
                f32x4 kv0 = {0.f, 0.f, 0.f, 0.f}, kv1 = kv0, zz = kv0;
                const bf16x8 ones = {0x3F80, 0x3F80, 0x3F80, 0x3F80, 0x3F80, 0x3F80, 0x3F80, 0x3F80};
                const int t0 = 8 * g + q4, t1 = t0 + 4, cp = hh * 8 + 2 * ft + (p4 >> 1), cv = hh * 4 + (p4 >> 1), hb8 = (p4 & 1) * 8;
                lds_t pf0 = lds + E_KP + t0 * 256 + ((cp ^ swzF(t0 & 15)) << 4) + hb8, pf1 = lds + E_KP + t1 * 256 + ((cp ^ swzF(t1 & 15)) << 4) + hb8;
                lds_t va0 = lds + E_V + t0 * 128 + ((cv ^ swzP(t0)) << 4) + hb8, va1 = lds + E_V + t1 * 128 + ((cv ^ swzP(t1)) << 4) + hb8;
                lds_t vb0 = lds + E_V + t0 * 128 + (((cv + 2) ^ swzP(t0)) << 4) + hb8, vb1 = lds + E_V + t1 * 128 + (((cv + 2) ^ swzP(t1)) << 4) + hb8;
#pragma unroll
                for (int k8 = 0; k8 < 8; ++k8) {
                    const bf16x8 pf = lds_tr2(pf0 + k8 * 8192, pf1 + k8 * 8192), v0 = lds_tr2(va0 + k8 * 4096, va1 + k8 * 4096), v1 = lds_tr2(vb0 + k8 * 4096, vb1 + k8 * 4096);
                    kv0 = MFMA(pf, v0, kv0); kv1 = MFMA(pf, v1, kv1); zz = MFMA(pf, ones, zz);
                }
                ST_SC1(((u32x4){pk_bf16(kv0[0], kv0[1]), pk_bf16(kv0[2], kv0[3]), pk_bf16(kv1[0], kv1[1]), pk_bf16(kv1[2], kv1[3])}), rs,
                       WS_PART + (unsigned)(((panel * 32 + 2 * j + hh) * 4 + ft) * 1024 + lane * 16));
                if (l15 == 0) ST_SC1(__builtin_bit_cast(u32x4, zz), rs, WS_PZ + (unsigned)(((panel * 32 + 2 * j + hh) * 64 + 16 * ft + 4 * g) * 4));
            }
            SCHED();
            u32x4 h0 = {0, 0, 0, 0};
            if (has_h) h0 = geglu_c(0);
            WAIT_V(0);
            LBAR();
            if (threadIdx.x == 0) flag_set(ws, FL_KV + j * 8 + panel, tag);
            prefetchB();
            if (has_h) {
                ST_SC1(h0, rs, hoff); SCHED();
                geglu(1); geglu(2); geglu(3); geglu(4); geglu(5); geglu(6); geglu(7);
            }
            WAIT_V(0);
            LBAR();
            if (threadIdx.x == 0) flag_set(ws, FL_H + (panel * 4 + ks) * 8 + c8, tag);
        } else {
            u32x4 lg[4], P[4], M[4];
            LBAR();
            logits(lg);
            soft_pm<4>(lg, P, M);
#pragma unroll
            for (int u = 0; u < 4; ++u) asm volatile("" : "+v"(P[u]), "+v"(M[u]));
            SCHED();
#pragma unroll
            for (int m = 0; m < 8; ++m) geglu(m);
            prefetchB();
            WAIT_V(4);
            LBAR();
            if (threadIdx.x == 0) flag_set(ws, FL_H + (panel * 4 + ks) * 8 + c8, tag);
            if (wid == 0) flag_wait(ws, FL_KV + j * 8, 1, 8, tag, lane);
            if (blockIdx.x == 0 && wid == 1) {
                GAS u64* p = flagp(ws, FL_LN + lane);
#pragma unroll 1
                for (unsigned it = 0; it < (1u << 21); ++it) {
                    const u64 v0 = __hip_atomic_load(p, __ATOMIC_RELAXED, __HIP_MEMORY_SCOPE_AGENT), v1 = __hip_atomic_load(p + 64 * 8, __ATOMIC_RELAXED, __HIP_MEMORY_SCOPE_AGENT);
                    const u64 v2 = __hip_atomic_load(p + 128 * 8, __ATOMIC_RELAXED, __HIP_MEMORY_SCOPE_AGENT), v3 = __hip_atomic_load(p + 192 * 8, __ATOMIC_RELAXED, __HIP_MEMORY_SCOPE_AGENT);
                    if (__all(v0 == tag && v1 == tag && v2 == tag && v3 == tag)) { if (lane == 0) flag_set(ws, FL_EPOCH, tag); break; }
                    __builtin_amdgcn_s_sleep(1);
                }
            }
            LBAR();
            u32x2 hfl = {0, 0};
            {
                const int hh = wid >> 2, ft = wid & 3;
                asm volatile("global_load_dwordx2 %0, %1, %2 sc1" : "+v"(hfl) : "v"((FL_H + (panel * 4 + ks) * 8 + (lane & 7)) * 64), "s"(ws + WS_FLG) : "memory");
                f32x4 a0, a1, az;
                u32x4 pw[8]; f32x4 pz[8];
#pragma unroll
                for (int p = 0; p < 8; ++p) {
                    pw[p] = LD_SC1(rs, WS_PART + (unsigned)(((p * 32 + 2 * j + hh) * 4 + ft) * 1024 + lane * 16));
                    pz[p] = __builtin_bit_cast(f32x4, LD_SC1(rs, WS_PZ + (unsigned)(((p * 32 + 2 * j + hh) * 64 + 16 * ft + 4 * g) * 4)));
                }
#pragma unroll
                for (int p = 0; p < 8; ++p) {
                    const f32x4 b0 = {lo_f(pw[p][0]), hi_f(pw[p][0]), lo_f(pw[p][1]), hi_f(pw[p][1])}, b1 = {lo_f(pw[p][2]), hi_f(pw[p][2]), lo_f(pw[p][3]), hi_f(pw[p][3])};
                    if (p == 0) { a0 = b0; a1 = b1; az = pz[0]; } else { a0 += b0; a1 += b1; az += pz[p]; }
                }
                lds_t kb = lds + L_KVL + ((hh * 2 + (ft >> 1)) * 3) * 1024 + lane * 16 + (ft & 1) * 8;
                *(LAS u32x2*)(kb) = (u32x2){pk_bf16(a0[0], a0[1]), pk_bf16(a0[2], a0[3])};
                *(LAS u32x2*)(kb + 1024) = (u32x2){pk_bf16(a1[0], a1[1]), pk_bf16(a1[2], a1[3])};
                *(LAS u32x2*)(kb + 2048) = (u32x2){pk_bf16(az[0], az[1]), pk_bf16(az[2], az[3])};
            }
            LBAR();
            {
                asm volatile("" : "+v"(hfl));
                const u64 v = ((u64)hfl[1] << 32) | hfl[0];
                if (!__all(v == tag)) flag_wait(ws, FL_H + (panel * 4 + ks) * 8, 1, 8, tag, lane);
                startA(0); startA(1);
            }
#pragma unroll
            for (int hh = 0; hh < 2; ++hh) {
                bf16x8 kf[2][3];
#pragma unroll
                for (int s = 0; s < 2; ++s)
#pragma unroll
                    for (int d = 0; d < 3; ++d) kf[s][d] = lds_b128(lds + L_KVL + ((hh * 2 + s) * 3 + d) * 1024 + lane * 16);
#pragma unroll
                for (int tb = 0; tb < 2; ++tb) {
                    const bf16x8 Pf = __builtin_bit_cast(bf16x8, P[hh * 2 + tb]), Mf = __builtin_bit_cast(bf16x8, M[hh * 2 + tb]);
                    const f32x4 z4 = {0.f, 0.f, 0.f, 0.f};
                    const f32x4 n0 = MFMA(kf[1][0], Mf, MFMA(kf[0][0], Pf, z4));
                    const f32x4 n1 = MFMA(kf[1][1], Mf, MFMA(kf[0][1], Pf, z4));
                    const f32x4 dn = MFMA(kf[1][2], Mf, MFMA(kf[0][2], Pf, z4));
                    const float rden = __builtin_amdgcn_rcpf(rbf(rbf(dn[0]) + 1e-6f));
                    ST_SC1(pack_frag(n0.lo * rden, n0.hi * rden, n1.lo * rden, n1.hi * rden), rs, WS_H + (unsigned)(((panel * NKC2 + 112 + 2 * j + hh) * 16 + 2 * wid + tb) * 1024 + frag_off(lane)));
                }
            }
        }
    }

    if (kvt) {
        if (wid == 0) flag_wait(ws, FL_H + (panel * 4 + ks) * 8, 1, 8, tag, lane);
        LBAR();
        startA(0); startA(1);
    }
    f32x4 c2[8][4];
    {
        int tz3 = threadIdx.x; asm volatile("" : "+v"(tz3));
        const int lane = tz3 & 63, g = lane >> 4;
        const unsigned avoff = (unsigned)(wc * 4096 + lane * 16);
        const unsigned bvoff = (unsigned)((8 * wc + (lane >> 3)) * 2048 + c8 * 256 + (((lane & 7) ^ swzB(8 * wc + (lane >> 3))) << 4));
        const int wm = wc >> 1, wn = wc & 1;
        unsigned bb[4];
        {
            const int q4 = (lane >> 2) & 3, p = lane & 3, kr = 8 * g + q4, sw = swzB(kr);
#pragma unroll
            for (int tl = 0; tl < 4; ++tl) bb[tl] = ldsb + G2_SB + grp * 24576 + wn * 4096 + kr * 128 + (((2 * tl + (p >> 1)) ^ sw) << 4) + (p & 1) * 8;
        }
        lds_t ap = lds + grp * 49152 + wm * 8192 + lane * 16;
        u32x2 ofl = {0, 0};
        const unsigned fo = (FL_O + (panel * 4 + ks) * 4 + (lane & 3)) * 64;
        if (kvt) WAIT_V(4); else WAIT_V(8);
        LBAR();
        if (grp == 1) BAR();
        auto step = [&](auto uc, auto fc, const int t) {
            constexpr int U = decltype(uc)::value, W = (U + 2) % 3;
            constexpr bool FIRST = decltype(fc)::value;
            bf16x8 A[8], B[4];
            if (U == 0) {
                if (t == 12) {
                    asm volatile("" : "+v"(ofl));
                    const u64 v = ((u64)ofl[1] << 32) | ofl[0];
                    if (!__all(v == tag)) flag_wait(ws, FL_O + (panel * 4 + ks) * 4, 1, 4, tag, lane);
                    sa = uni((cptr)ws + WS_H + ((size_t)(panel * NKC2 + 112 + 8 * ks + grp) << 14));
                    sbp = uni((cptr)wo + ((size_t)(8 * ks + grp) << 16));
                }
                asm volatile("s_cmp_lg_u32 %3, 9\n\ts_cbranch_scc1 .Lofl_%=\n\tglobal_load_dwordx2 %0, %1, %2 sc1\n.Lofl_%=:" : "+v"(ofl) : "v"(fo), "s"(ws + WS_FLG), "s"(t) : "scc", "memory");
            }
            if (U == 2 && t == 2 && !kvt && threadIdx.x == 0) flag_set(ws, FL_O + (panel * 4 + ks) * 4 + (c8 >> 1), tag);
            const bool more = U == 0 || t < 16;
            B[0] = lds_tr2_asm<U * 8192>(bb[0]); B[1] = lds_tr2_asm<U * 8192>(bb[1]);
            if (more) glds_s(bvoff, sbp, bl + W * 8192);
            B[2] = lds_tr2_asm<U * 8192>(bb[2]); B[3] = lds_tr2_asm<U * 8192>(bb[3]);
            if (more) { glds_s_o128(bvoff, sbp, bl + W * 8192 + 4096); sbp += 131072; }
            A[0] = lds_b128(ap + U * 16384); A[1] = lds_b128(ap + U * 16384 + 1024);
            if (more) { glds_s_sc1_first<0>(avoff, sa, al + W * 16384); glds_s_sc1_next<1024>(avoff, sa); }
            A[2] = lds_b128(ap + U * 16384 + 2048); A[3] = lds_b128(ap + U * 16384 + 3072); A[4] = lds_b128(ap + U * 16384 + 4096);
            if (more) { glds_s_sc1_next<2048>(avoff, sa); glds_s_sc1_next<3072>(avoff, sa); sa += 32768; }
            A[5] = lds_b128(ap + U * 16384 + 5120); A[6] = lds_b128(ap + U * 16384 + 6144); A[7] = lds_b128(ap + U * 16384 + 7168);
            WAIT_L0();
            if (!more) WAIT_V(0);
            SCHED(); BAR(); SCHED();
#pragma unroll
            for (int m = 0; m < 8; ++m)
#pragma unroll
                for (int n = 0; n < 4; ++n) { const int nn = (m & 1) ? 3 - n : n; if (FIRST) MFMA_SET(c2[m][nn], B[nn], A[m]); else MFMA_ACC(c2[m][nn], B[nn], A[m]); }
            WAIT_V(6);
            SCHED(); BAR(); SCHED();
        };
        step(std::integral_constant<int, 0>{}, std::true_type{}, 0);
#pragma unroll 1
        for (int t = 1; t < 18; t += 3) {
            step(std::integral_constant<int, 1>{}, std::false_type{}, t); step(std::integral_constant<int, 2>{}, std::false_type{}, t + 1);
            if (t < 16) step(std::integral_constant<int, 0>{}, std::false_type{}, t + 2);
        }
    }
    {
        int tz2 = threadIdx.x; asm volatile("" : "+v"(tz2));
        const int ln = tz2 & 63, tile = panel * 8 + c8, wm = wc >> 1, wn = wc & 1;
        const int qk = 2 * wm + grp, qs = qk ^ 1;
        const bool publisher = qk != ks;
        const int mh = wid >> 2, nq = wid & 3;
        const int row0 = panel * 256 + ks * 64 + mh * 32 + (ln & 15), col0 = c8 * 128 + nq * 32 + 8 * frag_kq(ln);
        constexpr int L_OWN = 0, L_SEND = 65536;
        u32x4 xr0 = {0, 0, 0, 0}, xr1 = xr0;
        {
            const unsigned xo = (unsigned)(row0 * D_ + col0) * 2u;
            asm volatile("global_load_dwordx4 %0, %1, %2" : "+v"(xr0) : "v"(xo), "s"(x) : "memory");
            asm volatile("global_load_dwordx4 %0, %1, %2" : "+v"(xr1) : "v"(xo + 16 * D_ * 2), "s"(x) : "memory");
        }
        f32x4 hk[4][4], hs[4][4];
#pragma unroll
        for (int m = 0; m < 4; ++m)
#pragma unroll
            for (int n = 0; n < 4; ++n) { hk[m][n] = grp ? c2[4 + m][n] : c2[m][n]; hs[m][n] = grp ? c2[m][n] : c2[4 + m][n]; }
        auto dump_own = [&](const f32x4 (&v)[4][4]) {
#pragma unroll
            for (int m = 0; m < 4; ++m)
#pragma unroll
                for (int n = 0; n < 4; ++n) *(LAS f32x4*)(lds + L_OWN + ((grp * 4 + m) * 8 + 4 * wn + n) * 1024 + ln * 16) = v[m][n];
        };
        if (qs == ks) dump_own(hs);
        else {
#pragma unroll
            for (int m = 0; m < 4; ++m)
#pragma unroll
                for (int n2 = 0; n2 < 2; ++n2) {
                    const f32x4 a = hs[m][2 * n2], b = hs[m][2 * n2 + 1];
                    *(LAS u32x4*)(lds + L_SEND + (wid ^ 4) * 8192 + (m * 2 + n2) * 1024 + ln * 16) = (u32x4){pk_bf16(a[0], a[1]), pk_bf16(a[2], a[3]), pk_bf16(b[0], b[1]), pk_bf16(b[2], b[3])};
                }
        }
        if (qk == ks) dump_own(hk);
        if (grp == 0) BAR();
        LBAR();
        if (publisher) {
            const unsigned xbase = WS_XCH + (unsigned)((((tile * 4 + ks) * 8 + wid) * 8) * 1024 + ln * 16);
#pragma unroll
            for (int m = 0; m < 4; ++m)
#pragma unroll
                for (int n2 = 0; n2 < 2; ++n2) {
                    const u32x4 w = *(const LAS u32x4*)(lds + L_SEND + wid * 8192 + (m * 2 + n2) * 1024 + ln * 16);
                    const f32x4 a = hk[m][2 * n2] + (f32x4){lo_f(w[0]), hi_f(w[0]), lo_f(w[1]), hi_f(w[1])}, b = hk[m][2 * n2 + 1] + (f32x4){lo_f(w[2]), hi_f(w[2]), lo_f(w[3]), hi_f(w[3])};
                    ST_SC1(((u32x4){pk_bf16(a[0], a[1]), pk_bf16(a[2], a[3]), pk_bf16(b[0], b[1]), pk_bf16(b[2], b[3])}), rs, xbase + (m * 2 + n2) * 1024);
                }
        }
        f32x4 e[2][2];
#pragma unroll
        for (int a = 0; a < 2; ++a)
#pragma unroll
            for (int b = 0; b < 2; ++b)
                e[a][b] = *(const LAS f32x4*)(lds + L_OWN + ((2 * mh + a) * 8 + 2 * nq + b) * 1024 + ln * 16) + *(const LAS f32x4*)(lds + L_OWN + ((4 + 2 * mh + a) * 8 + 2 * nq + b) * 1024 + ln * 16);
        asm volatile("s_waitcnt vmcnt(0)" : "+v"(xr0), "+v"(xr1)::"memory");
        if (publisher && ln == 0) flag_set(ws, FL_X + (tile * 4 + ks) * 8 + wid, tag);
        const int wsrc = (ks & 1) * 4 + (ks >> 1) * 2 + (nq >> 1);
        {
            GAS u64* p = flagp(ws, FL_X + (tile * 4 + ((ks + 1 + (ln < 3 ? ln : 0)) & 3)) * 8 + wsrc);
#pragma unroll 1
            for (unsigned it = 0; it < (1u << 21); ++it) {
                const u64 v = __hip_atomic_load(p, __ATOMIC_RELAXED, __HIP_MEMORY_SCOPE_AGENT);
                if (__all(v == tag)) break;
                __builtin_amdgcn_s_sleep(1);
            }
            asm volatile("" ::: "memory");
        }
        u32x4 pw[3][2];
#pragma unroll
        for (int i = 0; i < 3; ++i) {
            const unsigned base = WS_XCH + (unsigned)((((tile * 4 + i + (i >= ks ? 1 : 0)) * 8 + wsrc) * 8 + (nq & 1) + 4 * mh) * 1024 + ln * 16);
            pw[i][0] = LD_SC1(rs, base); pw[i][1] = LD_SC1(rs, base + 2048);
        }
#pragma unroll
        for (int i = 0; i < 3; ++i)
#pragma unroll
            for (int a = 0; a < 2; ++a) {
                const u32x4 w = pw[i][a];
                e[a][0] += (f32x4){lo_f(w[0]), hi_f(w[0]), lo_f(w[1]), hi_f(w[1])};
                e[a][1] += (f32x4){lo_f(w[2]), hi_f(w[2]), lo_f(w[3]), hi_f(w[3])};
            }
        const __amdgpu_buffer_rsrc_t ors = __builtin_amdgcn_make_buffer_rsrc((void*)out, 0, S_ * D_ * 2, 0x00020000);
#pragma unroll
        for (int a = 0; a < 2; ++a) {
            float u[4], v[4];
#pragma unroll
            for (int q = 0; q < 4; ++q) {
                auto rr = __builtin_amdgcn_permlane16_swap(__float_as_uint(e[a][0][q]), __float_as_uint(e[a][1][q]), false, false);
                u[q] = __uint_as_float(rr[0]); v[q] = __uint_as_float(rr[1]);
            }
            const u32x4 xv = a ? xr1 : xr0;
            u32x4 o;
            o[0] = pk_bf16(u[0] + lo_f(xv[0]), u[1] + hi_f(xv[0])); o[1] = pk_bf16(u[2] + lo_f(xv[1]), u[3] + hi_f(xv[1]));
            o[2] = pk_bf16(v[0] + lo_f(xv[2]), v[1] + hi_f(xv[2])); o[3] = pk_bf16(v[2] + lo_f(xv[3]), v[3] + hi_f(xv[3]));
            __builtin_amdgcn_raw_buffer_store_b128(o, ors, (unsigned)(((row0 + 16 * a) * D_ + col0) * 2), 0, 16);
        }
    }
}

extern "C" void kernel_launch(void* const* d_in, const int* in_sizes, int n_in, void* d_out, int out_size, void* d_ws, size_t ws_size, hipStream_t stream) {
    const u16* x = (const u16*)d_in[0]; const u16* wqkv = (const u16*)d_in[1]; const u16* wphi = (const u16*)d_in[2]; const u16* wo = (const u16*)d_in[3];
    const u16* wg = (const u16*)d_in[5]; const u16* wu = (const u16*)d_in[6]; const u16* wd = (const u16*)d_in[7];
    (void)hipFuncSetAttribute((const void*)k_fused, hipFuncAttributeMaxDynamicSharedMemorySize, L_TOTAL);
    k_fused<<<256, 512, L_TOTAL, stream>>>(x, wqkv, wphi, wo, wg, wu, wd, (unsigned char*)d_ws, (u16*)d_out);
}
```
